```python
import torch

def _softmax(x, axis=-1):
    return torch.softmax(x, dim=axis)

def _gelu(x):
    return torch.nn.functional.gelu(x, approximate='tanh')

def _silu(x):
    return torch.nn.functional.silu(x)

def _sigmoid(x):
    return torch.sigmoid(x)

def _layernorm(x, axis=-1, eps=1e-05):
    m = x.mean(dim=axis, keepdim=True)
    v = x.var(dim=axis, keepdim=True, unbiased=False)
    return (x - m) / torch.sqrt(v + eps)

def _scatter_add(tgt, idx, src, axis=0):
    return tgt.clone().index_add_(axis, idx, src)

def _segment_sum(src, seg, n):
    out = torch.zeros((n,) + src.shape[1:], dtype=src.dtype)
    return out.index_add_(0, seg, src)

def _pad(x, pw):
    flat = tuple((v for pair in reversed(pw) for v in pair))
    return torch.nn.functional.pad(x, flat)
kernel_name = 'concat_heads__linear_attn_block__memory_aug_recurrent_mem_tr__prompt'

def setup_inputs():

    _w = lambda a: (lambda t: t.to(torch.bfloat16) if t.is_floating_point() else t)(torch.as_tensor(a))

    def _A_setup():
        B, S, D = (1, 2048, 448)
        return {'x': _w((lambda *s: torch.randn(*s, dtype=torch.bfloat16) * 0.05)(B, S, D)), 'wqkv': _w((lambda *s: torch.randn(*s, dtype=torch.bfloat16) * 1.6)(D, 3 * D)), 'wo': _w((lambda *s: torch.randn(*s, dtype=torch.bfloat16) * 0.1)(D, D))}
    _A_in = _A_setup()

    def _B_setup():
        B, S, Dm = (1, 2048, 448)
        H, Dh = (16, 256)
        M, seg = (8, 32)
        seg = min(seg, S)
        S = S // seg * seg
        return {'x': _w((lambda *s: torch.randn(*s, dtype=torch.bfloat16) * 0.05)(B, S, Dm)), 'mem0': _w((lambda *s: torch.randn(*s, dtype=torch.bfloat16) * 0.8)(B, M, Dm)), 'Wq': _w((lambda *s: torch.randn(*s, dtype=torch.bfloat16) * 1.6)(Dm, H, Dh) * Dm ** (-0.5)), 'Wk': _w((lambda *s: torch.randn(*s, dtype=torch.bfloat16) * 1.6)(Dm, H, Dh) * Dm ** (-0.5)), 'Wv': _w((lambda *s: torch.randn(*s, dtype=torch.bfloat16) * 0.8)(Dm, H, Dh) * Dm ** (-0.5)), 'Wo': _w((lambda *s: torch.randn(*s, dtype=torch.bfloat16) * 0.8)(H, Dh, Dm) * (H * Dh) ** (-0.5)), 'seg_len': seg}
    _B_in = _B_setup()
    return {'A_x': _A_in['x'], 'A_wqkv': _A_in['wqkv'], 'A_wo': _A_in['wo'], 'B_x': _B_in['x'], 'B_mem0': _B_in['mem0'], 'B_Wq': _B_in['Wq'], 'B_Wk': _B_in['Wk'], 'B_Wv': _B_in['Wv'], 'B_Wo': _B_in['Wo'], 'B_seg_len': _B_in['seg_len']}

def reference(A_x, A_wqkv, A_wo, B_x, B_mem0, B_Wq, B_Wk, B_Wv, B_Wo, B_seg_len):

    def _A_ref(x, wqkv, wo):
        B, S, D = x.shape
        H = 16
        Dh = D // H
        qkv = (lambda x, *d: x.permute(*d))((x @ wqkv).reshape(B, S, 3, H, Dh), 2, 0, 3, 1, 4)
        q, k, v = (qkv[0], qkv[1], qkv[2])
        phi = lambda t: torch.where(t > 0, t + 1, torch.exp(t)) + 1e-06
        pq, pk = (phi(q), phi(k))
        kv = torch.einsum('bhsd,bhse->bhde', pk, v)
        z = torch.einsum('bhsd->bhd', pk)
        num = torch.einsum('bhqd,bhde->bhqe', pq, kv)
        den = torch.einsum('bhqd,bhd->bhq', pq, z)[..., None] + 1e-06
        o = num / den
        return (lambda x, *d: x.permute(*d))(o, 0, 2, 1, 3).reshape(B, S, D) @ wo

    def _B_ref(x, mem0, Wq, Wk, Wv, Wo, seg_len):
        B, S, Dm = x.shape
        H, Dh = (Wq.shape[1], Wq.shape[2])
        M = mem0.shape[1]
        scale = Dh ** (-0.5)
        n_seg = S // seg_len
        L = M + seg_len
        idx = torch.arange(L)
        mask = (idx[:, None] < M) | (idx[:, None] >= idx[None, :])
        mem = mem0
        outs = []
        for s in range(n_seg):
            xs = x[:, s * seg_len:(s + 1) * seg_len, :].clone()
            h = torch.concatenate([mem, xs], axis=1)
            q = torch.einsum('btd,dhe->bhte', h, Wq)
            k = torch.einsum('btd,dhe->bhte', h, Wk)
            v = torch.einsum('btd,dhe->bhte', h, Wv)
            scores = torch.einsum('bhqe,bhke->bhqk', q, k) * scale
            scores = torch.where(mask[None, None], scores, -1000000000.0)
            p = _softmax(scores, axis=-1)
            o = torch.einsum('bhqk,bhke->bhqe', p, v)
            o = torch.einsum('bhte,hed->btd', o, Wo)
            mem = o[:, :M, :].clone()
            outs.append(o[:, M:, :])
        return torch.concatenate(outs, axis=1)
    a = _A_ref(A_x, A_wqkv, A_wo)
    b = _B_ref(B_x, B_mem0, B_Wq, B_Wk, B_Wv, B_Wo, B_seg_len)
    return torch.concatenate([a, b], axis=-1)
```

```cpp
#include <hip/hip_runtime.h>
#include <hip/hip_bf16.h>
#include <stdint.h>

typedef unsigned short u16;
__device__ __forceinline__ float b2f(u16 v) { return __uint_as_float((unsigned)v << 16); }
__device__ __forceinline__ u16 f2b(float f) { __hip_bfloat16 h = __float2bfloat16(f); return *(u16*)&h; }
__device__ __forceinline__ float rb(float f) { return b2f(f2b(f)); }

constexpr int S = 2048, D = 448, HA = 16, DA = 28, H = 16, DH = 256, M = 8, SEG = 32, NSEG = 64, L = 40;

__global__ void gemm_nn(const u16* A, int lda, const u16* B, int ldb, int bt, u16* C, int ldc, int Mm, int N, int K, float scale,
                        long sA, long sB, long sC) {
    int n = blockIdx.x * 64 + (threadIdx.x & 63);
    int m = blockIdx.y * 4 + (threadIdx.x >> 6);
    A += sA * blockIdx.z; B += sB * blockIdx.z; C += sC * blockIdx.z;
    if (n >= N || m >= Mm) return;
    float acc = 0.f;
    for (int k = 0; k < K; ++k) acc += b2f(A[(long)m * lda + k]) * b2f(bt ? B[(long)n * ldb + k] : B[(long)k * ldb + n]);
    C[(long)m * ldc + n] = f2b(acc * scale);
}

__device__ __forceinline__ float phi(float t) { return rb(rb(t > 0 ? t + 1.f : __expf(t)) + 1e-6f); }

__global__ void kv_kernel(const u16* qkv, float* kv, float* z) {
    int h = blockIdx.x, t = threadIdx.x;
    if (t >= DA * (DA + 1)) return;
    int d = t / (DA + 1), e = t % (DA + 1);
    float acc = 0.f;
    for (int s = 0; s < S; ++s) {
        float pk = phi(b2f(qkv[(long)s * 1344 + 448 + h * DA + d]));
        float v = e < DA ? b2f(qkv[(long)s * 1344 + 896 + h * DA + e]) : 1.f;
        acc += pk * v;
    }
    if (e < DA) kv[(h * DA + d) * DA + e] = rb(acc); else z[h * DA + d] = rb(acc);
}
__global__ void oa_kernel(const u16* qkv, const float* kv, const float* z, u16* o) {
    int s = blockIdx.x, t = threadIdx.x;
    int h = t / DA, e = t % DA;
    float num = 0.f, den = 0.f;
    for (int d = 0; d < DA; ++d) {
        float pq = phi(b2f(qkv[(long)s * 1344 + h * DA + d]));
        num += pq * kv[(h * DA + d) * DA + e];
        den += pq * z[h * DA + d];
    }
    o[(long)s * D + t] = f2b(num / (den + 1e-6f));
}

__global__ void attn_kernel(const u16* mem, const u16* x, const u16* U, const u16* V, const u16* UM, const u16* MB, u16* memn, u16* out, int seg) {
    __shared__ float p[H][L];
    __shared__ float q[D];
    int r = blockIdx.x, t = threadIdx.x;
    q[t] = b2f(r < M ? mem[r * D + t] : x[(long)(seg * SEG + r - M) * D + t]);
    __syncthreads();
    for (int idx = t; idx < H * L; idx += D) {
        int h = idx / L, j = idx % L;
        const u16* u = j < M ? UM + ((long)j * H + h) * D : U + ((long)(seg * SEG + j - M) * H + h) * D;
        float acc = 0.f;
        for (int k = 0; k < D; ++k) acc += q[k] * b2f(u[k]);
        bool ok = r < M || r >= j;
        p[h][j] = ok ? acc : -1e9f;
    }
    __syncthreads();
    if (t < H) {
        float mx = -3e38f; for (int j = 0; j < L; ++j) mx = fmaxf(mx, p[t][j]);
        float sum = 0.f; for (int j = 0; j < L; ++j) { float e = __expf(p[t][j] - mx); p[t][j] = e; sum += e; }
        for (int j = 0; j < L; ++j) p[t][j] = rb(p[t][j] / sum);
    }
    __syncthreads();
    float acc = 0.f;
    for (int h = 0; h < H; ++h)
        for (int j = 0; j < L; ++j) {
            const u16* v = j < M ? MB + ((long)j * H + h) * D : V + ((long)(seg * SEG + j - M) * H + h) * D;
            acc += p[h][j] * b2f(v[t]);
        }
    if (r < M) memn[r * D + t] = f2b(acc); else out[(long)(seg * SEG + r - M) * 896 + 448 + t] = f2b(acc);
}

extern "C" void kernel_launch(void* const* d_in, const int* in_sizes, int n_in, void* d_out, int out_size, void* d_ws, size_t ws_size, hipStream_t stream) {
    const u16* Ax = (const u16*)d_in[0]; const u16* Awqkv = (const u16*)d_in[1]; const u16* Awo = (const u16*)d_in[2];
    const u16* Bx = (const u16*)d_in[3]; const u16* Bm0 = (const u16*)d_in[4];
    const u16* Wq = (const u16*)d_in[5]; const u16* Wk = (const u16*)d_in[6]; const u16* Wv = (const u16*)d_in[7]; const u16* Wo = (const u16*)d_in[8];
    u16* out = (u16*)d_out;
    char* ws = (char*)d_ws;
    u16* qkv = (u16*)ws; ws += (size_t)S * 1344 * 2;
    float* kv = (float*)ws; ws += 16 * 28 * 28 * 4;
    float* z = (float*)ws; ws += 16 * 28 * 4 + 256;
    u16* oa = (u16*)ws; ws += (size_t)S * D * 2;
    u16* At = (u16*)ws; ws += (size_t)D * H * D * 2;
    u16* Bm = (u16*)ws; ws += (size_t)D * H * D * 2;
    u16* U = (u16*)ws; ws += (size_t)S * H * D * 2;
    u16* V = (u16*)ws; ws += (size_t)S * H * D * 2;
    u16* UM = (u16*)ws; ws += (size_t)M * H * D * 2;
    u16* MB = (u16*)ws; ws += (size_t)M * H * D * 2;
    u16* mem = (u16*)ws; ws += (size_t)(NSEG + 1) * M * D * 2;
    gemm_nn<<<dim3(21, S / 4, 1), 256, 0, stream>>>(Ax, D, Awqkv, 1344, 0, qkv, 1344, S, 1344, D, 1.f, 0, 0, 0);
    kv_kernel<<<16, 832, 0, stream>>>(qkv, kv, z);
    oa_kernel<<<S, 448, 0, stream>>>(qkv, kv, z, oa);
    gemm_nn<<<dim3(7, S / 4, 1), 256, 0, stream>>>(oa, D, Awo, D, 0, out, 896, S, D, D, 1.f, 0, 0, 0);
    gemm_nn<<<dim3(7, D / 4, H), 256, 0, stream>>>(Wk, H * DH, Wq, H * DH, 1, At, H * D, D, D, DH, 0.0625f, DH, DH, D);
    gemm_nn<<<dim3(7, D / 4, H), 256, 0, stream>>>(Wv, H * DH, Wo, D, 0, Bm, H * D, D, D, DH, 1.f, DH, (long)DH * D, D);
    gemm_nn<<<dim3(112, S / 4, 1), 256, 0, stream>>>(Bx, D, At, H * D, 0, U, H * D, S, H * D, D, 1.f, 0, 0, 0);
    gemm_nn<<<dim3(112, S / 4, 1), 256, 0, stream>>>(Bx, D, Bm, H * D, 0, V, H * D, S, H * D, D, 1.f, 0, 0, 0);
    for (int s = 0; s < NSEG; ++s) {
        const u16* m = s == 0 ? Bm0 : mem + (size_t)s * M * D;
        gemm_nn<<<dim3(112, 2, 1), 256, 0, stream>>>(m, D, At, H * D, 0, UM, H * D, M, H * D, D, 1.f, 0, 0, 0);
        gemm_nn<<<dim3(112, 2, 1), 256, 0, stream>>>(m, D, Bm, H * D, 0, MB, H * D, M, H * D, D, 1.f, 0, 0, 0);
        attn_kernel<<<40, 448, 0, stream>>>(m, Bx, U, V, UM, MB, mem + (size_t)(s + 1) * M * D, out, s);
    }
}
```

```cpp
#include <hip/hip_runtime.h>
#include <hip/hip_bf16.h>
#include <stdint.h>

typedef unsigned u32;
typedef unsigned long long u64;
typedef unsigned short u16;
typedef u32 u2 __attribute__((ext_vector_type(2)));
typedef u32 u4 __attribute__((ext_vector_type(4)));
typedef u32 u8v __attribute__((ext_vector_type(8)));
typedef float f2 __attribute__((ext_vector_type(2)));
typedef float f4 __attribute__((ext_vector_type(4)));
typedef __bf16 bf2 __attribute__((ext_vector_type(2)));
typedef __bf16 bf8 __attribute__((ext_vector_type(8)));
typedef __bf16 bf16v __attribute__((ext_vector_type(16)));
typedef short s4 __attribute__((ext_vector_type(4)));
typedef short s8 __attribute__((ext_vector_type(8)));
#define LDS __attribute__((address_space(3)))
#define DEV __device__ __forceinline__
typedef __amdgpu_buffer_rsrc_t rsrc_t;
#define BC(T, x) __builtin_bit_cast(T, x)

constexpr int NSEG = 64;
constexpr int SC1 = 16;
constexpr u32 RING_SLOT = 18u * 16384u;
constexpr u32 RING_BYTES = 4u * RING_SLOT;
constexpr u32 FLAGS = RING_BYTES;
constexpr u32 F_W = FLAGS;
constexpr u32 F_B = FLAGS + 2048;
constexpr u32 F_NB = FLAGS + 2560;
constexpr u32 F_U = FLAGS + 4096;
constexpr u32 F_M = FLAGS + 6144;
constexpr u32 F_P = FLAGS + 7168;
constexpr u32 F_A1 = FLAGS + 8192;
constexpr u32 F_G = FLAGS + 9216;
constexpr u32 INIT_BYTES = FLAGS + 16384;
constexpr u32 O_U = 2u << 20;
constexpr u32 O_VO = O_U + 64u * 16u * 28672u;
constexpr u32 O_BM = O_VO + 64u * 16u * 28672u;
constexpr u32 O_UM = O_BM + 28u * 16u * 14336u;
constexpr u32 O_MB = O_UM + 64u * 16u * 7168u;
constexpr u32 O_PX = O_MB + 64u * 28u * 4096u;
constexpr u32 O_PM = O_PX + 64u * 16u * 2048u;
constexpr u32 O_PQ = O_PM + 64u * 8192u;
constexpr u32 O_KV = O_PQ + 128u * 16u * 1024u;
constexpr u32 O_G = O_KV + 128u * 4096u;
constexpr u32 O_Z = O_G + 28u * 16u * 1024u;
constexpr u32 O_DBG = O_Z + 16384u;
constexpr u32 WS_END = O_DBG + (1u << 20);
constexpr u32 R_MEM = 0, R_S = 8192, R_ST = 24576, R_Y = 32768;
DEV u32 rphys(u32 l) { return ((l >> 13) << 14) | (l & 8191u); }

constexpr int LDS_BYTES = 160 * 1024;
constexpr int L_BST = 0, L_WIMG = 57344;

struct Params {
    const u16* Ax; const u16* Awqkv; const u16* Awo; const u16* Bx; const u16* Bm0;
    const u16* Wq; const u16* Wk; const u16* Wv; const u16* Wo;
    u16* out; char* ws;
};

DEV float asf(u32 x) { return __builtin_bit_cast(float, x); }
DEV u32 asu(float x) { return __builtin_bit_cast(u32, x); }
DEV f4 mfma(u4 a, u4 b, f4 c) { return __builtin_amdgcn_mfma_f32_16x16x32_bf16(BC(bf8, a), BC(bf8, b), c, 0, 0, 0); }
DEV f4 smf64(u4 a, u8v b, f4 c, int idx) { return __builtin_amdgcn_smfmac_f32_16x16x64_bf16(BC(bf8, a), BC(bf16v, b), c, idx, 0, 0); }
DEV f4 smf32(u2 a, u4 b, f4 c, int idx) { return __builtin_amdgcn_smfmac_f32_16x16x32_bf16(BC(s4, a), BC(s8, b), c, idx, 0, 0); }
DEV u32 pk(float lo, float hi) { f2 v = {lo, hi}; return BC(u32, __builtin_convertvector(v, bf2)); }
DEV u8v cat(u4 a, u4 b) { return __builtin_shufflevector(a, b, 0, 1, 2, 3, 4, 5, 6, 7); }
DEV rsrc_t mkrs(const void* p, u32 bytes) { return __builtin_amdgcn_make_buffer_rsrc((void*)p, (short)0, (int)bytes, 0x00020000); }
DEV u4 ld4(rsrc_t r, u32 v, u32 s) { return __builtin_amdgcn_raw_buffer_load_b128(r, v, s, 0); }
DEV u4 ld4c(rsrc_t r, u32 v, u32 s) { return __builtin_amdgcn_raw_buffer_load_b128(r, v, s, SC1); }
DEV u2 ld2c(rsrc_t r, u32 v, u32 s) { return __builtin_amdgcn_raw_buffer_load_b64(r, v, s, SC1); }
DEV u32 ld1c(rsrc_t r, u32 v, u32 s) { return __builtin_amdgcn_raw_buffer_load_b32(r, v, s, SC1); }
DEV void st4c(u4 x, rsrc_t r, u32 v, u32 s) { __builtin_amdgcn_raw_buffer_store_b128(x, r, v, s, SC1); }
DEV void st2c(u2 x, rsrc_t r, u32 v, u32 s) { __builtin_amdgcn_raw_buffer_store_b64(x, r, v, s, SC1); }
DEV void st1c(u32 x, rsrc_t r, u32 v, u32 s) { __builtin_amdgcn_raw_buffer_store_b32(x, r, v, s, SC1); }
DEV u4 lds4(LDS char* l, u32 o) { return *(LDS u4*)(l + o); }
DEV u2 ldtr(LDS char* l, u32 o) { return BC(u2, __builtin_amdgcn_ds_read_tr16_b64_v4i16((LDS s4*)(l + o))); }
DEV u4 ldtr2(LDS char* l, u32 o0, u32 o1) { u2 a = ldtr(l, o0), b = ldtr(l, o1); return (u4){a.x, a.y, b.x, b.y}; }
#define PINA(x) asm("" : "+a"(x))
#define PINV(x) asm("" : "+v"(x))
DEV int fresh_lane() { int l; asm volatile("v_mbcnt_lo_u32_b32 %0, -1, 0\n\tv_mbcnt_hi_u32_b32 %0, -1, %0" : "=v"(l)); return l; }
#define MEMBAR() asm volatile("" ::: "memory")
#define VMCNT(n) asm volatile("s_waitcnt vmcnt(" #n ")" ::: "memory")
DEV void bar() { asm volatile("s_waitcnt lgkmcnt(0)\n\ts_barrier" ::: "memory"); }
DEV float ror8(float x) { return asf((u32)__builtin_amdgcn_mov_dpp((int)asu(x), 0x128, 0xf, 0xf, true)); }
DEV void sw32(float& a, float& b) { auto r = __builtin_amdgcn_permlane32_swap(asu(a), asu(b), false, false); a = asf(r[0]); b = asf(r[1]); }
DEV void sw16u(u32& a, u32& b) { auto r = __builtin_amdgcn_permlane16_swap(a, b, false, false); a = r[0]; b = r[1]; }
DEV float fold32(float a, float b) { sw32(a, b); return a + b; }
DEV float xmax16(float x) { auto r = __builtin_amdgcn_permlane16_swap(asu(x), asu(x), false, false); return fmaxf(asf(r[0]), asf(r[1])); }
DEV float xmax32(float x) { auto r = __builtin_amdgcn_permlane32_swap(asu(x), asu(x), false, false); return fmaxf(asf(r[0]), asf(r[1])); }
DEV float xadd16(float x) { auto r = __builtin_amdgcn_permlane16_swap(asu(x), asu(x), false, false); return asf(r[0]) + asf(r[1]); }
DEV float xadd32(float x) { auto r = __builtin_amdgcn_permlane32_swap(asu(x), asu(x), false, false); return asf(r[0]) + asf(r[1]); }
DEV float ex2(float x) { return __builtin_amdgcn_exp2f(x); }
DEV u32 nos(u32 x) { return x < 0xFFFFFFFEu ? x : 0xFFFFFFFEu; }
DEV u32 umax4(u4 v) { u32 a = v.x > v.y ? v.x : v.y, b = v.z > v.w ? v.z : v.w; return a > b ? a : b; }
DEV u32 umax(u32 a, u32 b) { return a > b ? a : b; }
DEV bool anysent(u32 m) { return __builtin_amdgcn_ballot_w64(m == 0xFFFFFFFFu) != 0ull; }
DEV u4 fold4(f4 d0, f4 d1, f4 d2, f4 d3) {
    float a0 = fold32(d0[0], d2[0]), a1 = fold32(d0[1], d2[1]), a2 = fold32(d0[2], d2[2]), a3 = fold32(d0[3], d2[3]);
    float b0 = fold32(d1[0], d3[0]), b1 = fold32(d1[1], d3[1]), b2 = fold32(d1[2], d3[2]), b3 = fold32(d1[3], d3[3]);
    u32 x0 = pk(a0, a1), x1 = pk(b0, b1), x2 = pk(a2, a3), x3 = pk(b2, b3);
    sw16u(x0, x1); sw16u(x2, x3);
    return (u4){x0, x1, x2, x3};
}
DEV void wait_flags(rsrc_t ws, u32 voff, bool use, int target, int& budget, int slp) {
    while (true) {
        MEMBAR();
        int v = (int)ld1c(ws, voff, 0);
        bool bad = use && v < target;
        if (__builtin_amdgcn_ballot_w64(bad) == 0ull) break;
        if (--budget < 0) break;
        if (slp == 1) __builtin_amdgcn_s_sleep(2); else __builtin_amdgcn_s_sleep(8);
    }
    MEMBAR();
}
#define STAMP(role, s, k)

template <int TYPE>
DEV void w0(const Params& p, LDS char* lds, int h, int q, int rot, int wave) {
    const int lane = fresh_lane();
    const int x = lane & 15, g = lane >> 4;
    rsrc_t ra = mkrs(TYPE == 0 ? p.Wk : p.Wv, 448u * 4096u * 2u);
    rsrc_t rb = mkrs(TYPE == 0 ? p.Wq : p.Wo, 448u * 4096u * 2u);
    u4 bs[14];
#pragma unroll
    for (int j = 0; j < 14; ++j) {
        int es = 2 * wave + j / 7, ct = j % 7;
        u32 off;
        if (TYPE == 0) off = (u32)(((112 * q + 16 * ct + x) * 16 + h) * 256 + 32 * es + 8 * g) * 2u;
        else { int ir = lane >> 1; int e = 32 * es + 8 * ((ir >> 2) & 3) + 4 * (ir >> 4) + (ir & 3);
               off = (u32)((h * 256 + e) * 448 + 112 * q + 16 * ct + 8 * (lane & 1)) * 2u; }
        bs[j] = ld4(rb, off, 0);
        PINA(bs[j]);
    }
    u4 a[7][8];
#pragma unroll
    for (int t = 0; t < 7; ++t) {
        int kt = 7 * wave + t;
        int k = 32 * (kt >> 1) + 8 * (x >> 2) + 4 * (kt & 1) + (x & 3) + rot;
        k = k >= 448 ? k - 448 : k;
        u32 off = (u32)((k * 16 + h) * 256 + 8 * g) * 2u;
#pragma unroll
        for (int es = 0; es < 8; ++es) a[t][es] = ld4(ra, off + 64u * es, 0);
    }
#pragma unroll
    for (int j = 0; j < 14; ++j) *(LDS u4*)(lds + L_BST + (14 * wave + j) * 1024 + lane * 16) = bs[j];
    bar();
    const u32 tro = (u32)((4 * g + (x >> 2)) * 32 + (x & 3) * 8);
    u4 b[8][7];
#pragma unroll
    for (int es = 0; es < 8; ++es)
#pragma unroll
        for (int c = 0; c < 7; ++c) {
            u32 fo = L_BST + (es * 7 + c) * 1024;
            b[es][c] = TYPE == 0 ? lds4(lds, fo + lane * 16) : ldtr2(lds, fo + tro, fo + tro + 512);
            PINA(b[es][c]);
        }
    const float sc = TYPE == 0 ? 0.0625f * 1.4426950408889634f : 1.0f;
#pragma unroll
    for (int t = 0; t < 7; ++t) {
        f4 acc[7];
#pragma unroll
        for (int c = 0; c < 7; ++c) acc[c] = (f4){0, 0, 0, 0};
#pragma unroll
        for (int es = 0; es < 8; ++es)
#pragma unroll
            for (int c = 0; c < 7; ++c) acc[c] = mfma(a[t][es], b[es][c], acc[c]);
        int kt = 7 * wave + t;
#pragma unroll
        for (int c = 0; c < 7; ++c) {
            f4 v = acc[c] * sc;
            *(LDS u2*)(lds + L_WIMG + ((kt >> 1) * 7 + c) * 1024 + lane * 16 + (kt & 1) * 8) = (u2){pk(v[0], v[1]), pk(v[2], v[3])};
        }
    }
    bar();
}

DEV u32 get_near(rsrc_t ws, int& budget) {
    wait_flags(ws, F_NB, true, 0, budget, 0);
    return (__builtin_amdgcn_readfirstlane(ld1c(ws, F_NB, 0)) & 1u) << 13;
}

DEV void poll_mem(rsrc_t ws, LDS char* dst, u32 sbase, int wave, int lane, int& budget) {
    u32 v0 = wave * 2048 + lane * 16, v1 = wave < 3 ? v0 + 1024 : v0;
    u4 a, b;
    while (true) {
        MEMBAR();
        a = ld4c(ws, v0, sbase); b = ld4c(ws, v1, sbase);
        if (!anysent(umax(umax4(a), umax4(b)))) break;
        if (--budget < 0) break;
    }
    *(LDS u4*)(dst + v0) = a;
    *(LDS u4*)(dst + v1) = b;
}

DEV void role_s(const Params& p, LDS char* lds, int h, int q, int wave) {
    w0<0>(p, lds, h, q, 112 * q, wave);
    const int lane = fresh_lane();
    const int x = lane & 15, g = lane >> 4;
    rsrc_t ws = mkrs(p.ws, WS_END);
    int budget = 400000;
    u8v W0[7], W1[7];
    const int c0 = 2 * wave, c1 = wave < 3 ? c0 + 1 : c0;
#pragma unroll
    for (int ks = 0; ks < 7; ++ks) {
        W0[ks] = cat(lds4(lds, L_WIMG + ((2 * ks) * 7 + c0) * 1024 + lane * 16), lds4(lds, L_WIMG + ((2 * ks + 1) * 7 + c0) * 1024 + lane * 16));
        W1[ks] = cat(lds4(lds, L_WIMG + ((2 * ks) * 7 + c1) * 1024 + lane * 16), lds4(lds, L_WIMG + ((2 * ks + 1) * 7 + c1) * 1024 + lane * 16));
    }
    constexpr int L_MEM = 0, L_UM = 16384;
    if (wave == 0) *(LDS u32*)(lds + L_UM + 112 * 16 + lane * 4) = 0u;
    u32 nb = get_near(ws, budget);
    u32 ao[7];
#pragma unroll
    for (int ks = 0; ks < 7; ++ks) { int b = 7 * q + 4 * ks + g; b = b >= 28 ? b - 28 : b; ao[ks] = b * 256 + x * 16; }
    const int idx = (lane & 8) ? 0xEEEE : 0x4444;
    const int kk = x >> 2, nbq = x & 3;
    const u32 umw = L_UM + x * 16 + (2 * (g & 1) + (g >> 1)) * 4;
    const u32 trf = L_UM + (16 * g + 4 * (kk >> 1) + 2 * (nbq >> 1) + (kk & 1)) * 16 + (nbq & 1) * 8;
    const u32 trp = L_UM + (16 * g + 8 * (nbq >> 1) + kk) * 16 + (nbq & 1) * 8;
    const u32 so = rphys(R_S + (h * 4 + q) * 256) + (lane & 7) * 32 + (4 * (g & 1) + 2 * (g >> 1)) * 4;
    STAMP(0, 0, 0);
    for (int s = 0; s < NSEG; ++s) {
        u32 slot = nb + (u32)(s & 3) * RING_SLOT;
        LDS char* mi = lds + L_MEM + (s & 1) * 8192;
        poll_mem(ws, mi, slot + R_MEM, wave, lane, budget);
        bar();
        STAMP(0, s, 1 + s);
        u4 A[7];
#pragma unroll
        for (int ks = 0; ks < 7; ++ks) A[ks] = lds4(mi, ao[ks]);
        f4 d0 = {0, 0, 0, 0}, d1 = {0, 0, 0, 0};
        if (wave < 3) {
#pragma unroll
            for (int ks = 0; ks < 7; ++ks) { d0 = smf64(A[ks], W0[ks], d0, idx); d1 = smf64(A[ks], W1[ks], d1, idx); }
            *(LDS u32*)(lds + umw + c0 * 256) = pk(fold32(d0[0], d0[2]), fold32(d0[1], d0[3]));
            *(LDS u32*)(lds + umw + c1 * 256) = pk(fold32(d1[0], d1[2]), fold32(d1[1], d1[3]));
        } else {
#pragma unroll
            for (int ks = 0; ks < 7; ++ks) d0 = smf64(A[ks], W0[ks], d0, idx);
            *(LDS u32*)(lds + umw + c0 * 256) = pk(fold32(d0[0], d0[2]), fold32(d0[1], d0[3]));
        }
        bar();
        if (wave == 3) {
            if (s < NSEG - 1) {
                u4 u0 = ldtr2(lds, trf, trf + 128), u1 = ldtr2(lds, trf + 1024, trf + 1152);
                f4 d = mfma(u0, A[0], (f4){0, 0, 0, 0});
                d = mfma(u1, A[1], d);
                float a0 = d[0], a2 = d[2], a1 = d[1], a3 = d[3];
                sw32(a0, a2); sw32(a1, a3);
                u2 o = {nos(asu(a0 + ror8(a2))), nos(asu(a1 + ror8(a3)))};
                if (!(lane & 8)) {
                    st2c(o, ws, so, slot);
                    st2c((u2){~0u, ~0u}, ws, so, nb + (u32)((s + 2) & 3) * RING_SLOT);
                }
            }
        } else if (wave == 0) {
            u4 u0 = ldtr2(lds, trp, trp + 64), u1 = ldtr2(lds, trp + 1024, trp + 1088);
            u32 sb = O_UM + (u32)(s * 16 + h) * 7168u + q * 1792;
            st4c(u0, ws, lane * 16, sb);
            if (g < 3) st4c(u1, ws, lane * 16 + 1024, sb);
            VMCNT(0);
            if (lane == 0) st1c((u32)s, ws, F_U + h * 128 + q * 4, 0);
        }
    }
}

DEV void role_v(const Params& p, LDS char* lds, int v, int wave) {
    const int lane = fresh_lane();
    const int x = lane & 15, g = lane >> 4, i = lane & 7;
    rsrc_t ws = mkrs(p.ws, WS_END);
    int budget = 400000;
    u32 nb = get_near(ws, budget);
    const int osel = lane >> 5, oi = (lane >> 2) & 7, op = lane & 3, oc = 4 * op + 2 * osel;
    const u32 mo = R_MEM + v * 256 + lane * 4;
    wait_flags(ws, F_B + (x * 4 + v / 7) * 4, true, 0, budget, 0);
    u8v W[4][7];
#pragma unroll
    for (int t = 0; t < 4; ++t)
#pragma unroll
        for (int ks = 0; ks < 7; ++ks) {
            u32 sb = O_BM + (u32)(v * 16 + 4 * wave + t) * 14336u;
            W[t][ks] = cat(ld4c(ws, lane * 16 + ks * 2048, sb), ld4c(ws, lane * 16 + ks * 2048 + 1024, sb));
        }
    if (wave == 0) {
        u32 m = *(const u32*)(p.Bm0 + oi * 448 + 16 * v + oc);
        st1c(nos(m), ws, mo, nb);
    }
    constexpr int L_MEM = 0, L_R = 16384;
    const int idx = (lane & 8) ? 0xEEEE : 0x4444;
    const int pos = 2 * (i >> 2) + (i & 1);
    const int idxy = pos * 0x55;
    const float mk0 = (!(lane & 8) && ((i >> 1) & 1) == 0) ? 1.f : 0.f, mk1 = (!(lane & 8) && ((i >> 1) & 1) == 1) ? 1.f : 0.f;
    const int hh = 4 * wave + g;
    const u32 sof = rphys(R_S + hh * 1024) + i * 32 + ((lane >> 3) & 1) * 16;
    const u32 sto = rphys(R_ST) + hh * 128 + i * 8;
    const u32 yo = rphys(R_Y + v * 4096) + wave * 1024 + lane * 16;
    const u32 rro = L_R + ((oi >> 2) * 64 + oc * 4 + (oi & 3)) * 4;
    STAMP(1, 0, 0);
    for (int s = 0; s < NSEG; ++s) {
        u32 slot = nb + (u32)(s & 3) * RING_SLOT;
        LDS char* mi = lds + L_MEM + (s & 1) * 8192;
        poll_mem(ws, mi, slot + R_MEM, wave, lane, budget);
        if (s > 0 && lane == 0) st1c((u32)(s - 1), ws, F_M + (v * 4 + wave) * 4, 0);
        bar();
        f4 d[4] = {{0, 0, 0, 0}, {0, 0, 0, 0}, {0, 0, 0, 0}, {0, 0, 0, 0}};
#pragma unroll
        for (int ks = 0; ks < 7; ++ks) {
            u4 A = lds4(mi, ks * 1024 + lane * 16);
#pragma unroll
            for (int t = 0; t < 4; ++t) d[t] = smf64(A, W[t][ks], d[t], idx);
        }
        u4 mb = fold4(d[0], d[1], d[2], d[3]);
        if (s < NSEG - 1) {
            u2 st; u4 s0, s1, s2, s3, y;
            while (true) {
                MEMBAR();
                st = ld2c(ws, sto, slot);
                s0 = ld4c(ws, sof, slot); s1 = ld4c(ws, sof + 256, slot); s2 = ld4c(ws, sof + 512, slot); s3 = ld4c(ws, sof + 768, slot);
                y = ld4c(ws, yo, slot);
                u32 m = umax(umax(umax(umax4(s0), umax4(s1)), umax(umax4(s2), umax4(s3))), umax(umax4(y), umax(st.x, st.y)));
                if (!anysent(m)) break;
                if (--budget < 0) break;
            }
            STAMP(1, s, 1 + s);
            f4 sc = (BC(f4, s0) + BC(f4, s1)) + (BC(f4, s2) + BC(f4, s3));
            float mx = asf(st.x), lx = asf(st.y);
            float m = fmaxf(fmaxf(fmaxf(sc[0], sc[1]), fmaxf(sc[2], sc[3])), mx);
            m = fmaxf(m, ror8(m));
            float e0 = ex2(sc[0] - m), e1 = ex2(sc[1] - m), e2 = ex2(sc[2] - m), e3 = ex2(sc[3] - m), ex = ex2(mx - m);
            float l = (e0 + e1) + (e2 + e3);
            l = l + ror8(l);
            l = fmaf(ex, lx, l);
            float r = __builtin_amdgcn_rcpf(l);
            u2 pa = {pk(e0 * r, e1 * r), pk(e2 * r, e3 * r)};
            u2 wa = {pk(ex * mk0 * r, 0.f), pk(ex * mk1 * r, 0.f)};
            f4 o = smf32(pa, mb, (f4){0, 0, 0, 0}, idx);
            o = smf32(wa, y, o, idxy);
            *(LDS f4*)(lds + L_R + wave * 1024 + lane * 16) = o;
            bar();
            if (wave == 0) {
                float a = 0.f, b = 0.f;
#pragma unroll
                for (int k = 0; k < 8; ++k) { a += *(LDS float*)(lds + rro + k * 512); b += *(LDS float*)(lds + rro + k * 512 + 16); }
                st1c(nos(pk(a, b)), ws, mo, nb + (u32)((s + 1) & 3) * RING_SLOT);
                st1c(~0u, ws, mo, nb + (u32)((s + 3) & 3) * RING_SLOT);
            }
        }
        st4c(mb, ws, lane * 16, O_MB + (u32)((s * 28 + v) * 4 + wave) * 1024u);
    }
    VMCNT(0);
    if (lane == 0) st1c((u32)(NSEG - 1), ws, F_M + (v * 4 + wave) * 4, 0);
}

DEV int ready_upto(rsrc_t ws, int h, int ntype, int need, int lane, int& budget) {
    int t = (lane >> 4) & 1, w = (lane >> 2) & 3;
    u32 vo = F_W + ((t * 16 + h) * 16 + (lane & 15)) * 4;
    bool use = lane < 16 * ntype;
    while (true) {
        MEMBAR();
        int v = (int)ld1c(ws, vo, 0);
        int up = use ? 4 * (v + 1) + w : 1000;
        if (__builtin_amdgcn_ballot_w64(up <= need) == 0ull) {
            int r = need + 1;
            while (r < NSEG && __builtin_amdgcn_ballot_w64(up <= r) == 0ull) ++r;
            MEMBAR();
            return r;
        }
        if (--budget < 0) return 1000;
        __builtin_amdgcn_s_sleep(2);
    }
}

DEV void role_x(const Params& p, LDS char* lds, int h, int wave) {
    const int lane = fresh_lane();
    const int x = lane & 15, g = lane >> 4, i = lane & 7, sel = (lane >> 3) & 1;
    rsrc_t ws = mkrs(p.ws, WS_END);
    int budget = 400000;
    u32 nb = get_near(ws, budget);
    const int kt = wave & 1, kh = wave >> 1;
    const int idx = (lane & 8) ? 0xEEEE : 0x4444;
    const u32 uo = (kt * 14 + kh * 8) * 1024 + lane * 16;
    const u32 vo = wave * 7 * 1024 + lane * 16;
    const u32 mo = kh * 4096 + lane * 16;
    const u32 k3 = kh ? 2 : 3;
    const u32 wro = (kh * 8 * 36 + (4 * (g & 1) + 2 * (g >> 1)) * 36 + 16 * kt + x) * 4;
    const u32 rdo = (i * 36 + 4 * g + 2 * sel) * 4;
    const u32 sto = rphys(R_ST) + h * 128 + i * 8;
    const u32 y0 = rphys(R_Y + (7 * wave + g) * 4096) + h * 256 + x * 16;
    const u32 y1 = rphys(R_Y + (7 * wave + 4 + (g < 3 ? g : 0)) * 4096) + h * 256 + x * 16;
    int ready = 0;
    u8v U[4]; u4 V[7];
    STAMP(2, 0, 0);
    for (int s = 0; s < NSEG - 1; ++s) {
        if (s >= ready) ready = ready_upto(ws, h, 2, s, lane, budget);
        {
            u32 sb = (u32)(s * 16 + h) * 28672u;
#pragma unroll
            for (int k = 0; k < 3; ++k) U[k] = cat(ld4c(ws, uo + k * 2048, O_U + sb), ld4c(ws, uo + k * 2048 + 1024, O_U + sb));
            if (kh == 0) U[3] = cat(ld4c(ws, uo + 6144, O_U + sb), ld4c(ws, uo + 7168, O_U + sb)); else U[3] = (u8v){0, 0, 0, 0, 0, 0, 0, 0};
#pragma unroll
            for (int k = 0; k < 7; ++k) V[k] = ld4c(ws, vo + k * 1024, O_VO + sb);
        }
        u32 slot = nb + (u32)(s & 3) * RING_SLOT;
        u4 a0, a1, a2, a3;
        while (true) {
            MEMBAR();
            a0 = ld4c(ws, mo, slot); a1 = ld4c(ws, mo + 1024, slot); a2 = ld4c(ws, mo + 2048, slot); a3 = ld4c(ws, mo + k3 * 1024, slot);
            if (!anysent(umax(umax(umax4(a0), umax4(a1)), umax(umax4(a2), umax4(a3))))) break;
            if (--budget < 0) break;
        }
        STAMP(2, s, 1 + s);
        f4 d = smf64(a0, U[0], (f4){0, 0, 0, 0}, idx);
        d = smf64(a1, U[1], d, idx); d = smf64(a2, U[2], d, idx); d = smf64(a3, U[3], d, idx);
        LDS char* sb = lds + (s & 1) * 4096;
        *(LDS float*)(sb + wro) = fold32(d[0], d[2]);
        *(LDS float*)(sb + wro + 144) = fold32(d[1], d[3]);
        bar();
        f2 p0 = *(LDS f2*)(sb + rdo) + *(LDS f2*)(sb + rdo + 1152);
        f2 p1 = *(LDS f2*)(sb + rdo + 64) + *(LDS f2*)(sb + rdo + 1152 + 64);
        float m = fmaxf(fmaxf(p0[0], p0[1]), fmaxf(p1[0], p1[1]));
        m = fmaxf(m, ror8(m)); m = xmax16(m); m = xmax32(m);
        float e0 = ex2(p0[0] - m), e1 = ex2(p0[1] - m), e2 = ex2(p1[0] - m), e3 = ex2(p1[1] - m);
        u2 pa = {pk(e0, e1), pk(e2, e3)};
        u32 nslot = nb + (u32)((s + 2) & 3) * RING_SLOT;
        if (wave == 3) {
            float l = (e0 + e1) + (e2 + e3);
            l = l + ror8(l); l = xadd16(l); l = xadd32(l);
            if (lane < 8) st2c((u2){nos(asu(m)), nos(asu(l))}, ws, sto, slot);
        }
        f4 y[7];
#pragma unroll
        for (int k = 0; k < 7; ++k) y[k] = smf32(pa, V[k], (f4){0, 0, 0, 0}, idx);
        u4 o0 = fold4(y[0], y[1], y[2], y[3]);
        st4c((u4){nos(o0.x), nos(o0.y), nos(o0.z), nos(o0.w)}, ws, y0, slot);
        u4 o1 = fold4(y[4], y[5], y[6], y[6]);
        if (g < 3) st4c((u4){nos(o1.x), nos(o1.y), nos(o1.z), nos(o1.w)}, ws, y1, slot);
        st4c((u4){~0u, ~0u, ~0u, ~0u}, ws, y0, nslot);
        if (g < 3) st4c((u4){~0u, ~0u, ~0u, ~0u}, ws, y1, nslot);
        if (wave == 3 && lane < 8) st2c((u2){~0u, ~0u}, ws, sto, nslot);
    }
}

DEV void role_p(const Params& p, int h, int wave) {
    const int lane = fresh_lane();
    const int x = lane & 15, g = lane >> 4;
    rsrc_t ws = mkrs(p.ws, WS_END);
    rsrc_t rx = mkrs(p.Bx, 2048u * 896u);
    int budget = 400000;
    const int it = wave & 1;
    int ready = 0;
    const float NEG = -1.0e9f * 1.4426950408889634f;
    for (int s = wave >> 1; s < NSEG; s += 2) {
        u4 X[14];
#pragma unroll
        for (int m = 0; m < 14; ++m) X[m] = ld4(rx, (u32)(16 * it + x) * 896u + g * 16 + m * 64, (u32)s * 28672u);
        if (s >= ready) ready = ready_upto(ws, h, 1, s, lane, budget);
        u32 sb = O_U + (u32)(s * 16 + h) * 28672u;
        f4 d0 = {0, 0, 0, 0}, d1 = {0, 0, 0, 0};
        {
            u4 Uf[14];
#pragma unroll
            for (int m = 0; m < 14; ++m) Uf[m] = ld4c(ws, lane * 16 + m * 1024, sb);
            if (it) {
                u4 Ug[14];
#pragma unroll
                for (int m = 0; m < 14; ++m) Ug[m] = ld4c(ws, lane * 16 + m * 1024 + 14336, sb);
#pragma unroll
                for (int m = 0; m < 14; ++m) { d0 = mfma(Uf[m], X[m], d0); d1 = mfma(Ug[m], X[m], d1); }
            } else {
#pragma unroll
                for (int m = 0; m < 14; ++m) d0 = mfma(Uf[m], X[m], d0);
            }
        }
#pragma unroll
        for (int r = 0; r < 4; ++r) {
            bool ok = 4 * g + r <= x;
            if (it) d1[r] = ok ? d1[r] : NEG; else { d0[r] = ok ? d0[r] : NEG; d1[r] = NEG; }
        }
        wait_flags(ws, F_U + h * 128 + (lane & 3) * 4, true, s, budget, 1);
        f4 dm = {0, 0, 0, 0}, dn = {0, 0, 0, 0};
        {
            u4 M[14];
            u32 mb = O_UM + (u32)(s * 16 + h) * 7168u;
#pragma unroll
            for (int m = 0; m < 14; ++m) M[m] = ld4c(ws, g * 128 + (lane & 7) * 16 + m * 512, mb);
#pragma unroll
            for (int m = 0; m < 14; m += 2) { dm = mfma(M[m], X[m], dm); dn = mfma(M[m + 1], X[m + 1], dn); }
        }
        dm += dn;
        float mx = fmaxf(fmaxf(fmaxf(d0[0], d0[1]), fmaxf(d0[2], d0[3])), fmaxf(fmaxf(d1[0], d1[1]), fmaxf(d1[2], d1[3])));
        mx = fmaxf(mx, fmaxf(fmaxf(dm[0], dm[1]), fmaxf(dm[2], dm[3])));
        mx = xmax16(mx); mx = xmax32(mx);
        float ma = g < 2 ? dm[0] : dm[2], mb2 = g < 2 ? dm[1] : dm[3];
        float ea = ex2(ma - mx), eb = ex2(mb2 - mx);
        float l = ea + eb;
#pragma unroll
        for (int r = 0; r < 4; ++r) { d0[r] = ex2(d0[r] - mx); d1[r] = ex2(d1[r] - mx); l += d0[r] + d1[r]; }
        l = xadd16(l); l = xadd32(l);
        float r = __builtin_amdgcn_rcpf(l);
        st4c((u4){pk(d0[0] * r, d0[1] * r), pk(d0[2] * r, d0[3] * r), pk(d1[0] * r, d1[1] * r), pk(d1[2] * r, d1[3] * r)}, ws, lane * 16,
             O_PX + (u32)((s * 16 + h) * 2 + it) * 1024u);
        st1c(pk(ea * r, eb * r), ws, ((h & 3) * 16 + x) * 16 + g * 4, O_PM + (u32)((s * 4 + (h >> 2)) * 2 + it) * 1024u);
        VMCNT(0);
        if (lane == 0) st1c((u32)s, ws, F_P + (h * 4 + wave) * 4, 0);
        STAMP(3, s, 1 + s);
    }
}

template <int TYPE>
DEV void gemm_uv(const Params& p, LDS char* lds, int h, int q, int wave) {
    w0<TYPE>(p, lds, h, q, 0, wave);
    const int lane = fresh_lane();
    const int x = lane & 15, g = lane >> 4;
    rsrc_t ws = mkrs(p.ws, WS_END);
    rsrc_t rx = mkrs(p.Bx, 2048u * 896u);
    STAMP(4, 0, 0);
    u32 xo = (u32)x * 896u + g * 16;
    u4 X[14];
#pragma unroll
    for (int m = 0; m < 14; ++m) X[m] = ld4(rx, xo + m * 64, (u32)wave * 28672u);
    if (TYPE == 1) {
        for (int f = wave; f < 98; f += 4) {
            int m = f / 7, c = f - 7 * m;
            st4c(lds4(lds, L_WIMG + f * 1024 + lane * 16), ws, lane * 16, O_BM + (u32)(((7 * q + c) * 16 + h) * 14 + m) * 1024u);
        }
        VMCNT(0);
        bar();
        if (wave == 0 && lane == 0) st1c(0u, ws, F_B + (h * 4 + q) * 4, 0);
    }
    u4 W[14][6];
#pragma unroll
    for (int m = 0; m < 14; ++m)
#pragma unroll
        for (int c = 0; c < 6; ++c) { W[m][c] = lds4(lds, L_WIMG + (m * 7 + c) * 1024 + lane * 16); if (c < 4) PINA(W[m][c]); else PINV(W[m][c]); }
    u32 w6a = L_WIMG + 6144 + lane * 16, w6b = w6a + 7 * 7168;
    u4 w6 = lds4(lds, w6a);
    const u32 fo = F_W + (((TYPE * 16 + h) * 4 + wave) * 4 + q) * 4;
    u32 so = TYPE == 0 ? (u32)((g & 1) * 14336 + ((g >> 1) * 16 + x) * 16 + q * 3584) : (u32)(lane * 16 + q * 7168);
    for (int n = 0; n < 16; ++n) {
        const int s = wave + 4 * n;
        u2 keep[7];
#pragma unroll
        for (int kt = 0; kt < 2; ++kt) {
            f4 acc[7];
#pragma unroll
            for (int c = 0; c < 7; ++c) acc[c] = (f4){0, 0, 0, 0};
            u32 nx = kt == 0 ? (u32)s * 28672u + 14336u : (u32)((s + 4) & 63) * 28672u;
            asm volatile("" : "+v"(xo), "+v"(w6a), "+v"(w6b));
#pragma unroll
            for (int m = 0; m < 14; ++m) {
                const int mn = (m + 1) % 14;
                u4 w6n = lds4(lds, (mn < 7 ? w6a : w6b) + (mn % 7) * 7168);
                u4 xf = X[m];
#pragma unroll
                for (int c = 0; c < 6; ++c) acc[c] = TYPE == 0 ? mfma(W[m][c], xf, acc[c]) : mfma(xf, W[m][c], acc[c]);
                acc[6] = TYPE == 0 ? mfma(w6, xf, acc[6]) : mfma(xf, w6, acc[6]);
                X[m] = ld4(rx, xo + m * 64, nx);
                w6 = w6n;
                __builtin_amdgcn_sched_barrier(0);
            }
            if (kt == 0) {
                if (n > 0) { VMCNT(14); if (lane == 0) st1c((u32)(n - 1), ws, fo, 0); }
#pragma unroll
                for (int c = 0; c < 7; ++c) keep[c] = (u2){pk(acc[c][0], acc[c][1]), pk(acc[c][2], acc[c][3])};
            } else {
                u32 sb = (TYPE == 0 ? O_U : O_VO) + (u32)(s * 16 + h) * 28672u;
                asm volatile("" : "+v"(so));
#pragma unroll
                for (int c = 0; c < 7; ++c) {
                    u32 a0 = keep[c].x, a1 = keep[c].y, b0 = pk(acc[c][0], acc[c][1]), b1 = pk(acc[c][2], acc[c][3]);
                    if (TYPE == 0) { sw16u(a0, b0); sw16u(a1, b1); st4c((u4){a0, a1, b0, b1}, ws, so + c * 512, sb); }
                    else st4c((u4){a0, a1, b0, b1}, ws, so + (c & 3) * 1024, sb + (c >> 2) * 4096);
                }
                MEMBAR();
            }
        }
    }
    VMCNT(0);
    if (lane == 0) st1c(15u, ws, fo, 0);
}

DEV float phi(float t) { return (t > 0.f ? t + 1.f : ex2(t * 1.4426950408889634f)) + 1e-6f; }

DEV void part_a(const Params& p, LDS char* lds, int wi, int wave, int& budget) {
    const int lane = fresh_lane();
    const int x = lane & 15, g = lane >> 4, tid = wave * 64 + lane;
    rsrc_t ws = mkrs(p.ws, WS_END);
    {
        const int h = wi >> 3, rc = wi & 7;
        rsrc_t rw = mkrs(p.Awqkv, 448u * 1344u * 2u);
        rsrc_t rx = mkrs(p.Ax, 2048u * 896u);
        bar();
        {
            u4 st[21];
            const int ir = lane >> 1, kl = 8 * ((ir >> 2) & 3) + 4 * (ir >> 4) + (ir & 3);
#pragma unroll
            for (int j = 0; j < 21; ++j) {
                int f = wave * 21 + j, m = f / 6, t = f % 6;
                st[j] = ld4(rw, (u32)((32 * m + kl) * 1344 + (t >> 1) * 448 + h * 28 + ((t & 1) ? 16 + (lane & 1) * 4 : (lane & 1) * 8)) * 2u, 0);
                if ((t & 1) && (lane & 1)) st[j] = (u4){st[j].z, st[j].w, 0u, 0u};
            }
#pragma unroll
            for (int j = 0; j < 21; ++j) *(LDS u4*)(lds + (wave * 21 + j) * 1024 + lane * 16) = st[j];
        }
        u4 X[4][14];
#pragma unroll
        for (int m = 0; m < 14; ++m)
#pragma unroll
            for (int t = 0; t < 4; ++t) X[t][m] = ld4(rx, (u32)(16 * t + x) * 896u + g * 16 + m * 64, (u32)(rc * 256 + wave * 64) * 896u);
        bar();
        f4 acc[4][6];
#pragma unroll
        for (int t = 0; t < 4; ++t)
#pragma unroll
            for (int c = 0; c < 6; ++c) acc[t][c] = (f4){0, 0, 0, 0};
        const u32 tro = (u32)((4 * g + (x >> 2)) * 32 + (x & 3) * 8);
#pragma unroll
        for (int m = 0; m < 14; ++m)
#pragma unroll
            for (int c = 0; c < 6; ++c) {
                u4 w = ldtr2(lds, (m * 6 + c) * 1024 + tro, (m * 6 + c) * 1024 + tro + 512);
#pragma unroll
                for (int t = 0; t < 4; ++t) acc[t][c] = c < 2 ? mfma(w, X[t][m], acc[t][c]) : mfma(X[t][m], w, acc[t][c]);
            }
#pragma unroll
        for (int t = 0; t < 4; ++t) {
            u4 o = {pk(phi(acc[t][0][0]), phi(acc[t][0][1])), pk(phi(acc[t][0][2]), phi(acc[t][0][3])),
                    pk(phi(acc[t][1][0]), phi(acc[t][1][1])), pk(phi(acc[t][1][2]), phi(acc[t][1][3]))};
            if (g == 3) { o.z = 0u; o.w = 0u; }
            st4c(o, ws, lane * 16, O_PQ + (u32)((rc * 16 + wave * 4 + t) * 16 + h) * 1024u);
        }
        f4 kv[2][2] = {{{0, 0, 0, 0}, {0, 0, 0, 0}}, {{0, 0, 0, 0}, {0, 0, 0, 0}}};
#pragma unroll
        for (int tp = 0; tp < 2; ++tp) {
            u4 kf[2], vf[2];
#pragma unroll
            for (int d = 0; d < 2; ++d) {
                f4 a = acc[2 * tp][2 + d], b = acc[2 * tp + 1][2 + d];
                kf[d] = (u4){pk(phi(a[0]), phi(a[1])), pk(phi(a[2]), phi(a[3])), pk(phi(b[0]), phi(b[1])), pk(phi(b[2]), phi(b[3]))};
                a = acc[2 * tp][4 + d]; b = acc[2 * tp + 1][4 + d];
                vf[d] = (u4){pk(a[0], a[1]), pk(a[2], a[3]), pk(b[0], b[1]), pk(b[2], b[3])};
            }
            if (x >= 12) { u32 f = x == 12 ? 0x3F803F80u : 0u; vf[1] = (u4){f, f, f, f}; kf[1] = (u4){0, 0, 0, 0}; }
#pragma unroll
            for (int d = 0; d < 2; ++d)
#pragma unroll
                for (int e = 0; e < 2; ++e) kv[d][e] = mfma(vf[e], kf[d], kv[d][e]);
        }
        constexpr int L_R = 90112;
#pragma unroll
        for (int d = 0; d < 2; ++d)
#pragma unroll
            for (int e = 0; e < 2; ++e) *(LDS f4*)(lds + L_R + wave * 4096 + (d * 2 + e) * 1024 + lane * 16) = kv[d][e];
        bar();
        f4 sum = (*(LDS f4*)(lds + L_R + tid * 16) + *(LDS f4*)(lds + L_R + 4096 + tid * 16)) +
                 (*(LDS f4*)(lds + L_R + 8192 + tid * 16) + *(LDS f4*)(lds + L_R + 12288 + tid * 16));
        st4c(BC(u4, sum), ws, tid * 16, O_KV + (u32)wi * 4096u);
        VMCNT(0);
        bar();
        if (tid == 0) st1c(0u, ws, F_A1 + wi * 4, 0);
    }
    if (wi < 112) {
        const int h = wi / 7, ct = (wi % 7) * 4 + wave;
        u32 wo[8];
#pragma unroll
        for (int j = 0; j < 8; ++j) {
            int e = 16 * (j >> 2) + 4 * g + (j & 3);
            wo[j] = e < 28 ? p.Awo[(h * 28 + e) * 448 + 16 * ct + x] : (u16)0;
        }
        u4 wf = {wo[0] | (wo[1] << 16), wo[2] | (wo[3] << 16), wo[4] | (wo[5] << 16), wo[6] | (wo[7] << 16)};
        wait_flags(ws, F_A1 + (h * 8 + (lane & 7)) * 4, true, 0, budget, 0);
        f4 kv[4];
#pragma unroll
        for (int t = 0; t < 4; ++t) {
            kv[t] = (f4){0, 0, 0, 0};
#pragma unroll
            for (int rc = 0; rc < 8; ++rc) kv[t] += BC(f4, ld4c(ws, lane * 16 + t * 1024, O_KV + (u32)(h * 8 + rc) * 4096u));
        }
        f4 g0 = mfma((u4){pk(kv[0][0], kv[0][1]), pk(kv[0][2], kv[0][3]), pk(kv[1][0], kv[1][1]), pk(kv[1][2], kv[1][3])}, wf, (f4){0, 0, 0, 0});
        f4 g1 = mfma((u4){pk(kv[2][0], kv[2][1]), pk(kv[2][2], kv[2][3]), pk(kv[3][0], kv[3][1]), pk(kv[3][2], kv[3][3])}, wf, (f4){0, 0, 0, 0});
        st4c((u4){pk(g0[0], g0[1]), pk(g0[2], g0[3]), pk(g1[0], g1[1]), pk(g1[2], g1[3])}, ws, lane * 16, O_G + (u32)(ct * 16 + h) * 1024u);
        if (ct == 0) {
            float z[8];
#pragma unroll
            for (int j = 0; j < 8; ++j) z[j] = __shfl(j < 4 ? kv[1][0] : kv[3][0], 48 + 4 * g + (j & 3));
            st4c((u4){pk(z[0], z[1]), pk(z[2], z[3]), pk(z[4], z[5]), pk(z[6], z[7])}, ws, lane * 16, O_Z + (u32)h * 1024u);
        }
        VMCNT(0);
        bar();
        if (tid == 0) st1c(0u, ws, F_G + wi * 4, 0);
    }
    {
        wait_flags(ws, F_G + lane * 4, true, 0, budget, 0);
        wait_flags(ws, F_G + 256 + lane * 4, lane < 48, 0, budget, 0);
        u4 G[7][4];
#pragma unroll
        for (int c = 0; c < 7; ++c)
#pragma unroll
            for (int j = 0; j < 4; ++j) G[c][j] = ld4c(ws, lane * 16 + j * 1024, O_G + (u32)((7 * wave + c) * 16) * 1024u);
#pragma unroll
        for (int j = 0; j < 4; ++j) {
            int h = 4 * wave + j;
            u4 pq = ld4c(ws, lane * 16, O_PQ + (u32)(wi * 16 + h) * 1024u);
            u4 z = ld4c(ws, lane * 16, O_Z + (u32)h * 1024u);
            f4 den = mfma(z, pq, (f4){0, 0, 0, 0});
            float r = 1.0f / (den[0] + 1e-6f);
            u4 o;
#pragma unroll
            for (int k = 0; k < 4; ++k) o[k] = pk(asf(pq[k] << 16) * r, asf(pq[k] & 0xFFFF0000u) * r);
            *(LDS u4*)(lds + h * 1024 + lane * 16) = o;
        }
        bar();
        f4 acc[7];
#pragma unroll
        for (int c = 0; c < 7; ++c) acc[c] = (f4){0, 0, 0, 0};
#pragma unroll
        for (int hb = 0; hb < 4; ++hb) {
#pragma unroll
            for (int j = 0; j < 4; ++j) {
                u4 b = lds4(lds, (hb * 4 + j) * 1024 + lane * 16);
#pragma unroll
                for (int c = 0; c < 7; ++c) {
                    acc[c] = mfma(G[c][j], b, acc[c]);
                    if (hb < 3) G[c][j] = ld4c(ws, lane * 16 + j * 1024, O_G + (u32)((7 * wave + c) * 16 + hb * 4 + 4) * 1024u);
                }
            }
        }
#pragma unroll
        for (int c = 0; c < 7; ++c)
            *(u2*)(p.out + (size_t)(wi * 16 + x) * 896 + (7 * wave + c) * 16 + 4 * g) = (u2){pk(acc[c][0], acc[c][1]), pk(acc[c][2], acc[c][3])};
        bar();
    }
}

DEV void o_items(const Params& p, LDS char* lds, int wi, int wave, int& budget) {
    const int lane = fresh_lane();
    const int tid = wave * 64 + lane;
    rsrc_t ws = mkrs(p.ws, WS_END);
    wait_flags(ws, F_W + 1024 + tid * 4, true, 15, budget, 0);
    for (int n = wi; n < NSEG * 7; n += 128) {
        const int s = n / 7, cg = n - 7 * s;
        u4 V[4][4];
#pragma unroll
        for (int j = 0; j < 4; ++j)
#pragma unroll
            for (int c = 0; c < 4; ++c) V[j][c] = ld4c(ws, lane * 16 + c * 1024, O_VO + (u32)(s * 16 + 4 * wave + j) * 28672u + cg * 4096);
        {
            bool isp = lane < 16;
            u32 vo = isp ? F_P + (16 * wave + lane) * 4 : F_M + ((4 * cg + (lane & 3)) * 4 + wave) * 4;
            bool use = isp ? (((lane >> 1) & 1) == (s & 1)) : lane < 20;
            wait_flags(ws, vo, use, s, budget, 0);
        }
        u4 PX[4][2], PM[2], MB[4];
#pragma unroll
        for (int j = 0; j < 4; ++j)
#pragma unroll
            for (int t = 0; t < 2; ++t) PX[j][t] = ld4c(ws, lane * 16 + (j * 2 + t) * 1024, O_PX + (u32)(s * 16 + 4 * wave) * 2048u);
#pragma unroll
        for (int t = 0; t < 2; ++t) PM[t] = ld4c(ws, lane * 16 + t * 1024, O_PM + (u32)(s * 4 + wave) * 2048u);
#pragma unroll
        for (int c = 0; c < 4; ++c) MB[c] = ld4c(ws, lane * 16, O_MB + (u32)((s * 28 + 4 * cg + c) * 4 + wave) * 1024u);
        f4 acc[2][4];
#pragma unroll
        for (int t = 0; t < 2; ++t)
#pragma unroll
            for (int c = 0; c < 4; ++c) {
                acc[t][c] = (f4){0, 0, 0, 0};
#pragma unroll
                for (int j = 0; j < 4; ++j) acc[t][c] = mfma(V[j][c], PX[j][t], acc[t][c]);
                acc[t][c] = mfma(MB[c], PM[t], acc[t][c]);
                *(LDS f4*)(lds + wave * 8192 + (t * 4 + c) * 1024 + lane * 16) = acc[t][c];
            }
        bar();
        {
            int row = tid >> 3, c8 = tid & 7;
            u32 ro = ((row >> 4) * 4 + (c8 >> 1)) * 1024 + ((2 * (c8 & 1)) * 16 + (row & 15)) * 16;
            f4 a = {0, 0, 0, 0}, b = {0, 0, 0, 0};
#pragma unroll
            for (int w = 0; w < 4; ++w) { a += *(LDS f4*)(lds + w * 8192 + ro); b += *(LDS f4*)(lds + w * 8192 + ro + 256); }
            *(u4*)(p.out + (size_t)(s * 32 + row) * 896 + 448 + cg * 64 + c8 * 8) = (u4){pk(a[0], a[1]), pk(a[2], a[3]), pk(b[0], b[1]), pk(b[2], b[3])};
        }
        bar();
        STAMP(5, s, 8 + n / 128);
    }
}

DEV void role_probe(const Params& p, int wave) {
    const int lane = fresh_lane();
    if (wave != 0) return;
    rsrc_t ws = mkrs(p.ws, WS_END);
    u32 best[2] = {~0u, ~0u};
    if (lane == 0) {
        u32 zero = 0;
        for (int r = 0; r < 8; ++r) {
            char* a = p.ws + FLAGS + 3072 + (r & 1) * 8192 + (r >> 1) * 256;
            u64 t0, t1; u32 v;
            asm volatile("s_memtime %0\n\ts_waitcnt lgkmcnt(0)\n\tglobal_atomic_or %2, %3, %4, off sc0 sc1\n\ts_waitcnt vmcnt(0)\n\ts_memtime %1\n\ts_waitcnt lgkmcnt(0)"
                         : "=&s"(t0), "=&s"(t1), "=&v"(v) : "v"(a), "v"(zero) : "memory");
            u32 d = (u32)(t1 - t0);
            if (r >= 2) { if (r & 1) best[1] = d < best[1] ? d : best[1]; else best[0] = d < best[0] ? d : best[0]; }
        }
        st1c(best[1] < best[0] ? 1u : 0u, ws, F_NB, 0);
    }
}

__global__ void __launch_bounds__(256, 1) mega(Params p) {
    extern __shared__ __attribute__((aligned(16))) char smem[];
    LDS char* lds = (LDS char*)smem;
    const int wave = __builtin_amdgcn_readfirstlane(threadIdx.x >> 6);
    const int xc = blockIdx.x & 7, r = blockIdx.x >> 3;
    STAMP(9, 0, 127);
    if (xc < 4) {
        if (r < 16) role_s(p, lds, 4 * xc + (r >> 2), r & 3, wave);
        else if (r < 20) role_x(p, lds, 4 * xc + r - 16, wave);
        else if (r < 24) role_p(p, 4 * xc + r - 20, wave);
        else if (r < 31) role_v(p, lds, 7 * xc + r - 24, wave);
        else if (xc == 0) role_probe(p, wave);
    } else {
        const int wi = (xc - 4) * 32 + r;
        const int grp = wi >> 2;
        if (grp & 1) gemm_uv<1>(p, lds, grp >> 1, wi & 3, wave);
        else gemm_uv<0>(p, lds, grp >> 1, wi & 3, wave);
        STAMP(4, 0, 1);
        int budget = 400000;
        part_a(p, lds, wi, wave, budget);
        STAMP(4, 0, 2);
        o_items(p, lds, wi, wave, budget);
    }
    STAMP(9, 0, 126);
}

extern "C" void kernel_launch(void* const* d_in, const int* in_sizes, int n_in, void* d_out, int out_size, void* d_ws, size_t ws_size,
                              hipStream_t stream) {
    Params p;
    p.Ax = (const u16*)d_in[0]; p.Awqkv = (const u16*)d_in[1]; p.Awo = (const u16*)d_in[2];
    p.Bx = (const u16*)d_in[3]; p.Bm0 = (const u16*)d_in[4];
    p.Wq = (const u16*)d_in[5]; p.Wk = (const u16*)d_in[6]; p.Wv = (const u16*)d_in[7]; p.Wo = (const u16*)d_in[8];
    p.out = (u16*)d_out;
    p.ws = (char*)(((uintptr_t)d_ws + (2u << 20) - 1) & ~(uintptr_t)((2u << 20) - 1));
    (void)hipFuncSetAttribute((const void*)mega, hipFuncAttributeMaxDynamicSharedMemorySize, LDS_BYTES);
    (void)hipMemsetAsync(p.ws, 0xFF, INIT_BYTES, stream);
    mega<<<256, 256, LDS_BYTES, stream>>>(p);
}
```

```cpp
#define T_A 1600
#define T_B 1450
#define T_XG 1800
#include <hip/hip_runtime.h>
#include <hip/hip_bf16.h>
#include <stdint.h>

typedef unsigned u32;
typedef unsigned long long u64;
typedef unsigned short u16;
typedef u32 u2 __attribute__((ext_vector_type(2)));
typedef u32 u4 __attribute__((ext_vector_type(4)));
typedef u32 u8v __attribute__((ext_vector_type(8)));
typedef float f2 __attribute__((ext_vector_type(2)));
typedef float f4 __attribute__((ext_vector_type(4)));
typedef __bf16 bf2 __attribute__((ext_vector_type(2)));
typedef __bf16 bf8 __attribute__((ext_vector_type(8)));
typedef __bf16 bf16v __attribute__((ext_vector_type(16)));
typedef short s4 __attribute__((ext_vector_type(4)));
typedef short s8 __attribute__((ext_vector_type(8)));
#define LDS __attribute__((address_space(3)))
#define DEV __device__ __forceinline__
typedef __amdgpu_buffer_rsrc_t rsrc_t;
#define BC(T, x) __builtin_bit_cast(T, x)

constexpr int NSEG = 64;
constexpr int SC1 = 16;
constexpr u32 F_L = 0;
constexpr u32 F_S = 2048;
constexpr u32 F_U = 4096;
constexpr u32 F_A1 = 6144;
constexpr u32 F_G = 6656;
constexpr u32 F_Q = 16384;
constexpr u32 F_M = 20480;
constexpr u32 F_P = 32768;
constexpr u32 F_B = 49152;
constexpr u32 F_ARM = 57344;
constexpr u32 F_X = 61440;
constexpr u32 F_K = 65536;
constexpr u32 F_R = 69632;
constexpr u32 F_W = 81920;
constexpr u32 F_H = 114688;
constexpr u32 GRP = 32768u;
__host__ __device__ constexpr u32 kgrp(u32 j) { return (j << 15) | ((j << 8) & 0x800u) | ((j << 10) & 0x4000u) | ((j << 7) & 0x1000u); }
#define T_VM 100
constexpr u32 OOB = 0x80000000u;
constexpr u32 O_U = 10u << 20;
constexpr u32 O_VO = O_U + 64u * 16u * 28672u;
constexpr u32 O_BM = O_VO + 64u * 16u * 28672u;
constexpr u32 O_UM = O_BM + 28u * 16u * 14336u;
constexpr u32 O_XP = O_UM + 64u * 16u * 7168u;
constexpr u32 O_MB = O_XP + 64u * 28672u;
constexpr u32 O_PX = O_MB + 64u * 28u * 4096u;
constexpr u32 O_PM = O_PX + 64u * 16u * 2048u;
constexpr u32 O_PQ = O_PM + 64u * 8192u;
constexpr u32 O_KV = O_PQ + 128u * 16u * 1024u;
constexpr u32 O_G = O_KV + 128u * 4096u;
constexpr u32 O_Z = O_G + 28u * 16u * 1024u;
constexpr u32 O_AX = O_Z + 16384u;
constexpr u32 O_AW = O_AX + 128u * 14336u;
constexpr u32 O_DBG = O_AW + 16u * 84u * 1024u;
constexpr u32 O_DBG2 = O_DBG + (1u << 20);
constexpr u32 WS_END = O_DBG2 + (2u << 20);
constexpr int LDS_BYTES = 160 * 1024;
constexpr int L_BUF = 73728;

struct Params {
    const u16* Ax; const u16* Awqkv; const u16* Awo; const u16* Bx; const u16* Bm0;
    const u16* Wq; const u16* Wk; const u16* Wv; const u16* Wo;
    u16* out; char* ws;
};

DEV float asf(u32 x) { return __builtin_bit_cast(float, x); }
DEV u32 asu(float x) { return __builtin_bit_cast(u32, x); }
DEV f4 mfma(u4 a, u4 b, f4 c) { return __builtin_amdgcn_mfma_f32_16x16x32_bf16(BC(bf8, a), BC(bf8, b), c, 0, 0, 0); }
DEV f4 smf64(u4 a, u8v b, f4 c, int idx) { return __builtin_amdgcn_smfmac_f32_16x16x64_bf16(BC(bf8, a), BC(bf16v, b), c, idx, 0, 0); }
DEV f4 smf32(u2 a, u4 b, f4 c, int idx) { return __builtin_amdgcn_smfmac_f32_16x16x32_bf16(BC(s4, a), BC(s8, b), c, idx, 0, 0); }
DEV u32 pk(float lo, float hi) { f2 v = {lo, hi}; return BC(u32, __builtin_convertvector(v, bf2)); }
DEV u8v cat(u4 a, u4 b) { return __builtin_shufflevector(a, b, 0, 1, 2, 3, 4, 5, 6, 7); }
DEV rsrc_t mkrs(const void* p, u32 bytes) { return __builtin_amdgcn_make_buffer_rsrc((void*)p, (short)0, (int)bytes, 0x00020000); }
DEV u4 ld4(rsrc_t r, u32 v, u32 s) { return __builtin_amdgcn_raw_buffer_load_b128(r, v, s, 0); }
DEV u4 ld4c(rsrc_t r, u32 v, u32 s) { return __builtin_amdgcn_raw_buffer_load_b128(r, v, s, SC1); }
DEV u2 ld2c(rsrc_t r, u32 v, u32 s) { return __builtin_amdgcn_raw_buffer_load_b64(r, v, s, SC1); }
DEV u32 ld1c(rsrc_t r, u32 v, u32 s) { return __builtin_amdgcn_raw_buffer_load_b32(r, v, s, SC1); }
DEV void st4c(u4 x, rsrc_t r, u32 v, u32 s) { __builtin_amdgcn_raw_buffer_store_b128(x, r, v, s, SC1); asm volatile("s_nop 1" :: "v"(x)); }
DEV void st2c(u2 x, rsrc_t r, u32 v, u32 s) { __builtin_amdgcn_raw_buffer_store_b64(x, r, v, s, SC1); }
DEV void st1c(u32 x, rsrc_t r, u32 v, u32 s) { __builtin_amdgcn_raw_buffer_store_b32(x, r, v, s, SC1); }
DEV u4 lds4(LDS char* l, u32 o) { return *(LDS u4*)(l + o); }
DEV u2 ldtr(LDS char* l, u32 o) { return BC(u2, __builtin_amdgcn_ds_read_tr16_b64_v4i16((LDS s4*)(l + o))); }
DEV u4 ldtr2(LDS char* l, u32 o0, u32 o1) { u2 a = ldtr(l, o0), b = ldtr(l, o1); return (u4){a.x, a.y, b.x, b.y}; }
#define PINA(x) asm("" : "+a"(x))
#define PINV(x) asm("" : "+v"(x))
DEV int fresh_lane() { int l; asm volatile("v_mbcnt_lo_u32_b32 %0, -1, 0\n\tv_mbcnt_hi_u32_b32 %0, -1, %0" : "=v"(l)); return l; }
#define MEMBAR() asm volatile("" ::: "memory")
#define VMCNT(n) asm volatile("s_waitcnt vmcnt(" #n ")" ::: "memory")
DEV void bar() { asm volatile("s_waitcnt lgkmcnt(0)\n\ts_barrier" ::: "memory"); }
DEV float ror8(float x) { return asf((u32)__builtin_amdgcn_mov_dpp((int)asu(x), 0x128, 0xf, 0xf, true)); }
DEV void sw32(float& a, float& b) { auto r = __builtin_amdgcn_permlane32_swap(asu(a), asu(b), false, false); a = asf(r[0]); b = asf(r[1]); }
DEV void sw16u(u32& a, u32& b) { auto r = __builtin_amdgcn_permlane16_swap(a, b, false, false); a = r[0]; b = r[1]; }
DEV float fold32(float a, float b) { sw32(a, b); return a + b; }
DEV float xmax16(float x) { auto r = __builtin_amdgcn_permlane16_swap(asu(x), asu(x), false, false); return fmaxf(asf(r[0]), asf(r[1])); }
DEV float xmax32(float x) { auto r = __builtin_amdgcn_permlane32_swap(asu(x), asu(x), false, false); return fmaxf(asf(r[0]), asf(r[1])); }
DEV float xadd16(float x) { auto r = __builtin_amdgcn_permlane16_swap(asu(x), asu(x), false, false); return asf(r[0]) + asf(r[1]); }
DEV float xadd32(float x) { auto r = __builtin_amdgcn_permlane32_swap(asu(x), asu(x), false, false); return asf(r[0]) + asf(r[1]); }
DEV float ex2(float x) { return __builtin_amdgcn_exp2f(x); }
DEV u32 nos(u32 x) { return x < 0xFFFFFFFEu ? x : 0xFFFFFFFEu; }
DEV u32 umax4(u4 v) { u32 a = v.x > v.y ? v.x : v.y, b = v.z > v.w ? v.z : v.w; return a > b ? a : b; }
DEV u32 umax(u32 a, u32 b) { return a > b ? a : b; }
DEV bool anysent(u32 m) { return __builtin_amdgcn_ballot_w64(m == 0xFFFFFFFFu) != 0ull; }
DEV u4 fold4(f4 d0, f4 d1, f4 d2, f4 d3) {
    float a0 = fold32(d0[0], d2[0]), a1 = fold32(d0[1], d2[1]), a2 = fold32(d0[2], d2[2]), a3 = fold32(d0[3], d2[3]);
    float b0 = fold32(d1[0], d3[0]), b1 = fold32(d1[1], d3[1]), b2 = fold32(d1[2], d3[2]), b3 = fold32(d1[3], d3[3]);
    u32 x0 = pk(a0, a1), x1 = pk(b0, b1), x2 = pk(a2, a3), x3 = pk(b2, b3);
    sw16u(x0, x1); sw16u(x2, x3);
    return (u4){x0, x1, x2, x3};
}
DEV int wait_flags(rsrc_t ws, u32 voff, bool use, int target, int& budget, int slp) {
    int v;
    while (true) {
        MEMBAR();
        v = (int)ld1c(ws, voff, 0);
        bool bad = use && v < target;
        if (__builtin_amdgcn_ballot_w64(bad) == 0ull) break;
        if (--budget < 0) break;
        if (slp == 1) __builtin_amdgcn_s_sleep(2); else __builtin_amdgcn_s_sleep(8);
    }
    MEMBAR();
    return v;
}
#define STAMP(role, s, k)

DEV u32 beat_off(int lane) { return F_H + ((blockIdx.x & 7) * 4 + (lane & 3)) * 128; }
DEV u32 beat0(const Params& p) { MEMBAR(); u32 v = ld1c(mkrs(p.ws, WS_END), beat_off(fresh_lane()), 0); MEMBAR(); return v; }
DEV void wait_beat(rsrc_t ws, int lane, u32 h0, int& budget) {
    while (true) {
        MEMBAR();
        const u32 v = ld1c(ws, beat_off(lane), 0);
        if (__builtin_amdgcn_ballot_w64(v == h0) == 0ull || --budget < 0) break;
        __builtin_amdgcn_s_sleep(8);
    }
    MEMBAR();
}

#define KQ 0
#define W0T(k)
#define W0CL
#define W0END
#define W0DBG
#define W0_ASM0 \
    "s_sub_u32 s44, %[sa], 262144\n\ts_mov_b32 s49, 3670016\n\ts_mov_b32 s48, 0\n\ts_add_u32 s45, %[kq], 0\n\ts_and_b32 s45, s45, 3\n\ts_sub_u32 s44, s44, s48\n\t" \
    "s_lshl_b32 s48, s45, 7\n\ts_add_u32 s44, s44, s48\n\ts_mul_i32 s46, s45, 128\n\ts_add_u32 s46, s46, %[sb]\n\ts_add_u32 m0, %[lm], 57344\n\ts_add_u32 s47, s46, 0\n\t" \
    "buffer_load_dwordx4 %[vb0], %[rb], s47 offen lds\n\ts_add_u32 m0, m0, 4096\n\ts_add_u32 s47, s46, 262144\n\tbuffer_load_dwordx4 %[vb0], %[rb], s47 offen lds\n\t" \
    "s_add_u32 m0, m0, 4096\n\ts_add_u32 s47, s46, 524288\n\tbuffer_load_dwordx4 %[vb0], %[rb], s47 offen lds\n\ts_add_u32 m0, m0, 4096\n\ts_add_u32 s47, s46, 786432\n\t" \
    "buffer_load_dwordx4 %[vb0], %[rb], s47 offen lds\n\ts_add_u32 m0, %[lm], 0\n\ts_add_u32 s44, s44, 262144\n\ts_cmp_ge_u32 s44, s49\n\ts_cselect_b32 s47, s49, 0\n\t" \
    "s_sub_u32 s44, s44, s47\n\tbuffer_load_dwordx4 %[va], %[ra], s44 offen lds\n\ts_add_u32 m0, m0, 4096\n\ts_add_u32 s44, s44, 262144\n\ts_cmp_ge_u32 s44, s49\n\t" \
    "s_cselect_b32 s47, s49, 0\n\ts_sub_u32 s44, s44, s47\n\tbuffer_load_dwordx4 %[va], %[ra], s44 offen lds\n\ts_add_u32 m0, m0, 4096\n\ts_add_u32 s44, s44, 262144\n\t" \
    "s_cmp_ge_u32 s44, s49\n\ts_cselect_b32 s47, s49, 0\n\ts_sub_u32 s44, s44, s47\n\tbuffer_load_dwordx4 %[va], %[ra], s44 offen lds\n\ts_add_u32 m0, m0, 4096\n\t" \
    "s_add_u32 s44, s44, 262144\n\ts_cmp_ge_u32 s44, s49\n\ts_cselect_b32 s47, s49, 0\n\ts_sub_u32 s44, s44, s47\n\tbuffer_load_dwordx4 %[va], %[ra], s44 offen lds\n\t" \
    "s_add_u32 m0, m0, 4096\n\ts_add_u32 s44, s44, 262144\n\ts_cmp_ge_u32 s44, s49\n\ts_cselect_b32 s47, s49, 0\n\ts_sub_u32 s44, s44, s47\n\t" \
    "buffer_load_dwordx4 %[va], %[ra], s44 offen lds\n\ts_add_u32 m0, m0, 4096\n\ts_add_u32 s44, s44, 262144\n\ts_cmp_ge_u32 s44, s49\n\ts_cselect_b32 s47, s49, 0\n\t" \
    "s_sub_u32 s44, s44, s47\n\tbuffer_load_dwordx4 %[va], %[ra], s44 offen lds\n\ts_add_u32 m0, m0, 4096\n\ts_add_u32 s44, s44, 262144\n\ts_cmp_ge_u32 s44, s49\n\t" \
    "s_cselect_b32 s47, s49, 0\n\ts_sub_u32 s44, s44, s47\n\tbuffer_load_dwordx4 %[va], %[ra], s44 offen lds\n\ts_add_u32 m0, m0, 4096\n\ts_add_u32 s44, s44, 262144\n\t" \
    "s_cmp_ge_u32 s44, s49\n\ts_cselect_b32 s47, s49, 0\n\ts_sub_u32 s44, s44, s47\n\tbuffer_load_dwordx4 %[va], %[ra], s44 offen lds\n\ts_add_u32 m0, m0, 4096\n\t" \
    "s_add_u32 s44, s44, 262144\n\ts_cmp_ge_u32 s44, s49\n\ts_cselect_b32 s47, s49, 0\n\ts_sub_u32 s44, s44, s47\n\tbuffer_load_dwordx4 %[va], %[ra], s44 offen lds\n\t" \
    "s_add_u32 m0, m0, 4096\n\ts_add_u32 s44, s44, 262144\n\ts_cmp_ge_u32 s44, s49\n\ts_cselect_b32 s47, s49, 0\n\ts_sub_u32 s44, s44, s47\n\t" \
    "buffer_load_dwordx4 %[va], %[ra], s44 offen lds\n\ts_add_u32 m0, m0, 4096\n\ts_add_u32 s44, s44, 262144\n\ts_cmp_ge_u32 s44, s49\n\ts_cselect_b32 s47, s49, 0\n\t" \
    "s_sub_u32 s44, s44, s47\n\tbuffer_load_dwordx4 %[va], %[ra], s44 offen lds\n\ts_add_u32 m0, m0, 4096\n\ts_add_u32 s44, s44, 262144\n\ts_cmp_ge_u32 s44, s49\n\t" \
    "s_cselect_b32 s47, s49, 0\n\ts_sub_u32 s44, s44, s47\n\tbuffer_load_dwordx4 %[va], %[ra], s44 offen lds\n\ts_add_u32 m0, m0, 4096\n\ts_add_u32 s44, s44, 262144\n\t" \
    "s_cmp_ge_u32 s44, s49\n\ts_cselect_b32 s47, s49, 0\n\ts_sub_u32 s44, s44, s47\n\tbuffer_load_dwordx4 %[va], %[ra], s44 offen lds\n\ts_add_u32 m0, m0, 4096\n\t" \
    "s_add_u32 s44, s44, 262144\n\ts_cmp_ge_u32 s44, s49\n\ts_cselect_b32 s47, s49, 0\n\ts_sub_u32 s44, s44, s47\n\tbuffer_load_dwordx4 %[va], %[ra], s44 offen lds\n\t" \
    "s_add_u32 s45, %[kq], 1\n\ts_and_b32 s45, s45, 3\n\ts_sub_u32 s44, s44, s48\n\ts_lshl_b32 s48, s45, 7\n\ts_add_u32 s44, s44, s48\n\ts_mul_i32 s46, s45, 128\n\t" \
    "s_add_u32 s46, s46, %[sb]\n\ts_add_u32 m0, %[lm], 131072\n\ts_add_u32 s47, s46, 0\n\tbuffer_load_dwordx4 %[vb0], %[rb], s47 offen lds\n\ts_add_u32 m0, m0, 4096\n\t" \
    "s_add_u32 s47, s46, 262144\n\tbuffer_load_dwordx4 %[vb0], %[rb], s47 offen lds\n\ts_add_u32 m0, m0, 4096\n\ts_add_u32 s47, s46, 524288\n\t" \
    "buffer_load_dwordx4 %[vb0], %[rb], s47 offen lds\n\ts_add_u32 m0, m0, 4096\n\ts_add_u32 s47, s46, 786432\n\tbuffer_load_dwordx4 %[vb0], %[rb], s47 offen lds\n\t" \
    "s_add_u32 m0, %[lm], 73728\n\ts_add_u32 s44, s44, 262144\n\ts_cmp_ge_u32 s44, s49\n\ts_cselect_b32 s47, s49, 0\n\ts_sub_u32 s44, s44, s47\n\t" \
    "buffer_load_dwordx4 %[va], %[ra], s44 offen lds\n\ts_add_u32 m0, m0, 4096\n\ts_add_u32 s44, s44, 262144\n\ts_cmp_ge_u32 s44, s49\n\ts_cselect_b32 s47, s49, 0\n\t" \
    "s_sub_u32 s44, s44, s47\n\tbuffer_load_dwordx4 %[va], %[ra], s44 offen lds\n\ts_add_u32 m0, m0, 4096\n\ts_add_u32 s44, s44, 262144\n\ts_cmp_ge_u32 s44, s49\n\t" \
    "s_cselect_b32 s47, s49, 0\n\ts_sub_u32 s44, s44, s47\n\tbuffer_load_dwordx4 %[va], %[ra], s44 offen lds\n\ts_add_u32 m0, m0, 4096\n\ts_add_u32 s44, s44, 262144\n\t" \
    "s_cmp_ge_u32 s44, s49\n\ts_cselect_b32 s47, s49, 0\n\ts_sub_u32 s44, s44, s47\n\tbuffer_load_dwordx4 %[va], %[ra], s44 offen lds\n\ts_add_u32 m0, m0, 4096\n\t" \
    "s_add_u32 s44, s44, 262144\n\ts_cmp_ge_u32 s44, s49\n\ts_cselect_b32 s47, s49, 0\n\ts_sub_u32 s44, s44, s47\n\tbuffer_load_dwordx4 %[va], %[ra], s44 offen lds\n\t" \
    "s_add_u32 m0, m0, 4096\n\ts_add_u32 s44, s44, 262144\n\ts_cmp_ge_u32 s44, s49\n\ts_cselect_b32 s47, s49, 0\n\ts_sub_u32 s44, s44, s47\n\t" \
    "buffer_load_dwordx4 %[va], %[ra], s44 offen lds\n\ts_add_u32 m0, m0, 4096\n\ts_add_u32 s44, s44, 262144\n\ts_cmp_ge_u32 s44, s49\n\ts_cselect_b32 s47, s49, 0\n\t" \
    "s_sub_u32 s44, s44, s47\n\tbuffer_load_dwordx4 %[va], %[ra], s44 offen lds\n\ts_add_u32 m0, m0, 4096\n\ts_add_u32 s44, s44, 262144\n\ts_cmp_ge_u32 s44, s49\n\t" \
    "s_cselect_b32 s47, s49, 0\n\ts_sub_u32 s44, s44, s47\n\tbuffer_load_dwordx4 %[va], %[ra], s44 offen lds\n\ts_add_u32 m0, m0, 4096\n\ts_add_u32 s44, s44, 262144\n\t" \
    "s_cmp_ge_u32 s44, s49\n\ts_cselect_b32 s47, s49, 0\n\ts_sub_u32 s44, s44, s47\n\tbuffer_load_dwordx4 %[va], %[ra], s44 offen lds\n\ts_add_u32 m0, m0, 4096\n\t" \
    "s_add_u32 s44, s44, 262144\n\ts_cmp_ge_u32 s44, s49\n\ts_cselect_b32 s47, s49, 0\n\ts_sub_u32 s44, s44, s47\n\tbuffer_load_dwordx4 %[va], %[ra], s44 offen lds\n\t" \
    "s_add_u32 m0, m0, 4096\n\ts_add_u32 s44, s44, 262144\n\ts_cmp_ge_u32 s44, s49\n\ts_cselect_b32 s47, s49, 0\n\ts_sub_u32 s44, s44, s47\n\t" \
    "buffer_load_dwordx4 %[va], %[ra], s44 offen lds\n\ts_add_u32 m0, m0, 4096\n\ts_add_u32 s44, s44, 262144\n\ts_cmp_ge_u32 s44, s49\n\ts_cselect_b32 s47, s49, 0\n\t" \
    "s_sub_u32 s44, s44, s47\n\tbuffer_load_dwordx4 %[va], %[ra], s44 offen lds\n\ts_add_u32 m0, m0, 4096\n\ts_add_u32 s44, s44, 262144\n\ts_cmp_ge_u32 s44, s49\n\t" \
    "s_cselect_b32 s47, s49, 0\n\ts_sub_u32 s44, s44, s47\n\tbuffer_load_dwordx4 %[va], %[ra], s44 offen lds\n\ts_add_u32 m0, m0, 4096\n\ts_add_u32 s44, s44, 262144\n\t" \
    "s_cmp_ge_u32 s44, s49\n\ts_cselect_b32 s47, s49, 0\n\ts_sub_u32 s44, s44, s47\n\tbuffer_load_dwordx4 %[va], %[ra], s44 offen lds\n\tv_add_u32 v26, 73728, %[r0]\n\t" \
    "v_add_u32 v27, 73728, %[r1]\n\tv_add_u32 v28, 73728, %[q0]\n\tv_add_u32 v29, 73728, %[q1]\n\ts_mov_b32 s54, 0x3db8aa3b\n\ts_mov_b32 s55, 0x3db8aa3b\n\t" W0T(0) "" \
    "s_waitcnt vmcnt(18)\n\ts_barrier\n\t" W0T(1) "ds_read_b128 a[0:3], %[r0] offset:0\n\tds_read_b128 a[8:11], %[r0] offset:2048\n\tds_read_b128 a[112:115], %[q0] offset:0\n\t" \
    "ds_read_b128 a[116:119], %[q0] offset:2048\n\tds_read_b128 a[120:123], %[q0] offset:4096\n\tds_read_b128 a[124:127], %[q0] offset:6144\n\tds_read_b128 a[4:7], %[r1] offset:0\n\t" \
    "ds_read_b128 a[12:15], %[r1] offset:2048\n\tds_read_b128 a[128:131], %[q1] offset:0\n\tds_read_b128 a[132:135], %[q1] offset:2048\n\tds_read_b128 a[136:139], %[q1] offset:4096\n\t" \
    "ds_read_b128 a[140:143], %[q1] offset:6144\n\ts_waitcnt lgkmcnt(6)\n\tv_mfma_f32_16x16x32_bf16 v[32:35], a[0:3], a[112:115], 0\n\tds_read_b128 a[16:19], %[r0] offset:4096\n\t" \
    "v_mfma_f32_16x16x32_bf16 v[36:39], a[0:3], a[116:119], 0\n\tds_read_b128 a[24:27], %[r0] offset:6144\n\tv_mfma_f32_16x16x32_bf16 v[40:43], a[0:3], a[120:123], 0\n\t" \
    "ds_read_b128 a[20:23], %[r1] offset:4096\n\tv_mfma_f32_16x16x32_bf16 v[44:47], a[0:3], a[124:127], 0\n\tds_read_b128 a[28:31], %[r1] offset:6144\n\t" \
    "v_mfma_f32_16x16x32_bf16 v[48:51], a[8:11], a[112:115], 0\n\tds_read_b128 a[32:35], %[r0] offset:8192\n\tv_mfma_f32_16x16x32_bf16 v[52:55], a[8:11], a[116:119], 0\n\t" \
    "ds_read_b128 a[40:43], %[r0] offset:10240\n\tv_mfma_f32_16x16x32_bf16 v[56:59], a[8:11], a[120:123], 0\n\tds_read_b128 a[36:39], %[r1] offset:8192\n\t" \
    "v_mfma_f32_16x16x32_bf16 v[60:63], a[8:11], a[124:127], 0\n\tds_read_b128 a[44:47], %[r1] offset:10240\n\ts_waitcnt lgkmcnt(8)\n\t" \
    "v_mfma_f32_16x16x32_bf16 v[32:35], a[4:7], a[128:131], v[32:35]\n\tds_read_b128 a[48:51], %[r0] offset:12288\n\tv_mfma_f32_16x16x32_bf16 v[36:39], a[4:7], a[132:135], v[36:39]\n\t" \
    "ds_read_b128 a[56:59], %[r0] offset:14336\n\tv_mfma_f32_16x16x32_bf16 v[40:43], a[4:7], a[136:139], v[40:43]\n\tds_read_b128 a[52:55], %[r1] offset:12288\n\t" \
    "v_mfma_f32_16x16x32_bf16 v[44:47], a[4:7], a[140:143], v[44:47]\n\tds_read_b128 a[60:63], %[r1] offset:14336\n\t" \
    "v_mfma_f32_16x16x32_bf16 v[48:51], a[12:15], a[128:131], v[48:51]\n\tds_read_b128 a[64:67], %[r0] offset:16384\n\t" \
    "v_mfma_f32_16x16x32_bf16 v[52:55], a[12:15], a[132:135], v[52:55]\n\tds_read_b128 a[72:75], %[r0] offset:18432\n\t" \
    "v_mfma_f32_16x16x32_bf16 v[56:59], a[12:15], a[136:139], v[56:59]\n\tds_read_b128 a[68:71], %[r1] offset:16384\n\t" \
    "v_mfma_f32_16x16x32_bf16 v[60:63], a[12:15], a[140:143], v[60:63]\n\tds_read_b128 a[76:79], %[r1] offset:18432\n\ts_waitcnt lgkmcnt(12)\n\t" \
    "v_mfma_f32_16x16x32_bf16 v[64:67], a[16:19], a[112:115], 0\n\tds_read_b128 a[80:83], %[r0] offset:20480\n\tv_mfma_f32_16x16x32_bf16 v[68:71], a[16:19], a[116:119], 0\n\t" \
    "ds_read_b128 a[88:91], %[r0] offset:22528\n\tv_mfma_f32_16x16x32_bf16 v[72:75], a[16:19], a[120:123], 0\n\tds_read_b128 a[84:87], %[r1] offset:20480\n\t" \
    "v_mfma_f32_16x16x32_bf16 v[76:79], a[16:19], a[124:127], 0\n\tds_read_b128 a[92:95], %[r1] offset:22528\n\tv_mfma_f32_16x16x32_bf16 v[80:83], a[24:27], a[112:115], 0\n\t" \
    "ds_read_b128 a[96:99], %[r0] offset:24576\n\tv_mfma_f32_16x16x32_bf16 v[84:87], a[24:27], a[116:119], 0\n\tds_read_b128 a[104:107], %[r0] offset:26624\n\t" \
    "v_mfma_f32_16x16x32_bf16 v[88:91], a[24:27], a[120:123], 0\n\tds_read_b128 a[100:103], %[r1] offset:24576\n\tv_mfma_f32_16x16x32_bf16 v[92:95], a[24:27], a[124:127], 0\n\t" \
    "ds_read_b128 a[108:111], %[r1] offset:26624\n\ts_waitcnt lgkmcnt(12)\n\tv_mfma_f32_16x16x32_bf16 v[64:67], a[20:23], a[128:131], v[64:67]\n\t" \
    "v_mfma_f32_16x16x32_bf16 v[68:71], a[20:23], a[132:135], v[68:71]\n\tv_mfma_f32_16x16x32_bf16 v[72:75], a[20:23], a[136:139], v[72:75]\n\t" \
    "v_mfma_f32_16x16x32_bf16 v[76:79], a[20:23], a[140:143], v[76:79]\n\tv_mfma_f32_16x16x32_bf16 v[80:83], a[28:31], a[128:131], v[80:83]\n\t" \
    "v_mfma_f32_16x16x32_bf16 v[84:87], a[28:31], a[132:135], v[84:87]\n\tv_mfma_f32_16x16x32_bf16 v[88:91], a[28:31], a[136:139], v[88:91]\n\t" \
    "v_mfma_f32_16x16x32_bf16 v[92:95], a[28:31], a[140:143], v[92:95]\n\ts_waitcnt lgkmcnt(0)\n\ts_barrier\n\tv_mfma_f32_16x16x32_bf16 v[96:99], a[32:35], a[112:115], 0\n\t" \
    "buffer_load_dword v30, %[bo], %[ws], 0 offen sc1\n\tv_mfma_f32_16x16x32_bf16 v[100:103], a[32:35], a[116:119], 0\n\tv_mfma_f32_16x16x32_bf16 v[104:107], a[32:35], a[120:123], 0\n\t" \
    "v_mfma_f32_16x16x32_bf16 v[108:111], a[32:35], a[124:127], 0\n\ts_add_u32 s45, %[kq], 2\n\ts_and_b32 s45, s45, 3\n\ts_sub_u32 s44, s44, s48\n\ts_lshl_b32 s48, s45, 7\n\t" \
    "s_add_u32 s44, s44, s48\n\ts_mul_i32 s46, s45, 128\n\ts_add_u32 s46, s46, %[sb]\n\ts_add_u32 m0, %[lm], 57344\n\ts_add_u32 s47, s46, 0\n\t" \
    "buffer_load_dwordx4 %[vb0], %[rb], s47 offen lds\n\tv_mfma_f32_16x16x32_bf16 v[112:115], a[40:43], a[112:115], 0\n\tv_mfma_f32_16x16x32_bf16 v[116:119], a[40:43], a[116:119], 0\n\t" \
    "v_mfma_f32_16x16x32_bf16 v[120:123], a[40:43], a[120:123], 0\n\ts_add_u32 m0, m0, 4096\n\ts_add_u32 s47, s46, 262144\n\tbuffer_load_dwordx4 %[vb0], %[rb], s47 offen lds\n\t" \
    "v_mfma_f32_16x16x32_bf16 v[124:127], a[40:43], a[124:127], 0\n\tv_mfma_f32_16x16x32_bf16 v[96:99], a[36:39], a[128:131], v[96:99]\n\t" \
    "v_mfma_f32_16x16x32_bf16 v[100:103], a[36:39], a[132:135], v[100:103]\n\ts_add_u32 m0, m0, 4096\n\ts_add_u32 s47, s46, 524288\n\t" \
    "buffer_load_dwordx4 %[vb0], %[rb], s47 offen lds\n\tv_mfma_f32_16x16x32_bf16 v[104:107], a[36:39], a[136:139], v[104:107]\n\t" \
    "v_mfma_f32_16x16x32_bf16 v[108:111], a[36:39], a[140:143], v[108:111]\n\tv_mfma_f32_16x16x32_bf16 v[112:115], a[44:47], a[128:131], v[112:115]\n\ts_add_u32 m0, m0, 4096\n\t" \
    "s_add_u32 s47, s46, 786432\n\tbuffer_load_dwordx4 %[vb0], %[rb], s47 offen lds\n\tv_mfma_f32_16x16x32_bf16 v[116:119], a[44:47], a[132:135], v[116:119]\n\t" \
    "v_mfma_f32_16x16x32_bf16 v[120:123], a[44:47], a[136:139], v[120:123]\n\tv_mfma_f32_16x16x32_bf16 v[124:127], a[44:47], a[140:143], v[124:127]\n\ts_add_u32 m0, %[lm], 0\n\t" \
    "s_add_u32 s44, s44, 262144\n\ts_cmp_ge_u32 s44, s49\n\ts_cselect_b32 s47, s49, 0\n\ts_sub_u32 s44, s44, s47\n\tbuffer_load_dwordx4 %[va], %[ra], s44 offen lds\n\t" \
    "v_mfma_f32_16x16x32_bf16 v[128:131], a[48:51], a[112:115], 0\n\tv_mfma_f32_16x16x32_bf16 v[132:135], a[48:51], a[116:119], 0\n\t" \
    "v_mfma_f32_16x16x32_bf16 v[136:139], a[48:51], a[120:123], 0\n\ts_add_u32 m0, m0, 4096\n\ts_add_u32 s44, s44, 262144\n\ts_cmp_ge_u32 s44, s49\n\ts_cselect_b32 s47, s49, 0\n\t" \
    "s_sub_u32 s44, s44, s47\n\tbuffer_load_dwordx4 %[va], %[ra], s44 offen lds\n\tv_mfma_f32_16x16x32_bf16 v[140:143], a[48:51], a[124:127], 0\n\t" \
    "v_mfma_f32_16x16x32_bf16 v[144:147], a[56:59], a[112:115], 0\n\tv_mfma_f32_16x16x32_bf16 v[148:151], a[56:59], a[116:119], 0\n\ts_add_u32 m0, m0, 4096\n\t" \
    "s_add_u32 s44, s44, 262144\n\ts_cmp_ge_u32 s44, s49\n\ts_cselect_b32 s47, s49, 0\n\ts_sub_u32 s44, s44, s47\n\tbuffer_load_dwordx4 %[va], %[ra], s44 offen lds\n\t" \
    "v_mfma_f32_16x16x32_bf16 v[152:155], a[56:59], a[120:123], 0\n\tv_mfma_f32_16x16x32_bf16 v[156:159], a[56:59], a[124:127], 0\n\t" \
    "v_mfma_f32_16x16x32_bf16 v[128:131], a[52:55], a[128:131], v[128:131]\n\ts_add_u32 m0, m0, 4096\n\ts_add_u32 s44, s44, 262144\n\ts_cmp_ge_u32 s44, s49\n\t" \
    "s_cselect_b32 s47, s49, 0\n\ts_sub_u32 s44, s44, s47\n\tbuffer_load_dwordx4 %[va], %[ra], s44 offen lds\n\tv_mfma_f32_16x16x32_bf16 v[132:135], a[52:55], a[132:135], v[132:135]\n\t" \
    "v_mfma_f32_16x16x32_bf16 v[136:139], a[52:55], a[136:139], v[136:139]\n\tv_mfma_f32_16x16x32_bf16 v[140:143], a[52:55], a[140:143], v[140:143]\n\ts_add_u32 m0, m0, 4096\n\t" \
    "s_add_u32 s44, s44, 262144\n\ts_cmp_ge_u32 s44, s49\n\ts_cselect_b32 s47, s49, 0\n\ts_sub_u32 s44, s44, s47\n\tbuffer_load_dwordx4 %[va], %[ra], s44 offen lds\n\t" \
    "v_mfma_f32_16x16x32_bf16 v[144:147], a[60:63], a[128:131], v[144:147]\n\tv_mfma_f32_16x16x32_bf16 v[148:151], a[60:63], a[132:135], v[148:151]\n\t" \
    "v_mfma_f32_16x16x32_bf16 v[152:155], a[60:63], a[136:139], v[152:155]\n\ts_add_u32 m0, m0, 4096\n\ts_add_u32 s44, s44, 262144\n\ts_cmp_ge_u32 s44, s49\n\t" \
    "s_cselect_b32 s47, s49, 0\n\ts_sub_u32 s44, s44, s47\n\tbuffer_load_dwordx4 %[va], %[ra], s44 offen lds\n\tv_mfma_f32_16x16x32_bf16 v[156:159], a[60:63], a[140:143], v[156:159]\n\t" \
    "v_mfma_f32_16x16x32_bf16 v[160:163], a[64:67], a[112:115], 0\n\tv_mfma_f32_16x16x32_bf16 v[164:167], a[64:67], a[116:119], 0\n\ts_add_u32 m0, m0, 4096\n\t" \
    "s_add_u32 s44, s44, 262144\n\ts_cmp_ge_u32 s44, s49\n\ts_cselect_b32 s47, s49, 0\n\ts_sub_u32 s44, s44, s47\n\tbuffer_load_dwordx4 %[va], %[ra], s44 offen lds\n\t" \
    "v_mfma_f32_16x16x32_bf16 v[168:171], a[64:67], a[120:123], 0\n\tv_mfma_f32_16x16x32_bf16 v[172:175], a[64:67], a[124:127], 0\n\t" \
    "v_mfma_f32_16x16x32_bf16 v[176:179], a[72:75], a[112:115], 0\n\ts_add_u32 m0, m0, 4096\n\ts_add_u32 s44, s44, 262144\n\ts_cmp_ge_u32 s44, s49\n\ts_cselect_b32 s47, s49, 0\n\t" \
    "s_sub_u32 s44, s44, s47\n\tbuffer_load_dwordx4 %[va], %[ra], s44 offen lds\n\tv_mfma_f32_16x16x32_bf16 v[180:183], a[72:75], a[116:119], 0\n\t" \
    "v_mfma_f32_16x16x32_bf16 v[184:187], a[72:75], a[120:123], 0\n\tv_mfma_f32_16x16x32_bf16 v[188:191], a[72:75], a[124:127], 0\n\ts_add_u32 m0, m0, 4096\n\t" \
    "s_add_u32 s44, s44, 262144\n\ts_cmp_ge_u32 s44, s49\n\ts_cselect_b32 s47, s49, 0\n\ts_sub_u32 s44, s44, s47\n\tbuffer_load_dwordx4 %[va], %[ra], s44 offen lds\n\t" \
    "v_mfma_f32_16x16x32_bf16 v[160:163], a[68:71], a[128:131], v[160:163]\n\tv_mfma_f32_16x16x32_bf16 v[164:167], a[68:71], a[132:135], v[164:167]\n\t" \
    "v_mfma_f32_16x16x32_bf16 v[168:171], a[68:71], a[136:139], v[168:171]\n\ts_add_u32 m0, m0, 4096\n\ts_add_u32 s44, s44, 262144\n\ts_cmp_ge_u32 s44, s49\n\t" \
    "s_cselect_b32 s47, s49, 0\n\ts_sub_u32 s44, s44, s47\n\tbuffer_load_dwordx4 %[va], %[ra], s44 offen lds\n\tv_mfma_f32_16x16x32_bf16 v[172:175], a[68:71], a[140:143], v[172:175]\n\t" \
    "v_mfma_f32_16x16x32_bf16 v[176:179], a[76:79], a[128:131], v[176:179]\n\tv_mfma_f32_16x16x32_bf16 v[180:183], a[76:79], a[132:135], v[180:183]\n\ts_add_u32 m0, m0, 4096\n\t" \
    "s_add_u32 s44, s44, 262144\n\ts_cmp_ge_u32 s44, s49\n\ts_cselect_b32 s47, s49, 0\n\ts_sub_u32 s44, s44, s47\n\tbuffer_load_dwordx4 %[va], %[ra], s44 offen lds\n\t" \
    "v_mfma_f32_16x16x32_bf16 v[184:187], a[76:79], a[136:139], v[184:187]\n\tv_mfma_f32_16x16x32_bf16 v[188:191], a[76:79], a[140:143], v[188:191]\n\t" \
    "v_mfma_f32_16x16x32_bf16 v[192:195], a[80:83], a[112:115], 0\n\ts_add_u32 m0, m0, 4096\n\ts_add_u32 s44, s44, 262144\n\ts_cmp_ge_u32 s44, s49\n\ts_cselect_b32 s47, s49, 0\n\t" \
    "s_sub_u32 s44, s44, s47\n\tbuffer_load_dwordx4 %[va], %[ra], s44 offen lds\n\tv_mfma_f32_16x16x32_bf16 v[196:199], a[80:83], a[116:119], 0\n\t" \
    "v_mfma_f32_16x16x32_bf16 v[200:203], a[80:83], a[120:123], 0\n\tv_mfma_f32_16x16x32_bf16 v[204:207], a[80:83], a[124:127], 0\n\ts_add_u32 m0, m0, 4096\n\t" \
    "s_add_u32 s44, s44, 262144\n\ts_cmp_ge_u32 s44, s49\n\ts_cselect_b32 s47, s49, 0\n\ts_sub_u32 s44, s44, s47\n\tbuffer_load_dwordx4 %[va], %[ra], s44 offen lds\n\t" \
    "v_mfma_f32_16x16x32_bf16 v[208:211], a[88:91], a[112:115], 0\n\tv_mfma_f32_16x16x32_bf16 v[212:215], a[88:91], a[116:119], 0\n\t" \
    "v_mfma_f32_16x16x32_bf16 v[216:219], a[88:91], a[120:123], 0\n\ts_add_u32 m0, m0, 4096\n\ts_add_u32 s44, s44, 262144\n\ts_cmp_ge_u32 s44, s49\n\ts_cselect_b32 s47, s49, 0\n\t" \
    "s_sub_u32 s44, s44, s47\n\tbuffer_load_dwordx4 %[va], %[ra], s44 offen lds\n\tv_mfma_f32_16x16x32_bf16 v[220:223], a[88:91], a[124:127], 0\n\t" \
    "v_mfma_f32_16x16x32_bf16 v[192:195], a[84:87], a[128:131], v[192:195]\n\tv_mfma_f32_16x16x32_bf16 v[196:199], a[84:87], a[132:135], v[196:199]\n\t" \
    "v_mfma_f32_16x16x32_bf16 v[200:203], a[84:87], a[136:139], v[200:203]\n\tv_mfma_f32_16x16x32_bf16 v[204:207], a[84:87], a[140:143], v[204:207]\n\t" \
    "v_mfma_f32_16x16x32_bf16 v[208:211], a[92:95], a[128:131], v[208:211]\n\tv_mfma_f32_16x16x32_bf16 v[212:215], a[92:95], a[132:135], v[212:215]\n\t" \
    "v_mfma_f32_16x16x32_bf16 v[216:219], a[92:95], a[136:139], v[216:219]\n\tv_mfma_f32_16x16x32_bf16 v[220:223], a[92:95], a[140:143], v[220:223]\n\t" \
    "v_mfma_f32_16x16x32_bf16 v[224:227], a[96:99], a[112:115], 0\n\tv_mfma_f32_16x16x32_bf16 v[228:231], a[96:99], a[116:119], 0\n\t" \
    "v_mfma_f32_16x16x32_bf16 v[232:235], a[96:99], a[120:123], 0\n\tv_mfma_f32_16x16x32_bf16 v[236:239], a[96:99], a[124:127], 0\n\t" \
    "v_mfma_f32_16x16x32_bf16 v[240:243], a[104:107], a[112:115], 0\n\tv_mfma_f32_16x16x32_bf16 v[244:247], a[104:107], a[116:119], 0\n\t" \
    "v_mfma_f32_16x16x32_bf16 v[248:251], a[104:107], a[120:123], 0\n\tv_mfma_f32_16x16x32_bf16 v[252:255], a[104:107], a[124:127], 0\n\t" \
    "v_mfma_f32_16x16x32_bf16 v[224:227], a[100:103], a[128:131], v[224:227]\n\tv_mfma_f32_16x16x32_bf16 v[228:231], a[100:103], a[132:135], v[228:231]\n\t" \
    "v_mfma_f32_16x16x32_bf16 v[232:235], a[100:103], a[136:139], v[232:235]\n\tv_mfma_f32_16x16x32_bf16 v[236:239], a[100:103], a[140:143], v[236:239]\n\t" \
    "v_mfma_f32_16x16x32_bf16 v[240:243], a[108:111], a[128:131], v[240:243]\n\tv_mfma_f32_16x16x32_bf16 v[244:247], a[108:111], a[132:135], v[244:247]\n\t" \
    "v_mfma_f32_16x16x32_bf16 v[248:251], a[108:111], a[136:139], v[248:251]\n\tv_mfma_f32_16x16x32_bf16 v[252:255], a[108:111], a[140:143], v[252:255]\n\t" W0T(2) "" \
    "s_waitcnt vmcnt(18)\n\ts_barrier\n\t" W0T(3) "ds_read_b128 a[0:3], v26 offset:0\n\tds_read_b128 a[8:11], v26 offset:2048\n\tds_read_b128 a[112:115], v28 offset:0\n\t" \
    "ds_read_b128 a[116:119], v28 offset:2048\n\tds_read_b128 a[120:123], v28 offset:4096\n\tds_read_b128 a[124:127], v28 offset:6144\n\tds_read_b128 a[4:7], v27 offset:0\n\t" \
    "ds_read_b128 a[12:15], v27 offset:2048\n\tds_read_b128 a[128:131], v29 offset:0\n\tds_read_b128 a[132:135], v29 offset:2048\n\tds_read_b128 a[136:139], v29 offset:4096\n\t" \
    "ds_read_b128 a[140:143], v29 offset:6144\n\ts_waitcnt lgkmcnt(6)\n\tv_mfma_f32_16x16x32_bf16 v[32:35], a[0:3], a[112:115], v[32:35]\n\tds_read_b128 a[16:19], v26 offset:4096\n\t" \
    "v_mfma_f32_16x16x32_bf16 v[36:39], a[0:3], a[116:119], v[36:39]\n\tds_read_b128 a[24:27], v26 offset:6144\n\tv_mfma_f32_16x16x32_bf16 v[40:43], a[0:3], a[120:123], v[40:43]\n\t" \
    "ds_read_b128 a[20:23], v27 offset:4096\n\tv_mfma_f32_16x16x32_bf16 v[44:47], a[0:3], a[124:127], v[44:47]\n\tds_read_b128 a[28:31], v27 offset:6144\n\t" \
    "v_mfma_f32_16x16x32_bf16 v[48:51], a[8:11], a[112:115], v[48:51]\n\tds_read_b128 a[32:35], v26 offset:8192\n\tv_mfma_f32_16x16x32_bf16 v[52:55], a[8:11], a[116:119], v[52:55]\n\t" \
    "ds_read_b128 a[40:43], v26 offset:10240\n\tv_mfma_f32_16x16x32_bf16 v[56:59], a[8:11], a[120:123], v[56:59]\n\tds_read_b128 a[36:39], v27 offset:8192\n\t" \
    "v_mfma_f32_16x16x32_bf16 v[60:63], a[8:11], a[124:127], v[60:63]\n\tds_read_b128 a[44:47], v27 offset:10240\n\ts_waitcnt lgkmcnt(8)\n\t" \
    "v_mfma_f32_16x16x32_bf16 v[32:35], a[4:7], a[128:131], v[32:35]\n\tds_read_b128 a[48:51], v26 offset:12288\n\tv_mfma_f32_16x16x32_bf16 v[36:39], a[4:7], a[132:135], v[36:39]\n\t" \
    "ds_read_b128 a[56:59], v26 offset:14336\n\tv_mfma_f32_16x16x32_bf16 v[40:43], a[4:7], a[136:139], v[40:43]\n\tds_read_b128 a[52:55], v27 offset:12288\n\t" \
    "v_mfma_f32_16x16x32_bf16 v[44:47], a[4:7], a[140:143], v[44:47]\n\tds_read_b128 a[60:63], v27 offset:14336\n\tv_mfma_f32_16x16x32_bf16 v[48:51], a[12:15], a[128:131], v[48:51]\n\t" \
    "ds_read_b128 a[64:67], v26 offset:16384\n\tv_mfma_f32_16x16x32_bf16 v[52:55], a[12:15], a[132:135], v[52:55]\n\tds_read_b128 a[72:75], v26 offset:18432\n\t" \
    "v_mfma_f32_16x16x32_bf16 v[56:59], a[12:15], a[136:139], v[56:59]\n\tds_read_b128 a[68:71], v27 offset:16384\n\t" \
    "v_mfma_f32_16x16x32_bf16 v[60:63], a[12:15], a[140:143], v[60:63]\n\tds_read_b128 a[76:79], v27 offset:18432\n\ts_waitcnt lgkmcnt(12)\n\t" \
    "v_mfma_f32_16x16x32_bf16 v[64:67], a[16:19], a[112:115], v[64:67]\n\tds_read_b128 a[80:83], v26 offset:20480\n\t" \
    "v_mfma_f32_16x16x32_bf16 v[68:71], a[16:19], a[116:119], v[68:71]\n\tds_read_b128 a[88:91], v26 offset:22528\n\t" \
    "v_mfma_f32_16x16x32_bf16 v[72:75], a[16:19], a[120:123], v[72:75]\n\tds_read_b128 a[84:87], v27 offset:20480\n\t" \
    "v_mfma_f32_16x16x32_bf16 v[76:79], a[16:19], a[124:127], v[76:79]\n\tds_read_b128 a[92:95], v27 offset:22528\n\t" \
    "v_mfma_f32_16x16x32_bf16 v[80:83], a[24:27], a[112:115], v[80:83]\n\tds_read_b128 a[96:99], v26 offset:24576\n\t" \
    "v_mfma_f32_16x16x32_bf16 v[84:87], a[24:27], a[116:119], v[84:87]\n\tds_read_b128 a[104:107], v26 offset:26624\n\t" \
    "v_mfma_f32_16x16x32_bf16 v[88:91], a[24:27], a[120:123], v[88:91]\n\tds_read_b128 a[100:103], v27 offset:24576\n\t" \
    "v_mfma_f32_16x16x32_bf16 v[92:95], a[24:27], a[124:127], v[92:95]\n\tds_read_b128 a[108:111], v27 offset:26624\n\ts_waitcnt lgkmcnt(12)\n\t" \
    "v_mfma_f32_16x16x32_bf16 v[64:67], a[20:23], a[128:131], v[64:67]\n\tv_mfma_f32_16x16x32_bf16 v[68:71], a[20:23], a[132:135], v[68:71]\n\t" \
    "v_mfma_f32_16x16x32_bf16 v[72:75], a[20:23], a[136:139], v[72:75]\n\tv_mfma_f32_16x16x32_bf16 v[76:79], a[20:23], a[140:143], v[76:79]\n\t" \
    "v_mfma_f32_16x16x32_bf16 v[80:83], a[28:31], a[128:131], v[80:83]\n\tv_mfma_f32_16x16x32_bf16 v[84:87], a[28:31], a[132:135], v[84:87]\n\t" \
    "v_mfma_f32_16x16x32_bf16 v[88:91], a[28:31], a[136:139], v[88:91]\n\tv_mfma_f32_16x16x32_bf16 v[92:95], a[28:31], a[140:143], v[92:95]\n\ts_waitcnt lgkmcnt(0)\n\ts_barrier\n\t" \
    "v_mfma_f32_16x16x32_bf16 v[96:99], a[32:35], a[112:115], v[96:99]\n\tv_mfma_f32_16x16x32_bf16 v[100:103], a[32:35], a[116:119], v[100:103]\n\ts_add_u32 s45, %[kq], 3\n\t" \
    "s_and_b32 s45, s45, 3\n\ts_sub_u32 s44, s44, s48\n\ts_lshl_b32 s48, s45, 7\n\ts_add_u32 s44, s44, s48\n\ts_mul_i32 s46, s45, 128\n\ts_add_u32 s46, s46, %[sb]\n\t" \
    "s_add_u32 m0, %[lm], 131072\n\ts_add_u32 s47, s46, 0\n\tbuffer_load_dwordx4 %[vb0], %[rb], s47 offen lds\n\t" \
    "v_mfma_f32_16x16x32_bf16 v[104:107], a[32:35], a[120:123], v[104:107]\n\tv_mfma_f32_16x16x32_bf16 v[108:111], a[32:35], a[124:127], v[108:111]\n\ts_add_u32 m0, m0, 4096\n\t" \
    "s_add_u32 s47, s46, 262144\n\tbuffer_load_dwordx4 %[vb0], %[rb], s47 offen lds\n\tv_mfma_f32_16x16x32_bf16 v[112:115], a[40:43], a[112:115], v[112:115]\n\t" \
    "v_mfma_f32_16x16x32_bf16 v[116:119], a[40:43], a[116:119], v[116:119]\n\ts_add_u32 m0, m0, 4096\n\ts_add_u32 s47, s46, 524288\n\t" \
    "buffer_load_dwordx4 %[vb0], %[rb], s47 offen lds\n\tv_mfma_f32_16x16x32_bf16 v[120:123], a[40:43], a[120:123], v[120:123]\n\t" \
    "v_mfma_f32_16x16x32_bf16 v[124:127], a[40:43], a[124:127], v[124:127]\n\ts_add_u32 m0, m0, 4096\n\ts_add_u32 s47, s46, 786432\n\t" \
    "buffer_load_dwordx4 %[vb0], %[rb], s47 offen lds\n\tv_mfma_f32_16x16x32_bf16 v[96:99], a[36:39], a[128:131], v[96:99]\n\t" \
    "v_mfma_f32_16x16x32_bf16 v[100:103], a[36:39], a[132:135], v[100:103]\n\ts_add_u32 m0, %[lm], 73728\n\ts_add_u32 s44, s44, 262144\n\ts_cmp_ge_u32 s44, s49\n\t" \
    "s_cselect_b32 s47, s49, 0\n\ts_sub_u32 s44, s44, s47\n\tbuffer_load_dwordx4 %[va], %[ra], s44 offen lds\n\tv_mfma_f32_16x16x32_bf16 v[104:107], a[36:39], a[136:139], v[104:107]\n\t" \
    "v_mfma_f32_16x16x32_bf16 v[108:111], a[36:39], a[140:143], v[108:111]\n\ts_add_u32 m0, m0, 4096\n\ts_add_u32 s44, s44, 262144\n\ts_cmp_ge_u32 s44, s49\n\t" \
    "s_cselect_b32 s47, s49, 0\n\ts_sub_u32 s44, s44, s47\n\tbuffer_load_dwordx4 %[va], %[ra], s44 offen lds\n\tv_mfma_f32_16x16x32_bf16 v[112:115], a[44:47], a[128:131], v[112:115]\n\t" \
    "v_mfma_f32_16x16x32_bf16 v[116:119], a[44:47], a[132:135], v[116:119]\n\ts_add_u32 m0, m0, 4096\n\ts_add_u32 s44, s44, 262144\n\ts_cmp_ge_u32 s44, s49\n\t" \
    "s_cselect_b32 s47, s49, 0\n\ts_sub_u32 s44, s44, s47\n\tbuffer_load_dwordx4 %[va], %[ra], s44 offen lds\n\tv_mfma_f32_16x16x32_bf16 v[120:123], a[44:47], a[136:139], v[120:123]\n\t" \
    "v_mfma_f32_16x16x32_bf16 v[124:127], a[44:47], a[140:143], v[124:127]\n\ts_add_u32 m0, m0, 4096\n\ts_add_u32 s44, s44, 262144\n\ts_cmp_ge_u32 s44, s49\n\t" \
    "s_cselect_b32 s47, s49, 0\n\ts_sub_u32 s44, s44, s47\n\tbuffer_load_dwordx4 %[va], %[ra], s44 offen lds\n\tv_mfma_f32_16x16x32_bf16 v[128:131], a[48:51], a[112:115], v[128:131]\n\t" \
    "v_mfma_f32_16x16x32_bf16 v[132:135], a[48:51], a[116:119], v[132:135]\n\ts_add_u32 m0, m0, 4096\n\ts_add_u32 s44, s44, 262144\n\ts_cmp_ge_u32 s44, s49\n\t" \
    "s_cselect_b32 s47, s49, 0\n\ts_sub_u32 s44, s44, s47\n\tbuffer_load_dwordx4 %[va], %[ra], s44 offen lds\n\tv_mfma_f32_16x16x32_bf16 v[136:139], a[48:51], a[120:123], v[136:139]\n\t" \
    "v_mfma_f32_16x16x32_bf16 v[140:143], a[48:51], a[124:127], v[140:143]\n\ts_add_u32 m0, m0, 4096\n\ts_add_u32 s44, s44, 262144\n\ts_cmp_ge_u32 s44, s49\n\t" \
    "s_cselect_b32 s47, s49, 0\n\ts_sub_u32 s44, s44, s47\n\tbuffer_load_dwordx4 %[va], %[ra], s44 offen lds\n\tv_mfma_f32_16x16x32_bf16 v[144:147], a[56:59], a[112:115], v[144:147]\n\t" \
    "v_mfma_f32_16x16x32_bf16 v[148:151], a[56:59], a[116:119], v[148:151]\n\ts_add_u32 m0, m0, 4096\n\ts_add_u32 s44, s44, 262144\n\ts_cmp_ge_u32 s44, s49\n\t" \
    "s_cselect_b32 s47, s49, 0\n\ts_sub_u32 s44, s44, s47\n\tbuffer_load_dwordx4 %[va], %[ra], s44 offen lds\n\tv_mfma_f32_16x16x32_bf16 v[152:155], a[56:59], a[120:123], v[152:155]\n\t" \
    "v_mfma_f32_16x16x32_bf16 v[156:159], a[56:59], a[124:127], v[156:159]\n\ts_add_u32 m0, m0, 4096\n\ts_add_u32 s44, s44, 262144\n\ts_cmp_ge_u32 s44, s49\n\t" \
    "s_cselect_b32 s47, s49, 0\n\ts_sub_u32 s44, s44, s47\n\tbuffer_load_dwordx4 %[va], %[ra], s44 offen lds\n\tv_mfma_f32_16x16x32_bf16 v[128:131], a[52:55], a[128:131], v[128:131]\n\t" \
    "v_mfma_f32_16x16x32_bf16 v[132:135], a[52:55], a[132:135], v[132:135]\n\ts_add_u32 m0, m0, 4096\n\ts_add_u32 s44, s44, 262144\n\ts_cmp_ge_u32 s44, s49\n\t" \
    "s_cselect_b32 s47, s49, 0\n\ts_sub_u32 s44, s44, s47\n\tbuffer_load_dwordx4 %[va], %[ra], s44 offen lds\n\tv_mfma_f32_16x16x32_bf16 v[136:139], a[52:55], a[136:139], v[136:139]\n\t" \
    "v_mfma_f32_16x16x32_bf16 v[140:143], a[52:55], a[140:143], v[140:143]\n\ts_add_u32 m0, m0, 4096\n\ts_add_u32 s44, s44, 262144\n\ts_cmp_ge_u32 s44, s49\n\t" \
    "s_cselect_b32 s47, s49, 0\n\ts_sub_u32 s44, s44, s47\n\tbuffer_load_dwordx4 %[va], %[ra], s44 offen lds\n\tv_mfma_f32_16x16x32_bf16 v[144:147], a[60:63], a[128:131], v[144:147]\n\t" \
    "v_mfma_f32_16x16x32_bf16 v[148:151], a[60:63], a[132:135], v[148:151]\n\ts_add_u32 m0, m0, 4096\n\ts_add_u32 s44, s44, 262144\n\ts_cmp_ge_u32 s44, s49\n\t" \
    "s_cselect_b32 s47, s49, 0\n\ts_sub_u32 s44, s44, s47\n\tbuffer_load_dwordx4 %[va], %[ra], s44 offen lds\n\tv_mfma_f32_16x16x32_bf16 v[152:155], a[60:63], a[136:139], v[152:155]\n\t" \
    "v_mfma_f32_16x16x32_bf16 v[156:159], a[60:63], a[140:143], v[156:159]\n\ts_add_u32 m0, m0, 4096\n\ts_add_u32 s44, s44, 262144\n\ts_cmp_ge_u32 s44, s49\n\t" \
    "s_cselect_b32 s47, s49, 0\n\ts_sub_u32 s44, s44, s47\n\tbuffer_load_dwordx4 %[va], %[ra], s44 offen lds\n\tv_mfma_f32_16x16x32_bf16 v[160:163], a[64:67], a[112:115], v[160:163]\n\t" \
    "v_mfma_f32_16x16x32_bf16 v[164:167], a[64:67], a[116:119], v[164:167]\n\ts_add_u32 m0, m0, 4096\n\ts_add_u32 s44, s44, 262144\n\ts_cmp_ge_u32 s44, s49\n\t" \
    "s_cselect_b32 s47, s49, 0\n\ts_sub_u32 s44, s44, s47\n\tbuffer_load_dwordx4 %[va], %[ra], s44 offen lds\n\tv_mfma_f32_16x16x32_bf16 v[168:171], a[64:67], a[120:123], v[168:171]\n\t" \
    "v_mfma_f32_16x16x32_bf16 v[172:175], a[64:67], a[124:127], v[172:175]\n\ts_add_u32 m0, m0, 4096\n\ts_add_u32 s44, s44, 262144\n\ts_cmp_ge_u32 s44, s49\n\t" \
    "s_cselect_b32 s47, s49, 0\n\ts_sub_u32 s44, s44, s47\n\tbuffer_load_dwordx4 %[va], %[ra], s44 offen lds\n\tv_mfma_f32_16x16x32_bf16 v[176:179], a[72:75], a[112:115], v[176:179]\n\t" \
    "v_mfma_f32_16x16x32_bf16 v[180:183], a[72:75], a[116:119], v[180:183]\n\tv_mfma_f32_16x16x32_bf16 v[184:187], a[72:75], a[120:123], v[184:187]\n\t" \
    "v_mfma_f32_16x16x32_bf16 v[188:191], a[72:75], a[124:127], v[188:191]\n\tv_mfma_f32_16x16x32_bf16 v[160:163], a[68:71], a[128:131], v[160:163]\n\t" \
    "v_mfma_f32_16x16x32_bf16 v[164:167], a[68:71], a[132:135], v[164:167]\n\tv_mfma_f32_16x16x32_bf16 v[168:171], a[68:71], a[136:139], v[168:171]\n\t" \
    "v_mfma_f32_16x16x32_bf16 v[172:175], a[68:71], a[140:143], v[172:175]\n\tv_mfma_f32_16x16x32_bf16 v[176:179], a[76:79], a[128:131], v[176:179]\n\t" \
    "v_mfma_f32_16x16x32_bf16 v[180:183], a[76:79], a[132:135], v[180:183]\n\tv_mfma_f32_16x16x32_bf16 v[184:187], a[76:79], a[136:139], v[184:187]\n\t" \
    "v_mfma_f32_16x16x32_bf16 v[188:191], a[76:79], a[140:143], v[188:191]\n\tv_mfma_f32_16x16x32_bf16 v[192:195], a[80:83], a[112:115], v[192:195]\n\t" \
    "v_mfma_f32_16x16x32_bf16 v[196:199], a[80:83], a[116:119], v[196:199]\n\tv_mfma_f32_16x16x32_bf16 v[200:203], a[80:83], a[120:123], v[200:203]\n\t" \
    "v_mfma_f32_16x16x32_bf16 v[204:207], a[80:83], a[124:127], v[204:207]\n\tv_mfma_f32_16x16x32_bf16 v[208:211], a[88:91], a[112:115], v[208:211]\n\t" \
    "v_mfma_f32_16x16x32_bf16 v[212:215], a[88:91], a[116:119], v[212:215]\n\tv_mfma_f32_16x16x32_bf16 v[216:219], a[88:91], a[120:123], v[216:219]\n\t" \
    "v_mfma_f32_16x16x32_bf16 v[220:223], a[88:91], a[124:127], v[220:223]\n\tv_mfma_f32_16x16x32_bf16 v[192:195], a[84:87], a[128:131], v[192:195]\n\t" \
    "v_mfma_f32_16x16x32_bf16 v[196:199], a[84:87], a[132:135], v[196:199]\n\tv_mfma_f32_16x16x32_bf16 v[200:203], a[84:87], a[136:139], v[200:203]\n\t" \
    "v_mfma_f32_16x16x32_bf16 v[204:207], a[84:87], a[140:143], v[204:207]\n\tv_mfma_f32_16x16x32_bf16 v[208:211], a[92:95], a[128:131], v[208:211]\n\t" \
    "v_mfma_f32_16x16x32_bf16 v[212:215], a[92:95], a[132:135], v[212:215]\n\tv_mfma_f32_16x16x32_bf16 v[216:219], a[92:95], a[136:139], v[216:219]\n\t" \
    "v_mfma_f32_16x16x32_bf16 v[220:223], a[92:95], a[140:143], v[220:223]\n\tv_mfma_f32_16x16x32_bf16 v[224:227], a[96:99], a[112:115], v[224:227]\n\t" \
    "v_mfma_f32_16x16x32_bf16 v[228:231], a[96:99], a[116:119], v[228:231]\n\tv_mfma_f32_16x16x32_bf16 v[232:235], a[96:99], a[120:123], v[232:235]\n\t" \
    "v_mfma_f32_16x16x32_bf16 v[236:239], a[96:99], a[124:127], v[236:239]\n\tv_mfma_f32_16x16x32_bf16 v[240:243], a[104:107], a[112:115], v[240:243]\n\t" \
    "v_mfma_f32_16x16x32_bf16 v[244:247], a[104:107], a[116:119], v[244:247]\n\tv_mfma_f32_16x16x32_bf16 v[248:251], a[104:107], a[120:123], v[248:251]\n\t" \
    "v_mfma_f32_16x16x32_bf16 v[252:255], a[104:107], a[124:127], v[252:255]\n\tv_mfma_f32_16x16x32_bf16 v[224:227], a[100:103], a[128:131], v[224:227]\n\t" \
    "v_mfma_f32_16x16x32_bf16 v[228:231], a[100:103], a[132:135], v[228:231]\n\tv_mfma_f32_16x16x32_bf16 v[232:235], a[100:103], a[136:139], v[232:235]\n\t" \
    "v_mfma_f32_16x16x32_bf16 v[236:239], a[100:103], a[140:143], v[236:239]\n\tv_mfma_f32_16x16x32_bf16 v[240:243], a[108:111], a[128:131], v[240:243]\n\t" \
    "v_mfma_f32_16x16x32_bf16 v[244:247], a[108:111], a[132:135], v[244:247]\n\tv_mfma_f32_16x16x32_bf16 v[248:251], a[108:111], a[136:139], v[248:251]\n\t" \
    "v_mfma_f32_16x16x32_bf16 v[252:255], a[108:111], a[140:143], v[252:255]\n\t" W0T(4) "s_waitcnt vmcnt(0)\n\ts_barrier\n\t" W0T(5) "ds_read_b128 a[0:3], %[r0] offset:0\n\t" \
    "ds_read_b128 a[16:19], %[r0] offset:2048\n\tds_read_b128 a[112:115], %[q0] offset:0\n\tds_read_b128 a[116:119], %[q0] offset:2048\n\tds_read_b128 a[120:123], %[q0] offset:4096\n\t" \
    "ds_read_b128 a[124:127], %[q0] offset:6144\n\tds_read_b128 a[4:7], %[r1] offset:0\n\tds_read_b128 a[20:23], %[r1] offset:2048\n\tds_read_b128 a[128:131], %[q1] offset:0\n\t" \
    "ds_read_b128 a[132:135], %[q1] offset:2048\n\tds_read_b128 a[136:139], %[q1] offset:4096\n\tds_read_b128 a[140:143], %[q1] offset:6144\n\t" \
    "buffer_load_dword v31, %[fx], %[ws], 0 offen sc1\n\ts_waitcnt lgkmcnt(6)\n\tv_mfma_f32_16x16x32_bf16 v[32:35], a[0:3], a[112:115], v[32:35]\n\t" \
    "ds_read_b128 a[8:11], v26 offset:0\n\tds_read_b128 a[24:27], v26 offset:2048\n\tv_mfma_f32_16x16x32_bf16 v[36:39], a[0:3], a[116:119], v[36:39]\n\t" \
    "ds_read_b128 a[144:147], v28 offset:0\n\tds_read_b128 a[148:151], v28 offset:2048\n\tv_mfma_f32_16x16x32_bf16 v[40:43], a[0:3], a[120:123], v[40:43]\n\t" \
    "ds_read_b128 a[152:155], v28 offset:4096\n\tds_read_b128 a[156:159], v28 offset:6144\n\tv_mfma_f32_16x16x32_bf16 v[44:47], a[0:3], a[124:127], v[44:47]\n\t" \
    "ds_read_b128 a[12:15], v27 offset:0\n\tds_read_b128 a[28:31], v27 offset:2048\n\tv_mfma_f32_16x16x32_bf16 v[48:51], a[16:19], a[112:115], v[48:51]\n\t" \
    "ds_read_b128 a[160:163], v29 offset:0\n\tds_read_b128 a[164:167], v29 offset:2048\n\tv_mfma_f32_16x16x32_bf16 v[52:55], a[16:19], a[116:119], v[52:55]\n\t" \
    "ds_read_b128 a[168:171], v29 offset:4096\n\tds_read_b128 a[172:175], v29 offset:6144\n\tv_mfma_f32_16x16x32_bf16 v[56:59], a[16:19], a[120:123], v[56:59]\n\t" \
    "ds_read_b128 a[32:35], %[r0] offset:4096\n\tds_read_b128 a[48:51], %[r0] offset:6144\n\tv_mfma_f32_16x16x32_bf16 v[60:63], a[16:19], a[124:127], v[60:63]\n\t" \
    "ds_read_b128 a[36:39], %[r1] offset:4096\n\tds_read_b128 a[52:55], %[r1] offset:6144\n\ts_waitcnt lgkmcnt(12)\n\tv_mfma_f32_16x16x32_bf16 v[32:35], a[4:7], a[128:131], v[32:35]\n\t" \
    "ds_read_b128 a[40:43], v26 offset:4096\n\tds_read_b128 a[56:59], v26 offset:6144\n\tv_mfma_f32_16x16x32_bf16 v[36:39], a[4:7], a[132:135], v[36:39]\n\t" \
    "ds_read_b128 a[44:47], v27 offset:4096\n\tds_read_b128 a[60:63], v27 offset:6144\n\tv_mfma_f32_16x16x32_bf16 v[40:43], a[4:7], a[136:139], v[40:43]\n\t" \
    "ds_read_b128 a[64:67], %[r0] offset:8192\n\tds_read_b128 a[80:83], %[r0] offset:10240\n\tv_mfma_f32_16x16x32_bf16 v[44:47], a[4:7], a[140:143], v[44:47]\n\t" \
    "ds_read_b128 a[68:71], %[r1] offset:8192\n\tds_read_b128 a[84:87], %[r1] offset:10240\n\tv_mfma_f32_16x16x32_bf16 v[48:51], a[20:23], a[128:131], v[48:51]\n\t" \
    "ds_read_b128 a[72:75], v26 offset:8192\n\tds_read_b128 a[88:91], v26 offset:10240\n\tv_mfma_f32_16x16x32_bf16 v[52:55], a[20:23], a[132:135], v[52:55]\n\t" \
    "ds_read_b128 a[76:79], v27 offset:8192\n\tds_read_b128 a[92:95], v27 offset:10240\n\tv_mfma_f32_16x16x32_bf16 v[56:59], a[20:23], a[136:139], v[56:59]\n\t" \
    "v_mfma_f32_16x16x32_bf16 v[60:63], a[20:23], a[140:143], v[60:63]\n\ts_waitcnt lgkmcnt(12)\n\tv_mfma_f32_16x16x32_bf16 v[32:35], a[8:11], a[144:147], v[32:35]\n\t" \
    "v_mfma_f32_16x16x32_bf16 v[36:39], a[8:11], a[148:151], v[36:39]\n\tv_mfma_f32_16x16x32_bf16 v[40:43], a[8:11], a[152:155], v[40:43]\n\t" \
    "v_mfma_f32_16x16x32_bf16 v[44:47], a[8:11], a[156:159], v[44:47]\n\tv_mfma_f32_16x16x32_bf16 v[48:51], a[24:27], a[144:147], v[48:51]\n\t" \
    "v_mfma_f32_16x16x32_bf16 v[52:55], a[24:27], a[148:151], v[52:55]\n\tv_mfma_f32_16x16x32_bf16 v[56:59], a[24:27], a[152:155], v[56:59]\n\t" \
    "v_mfma_f32_16x16x32_bf16 v[60:63], a[24:27], a[156:159], v[60:63]\n\ts_waitcnt lgkmcnt(12)\n\tv_mfma_f32_16x16x32_bf16 v[32:35], a[12:15], a[160:163], v[32:35]\n\t" \
    "v_mfma_f32_16x16x32_bf16 v[36:39], a[12:15], a[164:167], v[36:39]\n\tv_mfma_f32_16x16x32_bf16 v[40:43], a[12:15], a[168:171], v[40:43]\n\t" \
    "v_mfma_f32_16x16x32_bf16 v[44:47], a[12:15], a[172:175], v[44:47]\n\tv_mfma_f32_16x16x32_bf16 v[48:51], a[28:31], a[160:163], v[48:51]\n\t" \
    "v_mfma_f32_16x16x32_bf16 v[52:55], a[28:31], a[164:167], v[52:55]\n\tv_mfma_f32_16x16x32_bf16 v[56:59], a[28:31], a[168:171], v[56:59]\n\t" \
    "v_mfma_f32_16x16x32_bf16 v[60:63], a[28:31], a[172:175], v[60:63]\n\ts_waitcnt lgkmcnt(8)\n\ts_barrier\n\tv_mfma_f32_16x16x32_bf16 v[64:67], a[32:35], a[112:115], v[64:67]\n\t" \
    "ds_read_b128 a[0:3], %[r0] offset:12288\n\tv_mfma_f32_16x16x32_bf16 v[68:71], a[32:35], a[116:119], v[68:71]\n\tds_read_b128 a[16:19], %[r0] offset:14336\n\t" \
    "v_mfma_f32_16x16x32_bf16 v[72:75], a[32:35], a[120:123], v[72:75]\n\tds_read_b128 a[4:7], %[r1] offset:12288\n\t" \
    "v_mfma_f32_16x16x32_bf16 v[76:79], a[32:35], a[124:127], v[76:79]\n\tds_read_b128 a[20:23], %[r1] offset:14336\n\t" \
    "v_mfma_f32_16x16x32_bf16 v[80:83], a[48:51], a[112:115], v[80:83]\n\tds_read_b128 a[8:11], v26 offset:12288\n\tv_mfma_f32_16x16x32_bf16 v[84:87], a[48:51], a[116:119], v[84:87]\n\t" \
    "ds_read_b128 a[24:27], v26 offset:14336\n\tv_mfma_f32_16x16x32_bf16 v[88:91], a[48:51], a[120:123], v[88:91]\n\tds_read_b128 a[12:15], v27 offset:12288\n\t" \
    "v_mfma_f32_16x16x32_bf16 v[92:95], a[48:51], a[124:127], v[92:95]\n\tds_read_b128 a[28:31], v27 offset:14336\n\t" \
    "v_mfma_f32_16x16x32_bf16 v[64:67], a[36:39], a[128:131], v[64:67]\n\tv_pk_mul_f32 v[32:33], v[32:33], s[54:55]\n\tv_pk_mul_f32 v[34:35], v[34:35], s[54:55]\n\t" \
    "v_mfma_f32_16x16x32_bf16 v[68:71], a[36:39], a[132:135], v[68:71]\n\tv_pk_mul_f32 v[48:49], v[48:49], s[54:55]\n\tv_pk_mul_f32 v[50:51], v[50:51], s[54:55]\n\t" \
    "v_mfma_f32_16x16x32_bf16 v[72:75], a[36:39], a[136:139], v[72:75]\n\tv_cvt_pk_bf16_f32 v32, v32, v33\n\tv_cvt_pk_bf16_f32 v33, v34, v35\n\t" \
    "v_mfma_f32_16x16x32_bf16 v[76:79], a[36:39], a[140:143], v[76:79]\n\tv_cvt_pk_bf16_f32 v34, v48, v49\n\tv_cvt_pk_bf16_f32 v35, v50, v51\n\t" \
    "v_mfma_f32_16x16x32_bf16 v[80:83], a[52:55], a[128:131], v[80:83]\n\tds_write_b128 %[iw], v[32:35] offset:0\n\tv_pk_mul_f32 v[36:37], v[36:37], s[54:55]\n\t" \
    "v_mfma_f32_16x16x32_bf16 v[84:87], a[52:55], a[132:135], v[84:87]\n\tv_pk_mul_f32 v[38:39], v[38:39], s[54:55]\n\tv_pk_mul_f32 v[52:53], v[52:53], s[54:55]\n\t" \
    "v_mfma_f32_16x16x32_bf16 v[88:91], a[52:55], a[136:139], v[88:91]\n\tv_pk_mul_f32 v[54:55], v[54:55], s[54:55]\n\tv_cvt_pk_bf16_f32 v36, v36, v37\n\t" \
    "v_mfma_f32_16x16x32_bf16 v[92:95], a[52:55], a[140:143], v[92:95]\n\tv_cvt_pk_bf16_f32 v37, v38, v39\n\tv_cvt_pk_bf16_f32 v38, v52, v53\n\t" \
    "v_mfma_f32_16x16x32_bf16 v[64:67], a[40:43], a[144:147], v[64:67]\n\tv_cvt_pk_bf16_f32 v39, v54, v55\n\tds_write_b128 %[iw], v[36:39] offset:1024\n\t" \
    "v_mfma_f32_16x16x32_bf16 v[68:71], a[40:43], a[148:151], v[68:71]\n\tv_pk_mul_f32 v[40:41], v[40:41], s[54:55]\n\tv_pk_mul_f32 v[42:43], v[42:43], s[54:55]\n\t" \
    "v_mfma_f32_16x16x32_bf16 v[72:75], a[40:43], a[152:155], v[72:75]\n\tv_pk_mul_f32 v[56:57], v[56:57], s[54:55]\n\tv_pk_mul_f32 v[58:59], v[58:59], s[54:55]\n\t" \
    "v_mfma_f32_16x16x32_bf16 v[76:79], a[40:43], a[156:159], v[76:79]\n\tv_cvt_pk_bf16_f32 v40, v40, v41\n\tv_cvt_pk_bf16_f32 v41, v42, v43\n\t" \
    "v_mfma_f32_16x16x32_bf16 v[80:83], a[56:59], a[144:147], v[80:83]\n\tv_cvt_pk_bf16_f32 v42, v56, v57\n\tv_cvt_pk_bf16_f32 v43, v58, v59\n\t" \
    "v_mfma_f32_16x16x32_bf16 v[84:87], a[56:59], a[148:151], v[84:87]\n\tds_write_b128 %[iw], v[40:43] offset:2048\n\tv_pk_mul_f32 v[44:45], v[44:45], s[54:55]\n\t" \
    "v_mfma_f32_16x16x32_bf16 v[88:91], a[56:59], a[152:155], v[88:91]\n\tv_pk_mul_f32 v[46:47], v[46:47], s[54:55]\n\tv_pk_mul_f32 v[60:61], v[60:61], s[54:55]\n\t" \
    "v_mfma_f32_16x16x32_bf16 v[92:95], a[56:59], a[156:159], v[92:95]\n\tv_pk_mul_f32 v[62:63], v[62:63], s[54:55]\n\tv_cvt_pk_bf16_f32 v44, v44, v45\n\t" \
    "v_mfma_f32_16x16x32_bf16 v[64:67], a[44:47], a[160:163], v[64:67]\n\tv_cvt_pk_bf16_f32 v45, v46, v47\n\tv_cvt_pk_bf16_f32 v46, v60, v61\n\t" \
    "v_mfma_f32_16x16x32_bf16 v[68:71], a[44:47], a[164:167], v[68:71]\n\tv_cvt_pk_bf16_f32 v47, v62, v63\n\tds_write_b128 %[iw], v[44:47] offset:3072\n\t" \
    "v_mfma_f32_16x16x32_bf16 v[72:75], a[44:47], a[168:171], v[72:75]\n\tv_mfma_f32_16x16x32_bf16 v[76:79], a[44:47], a[172:175], v[76:79]\n\t" \
    "v_mfma_f32_16x16x32_bf16 v[80:83], a[60:63], a[160:163], v[80:83]\n\tv_mfma_f32_16x16x32_bf16 v[84:87], a[60:63], a[164:167], v[84:87]\n\t" \
    "v_mfma_f32_16x16x32_bf16 v[88:91], a[60:63], a[168:171], v[88:91]\n\tv_mfma_f32_16x16x32_bf16 v[92:95], a[60:63], a[172:175], v[92:95]\n\ts_waitcnt lgkmcnt(12)\n\ts_barrier\n\t" \
    "v_mfma_f32_16x16x32_bf16 v[96:99], a[64:67], a[112:115], v[96:99]\n\tds_read_b128 a[32:35], %[r0] offset:16384\n\t" \
    "v_mfma_f32_16x16x32_bf16 v[100:103], a[64:67], a[116:119], v[100:103]\n\tds_read_b128 a[48:51], %[r0] offset:18432\n\t" \
    "v_mfma_f32_16x16x32_bf16 v[104:107], a[64:67], a[120:123], v[104:107]\n\tds_read_b128 a[36:39], %[r1] offset:16384\n\t" \
    "v_mfma_f32_16x16x32_bf16 v[108:111], a[64:67], a[124:127], v[108:111]\n\tds_read_b128 a[52:55], %[r1] offset:18432\n\t" \
    "v_mfma_f32_16x16x32_bf16 v[112:115], a[80:83], a[112:115], v[112:115]\n\tds_read_b128 a[40:43], v26 offset:16384\n\t" \
    "v_mfma_f32_16x16x32_bf16 v[116:119], a[80:83], a[116:119], v[116:119]\n\tds_read_b128 a[56:59], v26 offset:18432\n\t" \
    "v_mfma_f32_16x16x32_bf16 v[120:123], a[80:83], a[120:123], v[120:123]\n\tds_read_b128 a[44:47], v27 offset:16384\n\t" \
    "v_mfma_f32_16x16x32_bf16 v[124:127], a[80:83], a[124:127], v[124:127]\n\tds_read_b128 a[60:63], v27 offset:18432\n\t" \
    "v_mfma_f32_16x16x32_bf16 v[96:99], a[68:71], a[128:131], v[96:99]\n\tv_pk_mul_f32 v[64:65], v[64:65], s[54:55]\n\tv_pk_mul_f32 v[66:67], v[66:67], s[54:55]\n\t" \
    "v_mfma_f32_16x16x32_bf16 v[100:103], a[68:71], a[132:135], v[100:103]\n\tv_pk_mul_f32 v[80:81], v[80:81], s[54:55]\n\tv_pk_mul_f32 v[82:83], v[82:83], s[54:55]\n\t" \
    "v_mfma_f32_16x16x32_bf16 v[104:107], a[68:71], a[136:139], v[104:107]\n\tv_cvt_pk_bf16_f32 v64, v64, v65\n\tv_cvt_pk_bf16_f32 v65, v66, v67\n\t" \
    "v_mfma_f32_16x16x32_bf16 v[108:111], a[68:71], a[140:143], v[108:111]\n\tv_cvt_pk_bf16_f32 v66, v80, v81\n\tv_cvt_pk_bf16_f32 v67, v82, v83\n\t" \
    "v_mfma_f32_16x16x32_bf16 v[112:115], a[84:87], a[128:131], v[112:115]\n\tds_write_b128 %[iw], v[64:67] offset:4096\n\tv_pk_mul_f32 v[68:69], v[68:69], s[54:55]\n\t" \
    "v_mfma_f32_16x16x32_bf16 v[116:119], a[84:87], a[132:135], v[116:119]\n\tv_pk_mul_f32 v[70:71], v[70:71], s[54:55]\n\tv_pk_mul_f32 v[84:85], v[84:85], s[54:55]\n\t" \
    "v_mfma_f32_16x16x32_bf16 v[120:123], a[84:87], a[136:139], v[120:123]\n\tv_pk_mul_f32 v[86:87], v[86:87], s[54:55]\n\tv_cvt_pk_bf16_f32 v68, v68, v69\n\t" \
    "v_mfma_f32_16x16x32_bf16 v[124:127], a[84:87], a[140:143], v[124:127]\n\tv_cvt_pk_bf16_f32 v69, v70, v71\n\tv_cvt_pk_bf16_f32 v70, v84, v85\n\t" \
    "v_mfma_f32_16x16x32_bf16 v[96:99], a[72:75], a[144:147], v[96:99]\n\tv_cvt_pk_bf16_f32 v71, v86, v87\n\tds_write_b128 %[iw], v[68:71] offset:5120\n\t" \
    "v_mfma_f32_16x16x32_bf16 v[100:103], a[72:75], a[148:151], v[100:103]\n\tv_pk_mul_f32 v[72:73], v[72:73], s[54:55]\n\tv_pk_mul_f32 v[74:75], v[74:75], s[54:55]\n\t" \
    "v_mfma_f32_16x16x32_bf16 v[104:107], a[72:75], a[152:155], v[104:107]\n\tv_pk_mul_f32 v[88:89], v[88:89], s[54:55]\n\tv_pk_mul_f32 v[90:91], v[90:91], s[54:55]\n\t" \
    "v_mfma_f32_16x16x32_bf16 v[108:111], a[72:75], a[156:159], v[108:111]\n\tv_cvt_pk_bf16_f32 v72, v72, v73\n\tv_cvt_pk_bf16_f32 v73, v74, v75\n\t" \
    "v_mfma_f32_16x16x32_bf16 v[112:115], a[88:91], a[144:147], v[112:115]\n\tv_cvt_pk_bf16_f32 v74, v88, v89\n\tv_cvt_pk_bf16_f32 v75, v90, v91\n\t" \
    "v_mfma_f32_16x16x32_bf16 v[116:119], a[88:91], a[148:151], v[116:119]\n\tds_write_b128 %[iw], v[72:75] offset:6144\n\tv_pk_mul_f32 v[76:77], v[76:77], s[54:55]\n\t" \
    "v_mfma_f32_16x16x32_bf16 v[120:123], a[88:91], a[152:155], v[120:123]\n\tv_pk_mul_f32 v[78:79], v[78:79], s[54:55]\n\tv_pk_mul_f32 v[92:93], v[92:93], s[54:55]\n\t" \
    "v_mfma_f32_16x16x32_bf16 v[124:127], a[88:91], a[156:159], v[124:127]\n\tv_pk_mul_f32 v[94:95], v[94:95], s[54:55]\n\tv_cvt_pk_bf16_f32 v76, v76, v77\n\t" \
    "v_mfma_f32_16x16x32_bf16 v[96:99], a[76:79], a[160:163], v[96:99]\n\tv_cvt_pk_bf16_f32 v77, v78, v79\n\tv_cvt_pk_bf16_f32 v78, v92, v93\n\t" \
    "v_mfma_f32_16x16x32_bf16 v[100:103], a[76:79], a[164:167], v[100:103]\n\tv_cvt_pk_bf16_f32 v79, v94, v95\n\tds_write_b128 %[iw], v[76:79] offset:7168\n\t" \
    "v_mfma_f32_16x16x32_bf16 v[104:107], a[76:79], a[168:171], v[104:107]\n\tv_mfma_f32_16x16x32_bf16 v[108:111], a[76:79], a[172:175], v[108:111]\n\t" \
    "v_mfma_f32_16x16x32_bf16 v[112:115], a[92:95], a[160:163], v[112:115]\n\tv_mfma_f32_16x16x32_bf16 v[116:119], a[92:95], a[164:167], v[116:119]\n\t" \
    "v_mfma_f32_16x16x32_bf16 v[120:123], a[92:95], a[168:171], v[120:123]\n\tv_mfma_f32_16x16x32_bf16 v[124:127], a[92:95], a[172:175], v[124:127]\n\ts_waitcnt vmcnt(0)\n\t" \
    "v_cmp_eq_u32 vcc, %[h0], v30\n\tv_cmp_gt_i32 s[46:47], 0, v31\n\ts_or_b64 vcc, vcc, s[46:47]\n\ts_cselect_b32 %[ok], 0, 1\n\ts_cbranch_scc1 np%=\n\t" \
    "buffer_load_dword v24, %[px], %[ws], 0 offen offset:0 sc1\n\tbuffer_load_dword v24, %[px], %[ws], 0 offen offset:128 sc1\n\t" \
    "buffer_load_dword v24, %[px], %[ws], 0 offen offset:256 sc1\n\tbuffer_load_dword v24, %[px], %[ws], 0 offen offset:384 sc1\n\tnp%=:\n\ts_waitcnt lgkmcnt(12)\n\ts_barrier\n\t" \
    "v_mfma_f32_16x16x32_bf16 v[128:131], a[0:3], a[112:115], v[128:131]\n\tds_read_b128 a[64:67], %[r0] offset:20480\n\t" \
    "v_mfma_f32_16x16x32_bf16 v[132:135], a[0:3], a[116:119], v[132:135]\n\tds_read_b128 a[80:83], %[r0] offset:22528\n\t" \
    "v_mfma_f32_16x16x32_bf16 v[136:139], a[0:3], a[120:123], v[136:139]\n\tds_read_b128 a[68:71], %[r1] offset:20480\n\t" \
    "v_mfma_f32_16x16x32_bf16 v[140:143], a[0:3], a[124:127], v[140:143]\n\tds_read_b128 a[84:87], %[r1] offset:22528\n\t" \
    "v_mfma_f32_16x16x32_bf16 v[144:147], a[16:19], a[112:115], v[144:147]\n\tds_read_b128 a[72:75], v26 offset:20480\n\t" \
    "v_mfma_f32_16x16x32_bf16 v[148:151], a[16:19], a[116:119], v[148:151]\n\tds_read_b128 a[88:91], v26 offset:22528\n\t" \
    "v_mfma_f32_16x16x32_bf16 v[152:155], a[16:19], a[120:123], v[152:155]\n\tds_read_b128 a[76:79], v27 offset:20480\n\t" \
    "v_mfma_f32_16x16x32_bf16 v[156:159], a[16:19], a[124:127], v[156:159]\n\tds_read_b128 a[92:95], v27 offset:22528\n\t" \
    "v_mfma_f32_16x16x32_bf16 v[128:131], a[4:7], a[128:131], v[128:131]\n\tv_pk_mul_f32 v[96:97], v[96:97], s[54:55]\n\tv_pk_mul_f32 v[98:99], v[98:99], s[54:55]\n\t" \
    "v_mfma_f32_16x16x32_bf16 v[132:135], a[4:7], a[132:135], v[132:135]\n\tv_pk_mul_f32 v[112:113], v[112:113], s[54:55]\n\tv_pk_mul_f32 v[114:115], v[114:115], s[54:55]\n\t" \
    "v_mfma_f32_16x16x32_bf16 v[136:139], a[4:7], a[136:139], v[136:139]\n\tv_cvt_pk_bf16_f32 v96, v96, v97\n\tv_cvt_pk_bf16_f32 v97, v98, v99\n\t" \
    "v_mfma_f32_16x16x32_bf16 v[140:143], a[4:7], a[140:143], v[140:143]\n\tv_cvt_pk_bf16_f32 v98, v112, v113\n\tv_cvt_pk_bf16_f32 v99, v114, v115\n\t" \
    "v_mfma_f32_16x16x32_bf16 v[144:147], a[20:23], a[128:131], v[144:147]\n\tds_write_b128 %[iw], v[96:99] offset:8192\n\tv_pk_mul_f32 v[100:101], v[100:101], s[54:55]\n\t" \
    "v_mfma_f32_16x16x32_bf16 v[148:151], a[20:23], a[132:135], v[148:151]\n\tv_pk_mul_f32 v[102:103], v[102:103], s[54:55]\n\tv_pk_mul_f32 v[116:117], v[116:117], s[54:55]\n\t" \
    "v_mfma_f32_16x16x32_bf16 v[152:155], a[20:23], a[136:139], v[152:155]\n\tv_pk_mul_f32 v[118:119], v[118:119], s[54:55]\n\tv_cvt_pk_bf16_f32 v100, v100, v101\n\t" \
    "v_mfma_f32_16x16x32_bf16 v[156:159], a[20:23], a[140:143], v[156:159]\n\tv_cvt_pk_bf16_f32 v101, v102, v103\n\tv_cvt_pk_bf16_f32 v102, v116, v117\n\t" \
    "v_mfma_f32_16x16x32_bf16 v[128:131], a[8:11], a[144:147], v[128:131]\n\tv_cvt_pk_bf16_f32 v103, v118, v119\n\tds_write_b128 %[iw], v[100:103] offset:9216\n\t" \
    "v_mfma_f32_16x16x32_bf16 v[132:135], a[8:11], a[148:151], v[132:135]\n\tv_pk_mul_f32 v[104:105], v[104:105], s[54:55]\n\tv_pk_mul_f32 v[106:107], v[106:107], s[54:55]\n\t" \
    "v_mfma_f32_16x16x32_bf16 v[136:139], a[8:11], a[152:155], v[136:139]\n\tv_pk_mul_f32 v[120:121], v[120:121], s[54:55]\n\tv_pk_mul_f32 v[122:123], v[122:123], s[54:55]\n\t" \
    "v_mfma_f32_16x16x32_bf16 v[140:143], a[8:11], a[156:159], v[140:143]\n\tv_cvt_pk_bf16_f32 v104, v104, v105\n\tv_cvt_pk_bf16_f32 v105, v106, v107\n\t" \
    "v_mfma_f32_16x16x32_bf16 v[144:147], a[24:27], a[144:147], v[144:147]\n\tv_cvt_pk_bf16_f32 v106, v120, v121\n\tv_cvt_pk_bf16_f32 v107, v122, v123\n\t" \
    "v_mfma_f32_16x16x32_bf16 v[148:151], a[24:27], a[148:151], v[148:151]\n\tds_write_b128 %[iw], v[104:107] offset:10240\n\tv_pk_mul_f32 v[108:109], v[108:109], s[54:55]\n\t" \
    "v_mfma_f32_16x16x32_bf16 v[152:155], a[24:27], a[152:155], v[152:155]\n\tv_pk_mul_f32 v[110:111], v[110:111], s[54:55]\n\tv_pk_mul_f32 v[124:125], v[124:125], s[54:55]\n\t" \
    "v_mfma_f32_16x16x32_bf16 v[156:159], a[24:27], a[156:159], v[156:159]\n\tv_pk_mul_f32 v[126:127], v[126:127], s[54:55]\n\tv_cvt_pk_bf16_f32 v108, v108, v109\n\t" \
    "v_mfma_f32_16x16x32_bf16 v[128:131], a[12:15], a[160:163], v[128:131]\n\tv_cvt_pk_bf16_f32 v109, v110, v111\n\tv_cvt_pk_bf16_f32 v110, v124, v125\n\t" \
    "v_mfma_f32_16x16x32_bf16 v[132:135], a[12:15], a[164:167], v[132:135]\n\tv_cvt_pk_bf16_f32 v111, v126, v127\n\tds_write_b128 %[iw], v[108:111] offset:11264\n\t" \
    "v_mfma_f32_16x16x32_bf16 v[136:139], a[12:15], a[168:171], v[136:139]\n\tv_mfma_f32_16x16x32_bf16 v[140:143], a[12:15], a[172:175], v[140:143]\n\t" \
    "v_mfma_f32_16x16x32_bf16 v[144:147], a[28:31], a[160:163], v[144:147]\n\tv_mfma_f32_16x16x32_bf16 v[148:151], a[28:31], a[164:167], v[148:151]\n\t" \
    "v_mfma_f32_16x16x32_bf16 v[152:155], a[28:31], a[168:171], v[152:155]\n\tv_mfma_f32_16x16x32_bf16 v[156:159], a[28:31], a[172:175], v[156:159]\n\ts_waitcnt lgkmcnt(12)\n\t" \
    "s_barrier\n\tv_mfma_f32_16x16x32_bf16 v[160:163], a[32:35], a[112:115], v[160:163]\n\tds_read_b128 a[0:3], %[r0] offset:24576\n\t" \
    "v_mfma_f32_16x16x32_bf16 v[164:167], a[32:35], a[116:119], v[164:167]\n\tds_read_b128 a[16:19], %[r0] offset:26624\n\t" \
    "v_mfma_f32_16x16x32_bf16 v[168:171], a[32:35], a[120:123], v[168:171]\n\tds_read_b128 a[4:7], %[r1] offset:24576\n\t" \
    "v_mfma_f32_16x16x32_bf16 v[172:175], a[32:35], a[124:127], v[172:175]\n\tds_read_b128 a[20:23], %[r1] offset:26624\n\t" \
    "v_mfma_f32_16x16x32_bf16 v[176:179], a[48:51], a[112:115], v[176:179]\n\tds_read_b128 a[8:11], v26 offset:24576\n\t" \
    "v_mfma_f32_16x16x32_bf16 v[180:183], a[48:51], a[116:119], v[180:183]\n\tds_read_b128 a[24:27], v26 offset:26624\n\t" \
    "v_mfma_f32_16x16x32_bf16 v[184:187], a[48:51], a[120:123], v[184:187]\n\tds_read_b128 a[12:15], v27 offset:24576\n\t" \
    "v_mfma_f32_16x16x32_bf16 v[188:191], a[48:51], a[124:127], v[188:191]\n\tds_read_b128 a[28:31], v27 offset:26624\n\t" \
    "v_mfma_f32_16x16x32_bf16 v[160:163], a[36:39], a[128:131], v[160:163]\n\tv_pk_mul_f32 v[128:129], v[128:129], s[54:55]\n\tv_pk_mul_f32 v[130:131], v[130:131], s[54:55]\n\t" \
    "v_mfma_f32_16x16x32_bf16 v[164:167], a[36:39], a[132:135], v[164:167]\n\tv_pk_mul_f32 v[144:145], v[144:145], s[54:55]\n\tv_pk_mul_f32 v[146:147], v[146:147], s[54:55]\n\t" \
    "v_mfma_f32_16x16x32_bf16 v[168:171], a[36:39], a[136:139], v[168:171]\n\tv_cvt_pk_bf16_f32 v128, v128, v129\n\tv_cvt_pk_bf16_f32 v129, v130, v131\n\t" \
    "v_mfma_f32_16x16x32_bf16 v[172:175], a[36:39], a[140:143], v[172:175]\n\tv_cvt_pk_bf16_f32 v130, v144, v145\n\tv_cvt_pk_bf16_f32 v131, v146, v147\n\t" \
    "v_mfma_f32_16x16x32_bf16 v[176:179], a[52:55], a[128:131], v[176:179]\n\tds_write_b128 %[iw], v[128:131] offset:12288\n\tv_pk_mul_f32 v[132:133], v[132:133], s[54:55]\n\t" \
    "v_mfma_f32_16x16x32_bf16 v[180:183], a[52:55], a[132:135], v[180:183]\n\tv_pk_mul_f32 v[134:135], v[134:135], s[54:55]\n\tv_pk_mul_f32 v[148:149], v[148:149], s[54:55]\n\t" \
    "v_mfma_f32_16x16x32_bf16 v[184:187], a[52:55], a[136:139], v[184:187]\n\tv_pk_mul_f32 v[150:151], v[150:151], s[54:55]\n\tv_cvt_pk_bf16_f32 v132, v132, v133\n\t" \
    "v_mfma_f32_16x16x32_bf16 v[188:191], a[52:55], a[140:143], v[188:191]\n\tv_cvt_pk_bf16_f32 v133, v134, v135\n\tv_cvt_pk_bf16_f32 v134, v148, v149\n\t" \
    "v_mfma_f32_16x16x32_bf16 v[160:163], a[40:43], a[144:147], v[160:163]\n\tv_cvt_pk_bf16_f32 v135, v150, v151\n\tds_write_b128 %[iw], v[132:135] offset:13312\n\t" \
    "v_mfma_f32_16x16x32_bf16 v[164:167], a[40:43], a[148:151], v[164:167]\n\tv_pk_mul_f32 v[136:137], v[136:137], s[54:55]\n\tv_pk_mul_f32 v[138:139], v[138:139], s[54:55]\n\t" \
    "v_mfma_f32_16x16x32_bf16 v[168:171], a[40:43], a[152:155], v[168:171]\n\tv_pk_mul_f32 v[152:153], v[152:153], s[54:55]\n\tv_pk_mul_f32 v[154:155], v[154:155], s[54:55]\n\t" \
    "v_mfma_f32_16x16x32_bf16 v[172:175], a[40:43], a[156:159], v[172:175]\n\tv_cvt_pk_bf16_f32 v136, v136, v137\n\tv_cvt_pk_bf16_f32 v137, v138, v139\n\t" \
    "v_mfma_f32_16x16x32_bf16 v[176:179], a[56:59], a[144:147], v[176:179]\n\tv_cvt_pk_bf16_f32 v138, v152, v153\n\tv_cvt_pk_bf16_f32 v139, v154, v155\n\t" \
    "v_mfma_f32_16x16x32_bf16 v[180:183], a[56:59], a[148:151], v[180:183]\n\tds_write_b128 %[iw], v[136:139] offset:14336\n\tv_pk_mul_f32 v[140:141], v[140:141], s[54:55]\n\t" \
    "v_mfma_f32_16x16x32_bf16 v[184:187], a[56:59], a[152:155], v[184:187]\n\tv_pk_mul_f32 v[142:143], v[142:143], s[54:55]\n\tv_pk_mul_f32 v[156:157], v[156:157], s[54:55]\n\t" \
    "v_mfma_f32_16x16x32_bf16 v[188:191], a[56:59], a[156:159], v[188:191]\n\tv_pk_mul_f32 v[158:159], v[158:159], s[54:55]\n\tv_cvt_pk_bf16_f32 v140, v140, v141\n\t" \
    "v_mfma_f32_16x16x32_bf16 v[160:163], a[44:47], a[160:163], v[160:163]\n\tv_cvt_pk_bf16_f32 v141, v142, v143\n\tv_cvt_pk_bf16_f32 v142, v156, v157\n\t" \
    "v_mfma_f32_16x16x32_bf16 v[164:167], a[44:47], a[164:167], v[164:167]\n\tv_cvt_pk_bf16_f32 v143, v158, v159\n\tds_write_b128 %[iw], v[140:143] offset:15360\n\t" \
    "v_mfma_f32_16x16x32_bf16 v[168:171], a[44:47], a[168:171], v[168:171]\n\tv_mfma_f32_16x16x32_bf16 v[172:175], a[44:47], a[172:175], v[172:175]\n\t" \
    "v_mfma_f32_16x16x32_bf16 v[176:179], a[60:63], a[160:163], v[176:179]\n\tv_mfma_f32_16x16x32_bf16 v[180:183], a[60:63], a[164:167], v[180:183]\n\t" \
    "v_mfma_f32_16x16x32_bf16 v[184:187], a[60:63], a[168:171], v[184:187]\n\tv_mfma_f32_16x16x32_bf16 v[188:191], a[60:63], a[172:175], v[188:191]\n\ts_waitcnt lgkmcnt(12)\n\t" \
    "s_barrier\n\tv_mfma_f32_16x16x32_bf16 v[192:195], a[64:67], a[112:115], v[192:195]\n\tv_pk_mul_f32 v[160:161], v[160:161], s[54:55]\n\t" \
    "v_pk_mul_f32 v[162:163], v[162:163], s[54:55]\n\tv_mfma_f32_16x16x32_bf16 v[196:199], a[64:67], a[116:119], v[196:199]\n\tv_pk_mul_f32 v[176:177], v[176:177], s[54:55]\n\t" \
    "v_pk_mul_f32 v[178:179], v[178:179], s[54:55]\n\tv_mfma_f32_16x16x32_bf16 v[200:203], a[64:67], a[120:123], v[200:203]\n\tv_cvt_pk_bf16_f32 v160, v160, v161\n\t" \
    "v_cvt_pk_bf16_f32 v161, v162, v163\n\tv_mfma_f32_16x16x32_bf16 v[204:207], a[64:67], a[124:127], v[204:207]\n\tv_cvt_pk_bf16_f32 v162, v176, v177\n\t" \
    "v_cvt_pk_bf16_f32 v163, v178, v179\n\tv_mfma_f32_16x16x32_bf16 v[208:211], a[80:83], a[112:115], v[208:211]\n\tds_write_b128 %[iw], v[160:163] offset:16384\n\t" \
    "v_pk_mul_f32 v[164:165], v[164:165], s[54:55]\n\tv_mfma_f32_16x16x32_bf16 v[212:215], a[80:83], a[116:119], v[212:215]\n\tv_pk_mul_f32 v[166:167], v[166:167], s[54:55]\n\t" \
    "v_pk_mul_f32 v[180:181], v[180:181], s[54:55]\n\tv_mfma_f32_16x16x32_bf16 v[216:219], a[80:83], a[120:123], v[216:219]\n\tv_pk_mul_f32 v[182:183], v[182:183], s[54:55]\n\t" \
    "v_cvt_pk_bf16_f32 v164, v164, v165\n\tv_mfma_f32_16x16x32_bf16 v[220:223], a[80:83], a[124:127], v[220:223]\n\tv_cvt_pk_bf16_f32 v165, v166, v167\n\t" \
    "v_cvt_pk_bf16_f32 v166, v180, v181\n\tv_mfma_f32_16x16x32_bf16 v[192:195], a[68:71], a[128:131], v[192:195]\n\tv_cvt_pk_bf16_f32 v167, v182, v183\n\t" \
    "ds_write_b128 %[iw], v[164:167] offset:17408\n\tv_mfma_f32_16x16x32_bf16 v[196:199], a[68:71], a[132:135], v[196:199]\n\tv_pk_mul_f32 v[168:169], v[168:169], s[54:55]\n\t" \
    "v_pk_mul_f32 v[170:171], v[170:171], s[54:55]\n\tv_mfma_f32_16x16x32_bf16 v[200:203], a[68:71], a[136:139], v[200:203]\n\tv_pk_mul_f32 v[184:185], v[184:185], s[54:55]\n\t" \
    "v_pk_mul_f32 v[186:187], v[186:187], s[54:55]\n\tv_mfma_f32_16x16x32_bf16 v[204:207], a[68:71], a[140:143], v[204:207]\n\tv_cvt_pk_bf16_f32 v168, v168, v169\n\t" \
    "v_cvt_pk_bf16_f32 v169, v170, v171\n\tv_mfma_f32_16x16x32_bf16 v[208:211], a[84:87], a[128:131], v[208:211]\n\tv_cvt_pk_bf16_f32 v170, v184, v185\n\t" \
    "v_cvt_pk_bf16_f32 v171, v186, v187\n\tv_mfma_f32_16x16x32_bf16 v[212:215], a[84:87], a[132:135], v[212:215]\n\tds_write_b128 %[iw], v[168:171] offset:18432\n\t" \
    "v_pk_mul_f32 v[172:173], v[172:173], s[54:55]\n\tv_mfma_f32_16x16x32_bf16 v[216:219], a[84:87], a[136:139], v[216:219]\n\tv_pk_mul_f32 v[174:175], v[174:175], s[54:55]\n\t" \
    "v_pk_mul_f32 v[188:189], v[188:189], s[54:55]\n\tv_mfma_f32_16x16x32_bf16 v[220:223], a[84:87], a[140:143], v[220:223]\n\tv_pk_mul_f32 v[190:191], v[190:191], s[54:55]\n\t" \
    "v_cvt_pk_bf16_f32 v172, v172, v173\n\tv_mfma_f32_16x16x32_bf16 v[192:195], a[72:75], a[144:147], v[192:195]\n\tv_cvt_pk_bf16_f32 v173, v174, v175\n\t" \
    "v_cvt_pk_bf16_f32 v174, v188, v189\n\tv_mfma_f32_16x16x32_bf16 v[196:199], a[72:75], a[148:151], v[196:199]\n\tv_cvt_pk_bf16_f32 v175, v190, v191\n\t" \
    "ds_write_b128 %[iw], v[172:175] offset:19456\n\tv_mfma_f32_16x16x32_bf16 v[200:203], a[72:75], a[152:155], v[200:203]\n\t" \
    "v_mfma_f32_16x16x32_bf16 v[204:207], a[72:75], a[156:159], v[204:207]\n\tv_mfma_f32_16x16x32_bf16 v[208:211], a[88:91], a[144:147], v[208:211]\n\t" \
    "v_mfma_f32_16x16x32_bf16 v[212:215], a[88:91], a[148:151], v[212:215]\n\tv_mfma_f32_16x16x32_bf16 v[216:219], a[88:91], a[152:155], v[216:219]\n\t" \
    "v_mfma_f32_16x16x32_bf16 v[220:223], a[88:91], a[156:159], v[220:223]\n\tv_mfma_f32_16x16x32_bf16 v[192:195], a[76:79], a[160:163], v[192:195]\n\t" \
    "v_mfma_f32_16x16x32_bf16 v[196:199], a[76:79], a[164:167], v[196:199]\n\tv_mfma_f32_16x16x32_bf16 v[200:203], a[76:79], a[168:171], v[200:203]\n\t" \
    "v_mfma_f32_16x16x32_bf16 v[204:207], a[76:79], a[172:175], v[204:207]\n\tv_mfma_f32_16x16x32_bf16 v[208:211], a[92:95], a[160:163], v[208:211]\n\t" \
    "v_mfma_f32_16x16x32_bf16 v[212:215], a[92:95], a[164:167], v[212:215]\n\tv_mfma_f32_16x16x32_bf16 v[216:219], a[92:95], a[168:171], v[216:219]\n\t" \
    "v_mfma_f32_16x16x32_bf16 v[220:223], a[92:95], a[172:175], v[220:223]\n\ts_waitcnt lgkmcnt(8)\n\ts_barrier\n\t" \
    "v_mfma_f32_16x16x32_bf16 v[224:227], a[0:3], a[112:115], v[224:227]\n\tv_pk_mul_f32 v[192:193], v[192:193], s[54:55]\n\tv_pk_mul_f32 v[194:195], v[194:195], s[54:55]\n\t" \
    "v_mfma_f32_16x16x32_bf16 v[228:231], a[0:3], a[116:119], v[228:231]\n\tv_pk_mul_f32 v[208:209], v[208:209], s[54:55]\n\tv_pk_mul_f32 v[210:211], v[210:211], s[54:55]\n\t" \
    "v_mfma_f32_16x16x32_bf16 v[232:235], a[0:3], a[120:123], v[232:235]\n\tv_cvt_pk_bf16_f32 v192, v192, v193\n\tv_cvt_pk_bf16_f32 v193, v194, v195\n\t" \
    "v_mfma_f32_16x16x32_bf16 v[236:239], a[0:3], a[124:127], v[236:239]\n\tv_cvt_pk_bf16_f32 v194, v208, v209\n\tv_cvt_pk_bf16_f32 v195, v210, v211\n\t" \
    "v_mfma_f32_16x16x32_bf16 v[240:243], a[16:19], a[112:115], v[240:243]\n\tds_write_b128 %[iw], v[192:195] offset:20480\n\tv_pk_mul_f32 v[196:197], v[196:197], s[54:55]\n\t" \
    "v_mfma_f32_16x16x32_bf16 v[244:247], a[16:19], a[116:119], v[244:247]\n\tv_pk_mul_f32 v[198:199], v[198:199], s[54:55]\n\tv_pk_mul_f32 v[212:213], v[212:213], s[54:55]\n\t" \
    "v_mfma_f32_16x16x32_bf16 v[248:251], a[16:19], a[120:123], v[248:251]\n\tv_pk_mul_f32 v[214:215], v[214:215], s[54:55]\n\tv_cvt_pk_bf16_f32 v196, v196, v197\n\t" \
    "v_mfma_f32_16x16x32_bf16 v[252:255], a[16:19], a[124:127], v[252:255]\n\tv_cvt_pk_bf16_f32 v197, v198, v199\n\tv_cvt_pk_bf16_f32 v198, v212, v213\n\t" \
    "v_mfma_f32_16x16x32_bf16 v[224:227], a[4:7], a[128:131], v[224:227]\n\tv_cvt_pk_bf16_f32 v199, v214, v215\n\tds_write_b128 %[iw], v[196:199] offset:21504\n\t" \
    "v_mfma_f32_16x16x32_bf16 v[228:231], a[4:7], a[132:135], v[228:231]\n\tv_pk_mul_f32 v[200:201], v[200:201], s[54:55]\n\tv_pk_mul_f32 v[202:203], v[202:203], s[54:55]\n\t" \
    "v_mfma_f32_16x16x32_bf16 v[232:235], a[4:7], a[136:139], v[232:235]\n\tv_pk_mul_f32 v[216:217], v[216:217], s[54:55]\n\tv_pk_mul_f32 v[218:219], v[218:219], s[54:55]\n\t" \
    "v_mfma_f32_16x16x32_bf16 v[236:239], a[4:7], a[140:143], v[236:239]\n\tv_cvt_pk_bf16_f32 v200, v200, v201\n\tv_cvt_pk_bf16_f32 v201, v202, v203\n\t" \
    "v_mfma_f32_16x16x32_bf16 v[240:243], a[20:23], a[128:131], v[240:243]\n\tv_cvt_pk_bf16_f32 v202, v216, v217\n\tv_cvt_pk_bf16_f32 v203, v218, v219\n\t" \
    "v_mfma_f32_16x16x32_bf16 v[244:247], a[20:23], a[132:135], v[244:247]\n\tds_write_b128 %[iw], v[200:203] offset:22528\n\tv_pk_mul_f32 v[204:205], v[204:205], s[54:55]\n\t" \
    "v_mfma_f32_16x16x32_bf16 v[248:251], a[20:23], a[136:139], v[248:251]\n\tv_pk_mul_f32 v[206:207], v[206:207], s[54:55]\n\tv_pk_mul_f32 v[220:221], v[220:221], s[54:55]\n\t" \
    "v_mfma_f32_16x16x32_bf16 v[252:255], a[20:23], a[140:143], v[252:255]\n\tv_pk_mul_f32 v[222:223], v[222:223], s[54:55]\n\tv_cvt_pk_bf16_f32 v204, v204, v205\n\t" \
    "v_mfma_f32_16x16x32_bf16 v[224:227], a[8:11], a[144:147], v[224:227]\n\tv_cvt_pk_bf16_f32 v205, v206, v207\n\tv_cvt_pk_bf16_f32 v206, v220, v221\n\t" \
    "v_mfma_f32_16x16x32_bf16 v[228:231], a[8:11], a[148:151], v[228:231]\n\tv_cvt_pk_bf16_f32 v207, v222, v223\n\tds_write_b128 %[iw], v[204:207] offset:23552\n\t" \
    "v_mfma_f32_16x16x32_bf16 v[232:235], a[8:11], a[152:155], v[232:235]\n\tv_mfma_f32_16x16x32_bf16 v[236:239], a[8:11], a[156:159], v[236:239]\n\t" \
    "v_mfma_f32_16x16x32_bf16 v[240:243], a[24:27], a[144:147], v[240:243]\n\tv_mfma_f32_16x16x32_bf16 v[244:247], a[24:27], a[148:151], v[244:247]\n\t" \
    "v_mfma_f32_16x16x32_bf16 v[248:251], a[24:27], a[152:155], v[248:251]\n\tv_mfma_f32_16x16x32_bf16 v[252:255], a[24:27], a[156:159], v[252:255]\n\t" \
    "v_mfma_f32_16x16x32_bf16 v[224:227], a[12:15], a[160:163], v[224:227]\n\tv_mfma_f32_16x16x32_bf16 v[228:231], a[12:15], a[164:167], v[228:231]\n\t" \
    "v_mfma_f32_16x16x32_bf16 v[232:235], a[12:15], a[168:171], v[232:235]\n\tv_mfma_f32_16x16x32_bf16 v[236:239], a[12:15], a[172:175], v[236:239]\n\t" \
    "v_mfma_f32_16x16x32_bf16 v[240:243], a[28:31], a[160:163], v[240:243]\n\tv_mfma_f32_16x16x32_bf16 v[244:247], a[28:31], a[164:167], v[244:247]\n\t" \
    "v_mfma_f32_16x16x32_bf16 v[248:251], a[28:31], a[168:171], v[248:251]\n\tv_mfma_f32_16x16x32_bf16 v[252:255], a[28:31], a[172:175], v[252:255]\n\ts_nop 3\n\t" \
    "v_pk_mul_f32 v[224:225], v[224:225], s[54:55]\n\tv_pk_mul_f32 v[226:227], v[226:227], s[54:55]\n\tv_pk_mul_f32 v[240:241], v[240:241], s[54:55]\n\t" \
    "v_pk_mul_f32 v[242:243], v[242:243], s[54:55]\n\tv_cvt_pk_bf16_f32 v224, v224, v225\n\tv_cvt_pk_bf16_f32 v225, v226, v227\n\tv_cvt_pk_bf16_f32 v226, v240, v241\n\t" \
    "v_cvt_pk_bf16_f32 v227, v242, v243\n\tds_write_b128 %[iw], v[224:227] offset:24576\n\tv_pk_mul_f32 v[228:229], v[228:229], s[54:55]\n\t" \
    "v_pk_mul_f32 v[230:231], v[230:231], s[54:55]\n\tv_pk_mul_f32 v[244:245], v[244:245], s[54:55]\n\tv_pk_mul_f32 v[246:247], v[246:247], s[54:55]\n\t" \
    "v_cvt_pk_bf16_f32 v228, v228, v229\n\tv_cvt_pk_bf16_f32 v229, v230, v231\n\tv_cvt_pk_bf16_f32 v230, v244, v245\n\tv_cvt_pk_bf16_f32 v231, v246, v247\n\t" \
    "ds_write_b128 %[iw], v[228:231] offset:25600\n\tv_pk_mul_f32 v[232:233], v[232:233], s[54:55]\n\tv_pk_mul_f32 v[234:235], v[234:235], s[54:55]\n\t" \
    "v_pk_mul_f32 v[248:249], v[248:249], s[54:55]\n\tv_pk_mul_f32 v[250:251], v[250:251], s[54:55]\n\tv_cvt_pk_bf16_f32 v232, v232, v233\n\tv_cvt_pk_bf16_f32 v233, v234, v235\n\t" \
    "v_cvt_pk_bf16_f32 v234, v248, v249\n\tv_cvt_pk_bf16_f32 v235, v250, v251\n\tds_write_b128 %[iw], v[232:235] offset:26624\n\tv_pk_mul_f32 v[236:237], v[236:237], s[54:55]\n\t" \
    "v_pk_mul_f32 v[238:239], v[238:239], s[54:55]\n\tv_pk_mul_f32 v[252:253], v[252:253], s[54:55]\n\tv_pk_mul_f32 v[254:255], v[254:255], s[54:55]\n\t" \
    "v_cvt_pk_bf16_f32 v236, v236, v237\n\tv_cvt_pk_bf16_f32 v237, v238, v239\n\tv_cvt_pk_bf16_f32 v238, v252, v253\n\tv_cvt_pk_bf16_f32 v239, v254, v255\n\t" \
    "ds_write_b128 %[iw], v[236:239] offset:27648\n\t" W0T(6) "s_waitcnt vmcnt(0) lgkmcnt(0)\n\ts_barrier\n\t" W0T(7) ""
#define W0_ASM1 \
    "s_sub_u32 s44, %[sa], 262144\n\ts_mov_b32 s49, 3670016\n\ts_mov_b32 s48, 0\n\ts_add_u32 s45, %[kq], 0\n\ts_and_b32 s45, s45, 3\n\ts_sub_u32 s44, s44, s48\n\t" \
    "s_lshl_b32 s48, s45, 7\n\ts_add_u32 s44, s44, s48\n\ts_mul_i32 s46, s45, 57344\n\ts_add_u32 s46, s46, %[sb]\n\ts_add_u32 m0, %[lm], 57344\n\ts_nop 0\n\t" \
    "buffer_load_dwordx4 %[vb0], %[rb], s46 offen lds\n\ts_add_u32 m0, m0, 4096\n\ts_nop 0\n\tbuffer_load_dwordx4 %[vb1], %[rb], s46 offen lds\n\ts_add_u32 m0, m0, 4096\n\ts_nop 0\n\t" \
    "buffer_load_dwordx4 %[vb2], %[rb], s46 offen lds\n\ts_add_u32 m0, m0, 4096\n\ts_nop 0\n\tbuffer_load_dwordx4 %[vb3], %[rb], s46 offen lds\n\ts_add_u32 m0, %[lm], 0\n\t" \
    "s_add_u32 s44, s44, 262144\n\ts_cmp_ge_u32 s44, s49\n\ts_cselect_b32 s47, s49, 0\n\ts_sub_u32 s44, s44, s47\n\tbuffer_load_dwordx4 %[va], %[ra], s44 offen lds\n\t" \
    "s_add_u32 m0, m0, 4096\n\ts_add_u32 s44, s44, 262144\n\ts_cmp_ge_u32 s44, s49\n\ts_cselect_b32 s47, s49, 0\n\ts_sub_u32 s44, s44, s47\n\t" \
    "buffer_load_dwordx4 %[va], %[ra], s44 offen lds\n\ts_add_u32 m0, m0, 4096\n\ts_add_u32 s44, s44, 262144\n\ts_cmp_ge_u32 s44, s49\n\ts_cselect_b32 s47, s49, 0\n\t" \
    "s_sub_u32 s44, s44, s47\n\tbuffer_load_dwordx4 %[va], %[ra], s44 offen lds\n\ts_add_u32 m0, m0, 4096\n\ts_add_u32 s44, s44, 262144\n\ts_cmp_ge_u32 s44, s49\n\t" \
    "s_cselect_b32 s47, s49, 0\n\ts_sub_u32 s44, s44, s47\n\tbuffer_load_dwordx4 %[va], %[ra], s44 offen lds\n\ts_add_u32 m0, m0, 4096\n\ts_add_u32 s44, s44, 262144\n\t" \
    "s_cmp_ge_u32 s44, s49\n\ts_cselect_b32 s47, s49, 0\n\ts_sub_u32 s44, s44, s47\n\tbuffer_load_dwordx4 %[va], %[ra], s44 offen lds\n\ts_add_u32 m0, m0, 4096\n\t" \
    "s_add_u32 s44, s44, 262144\n\ts_cmp_ge_u32 s44, s49\n\ts_cselect_b32 s47, s49, 0\n\ts_sub_u32 s44, s44, s47\n\tbuffer_load_dwordx4 %[va], %[ra], s44 offen lds\n\t" \
    "s_add_u32 m0, m0, 4096\n\ts_add_u32 s44, s44, 262144\n\ts_cmp_ge_u32 s44, s49\n\ts_cselect_b32 s47, s49, 0\n\ts_sub_u32 s44, s44, s47\n\t" \
    "buffer_load_dwordx4 %[va], %[ra], s44 offen lds\n\ts_add_u32 m0, m0, 4096\n\ts_add_u32 s44, s44, 262144\n\ts_cmp_ge_u32 s44, s49\n\ts_cselect_b32 s47, s49, 0\n\t" \
    "s_sub_u32 s44, s44, s47\n\tbuffer_load_dwordx4 %[va], %[ra], s44 offen lds\n\ts_add_u32 m0, m0, 4096\n\ts_add_u32 s44, s44, 262144\n\ts_cmp_ge_u32 s44, s49\n\t" \
    "s_cselect_b32 s47, s49, 0\n\ts_sub_u32 s44, s44, s47\n\tbuffer_load_dwordx4 %[va], %[ra], s44 offen lds\n\ts_add_u32 m0, m0, 4096\n\ts_add_u32 s44, s44, 262144\n\t" \
    "s_cmp_ge_u32 s44, s49\n\ts_cselect_b32 s47, s49, 0\n\ts_sub_u32 s44, s44, s47\n\tbuffer_load_dwordx4 %[va], %[ra], s44 offen lds\n\ts_add_u32 m0, m0, 4096\n\t" \
    "s_add_u32 s44, s44, 262144\n\ts_cmp_ge_u32 s44, s49\n\ts_cselect_b32 s47, s49, 0\n\ts_sub_u32 s44, s44, s47\n\tbuffer_load_dwordx4 %[va], %[ra], s44 offen lds\n\t" \
    "s_add_u32 m0, m0, 4096\n\ts_add_u32 s44, s44, 262144\n\ts_cmp_ge_u32 s44, s49\n\ts_cselect_b32 s47, s49, 0\n\ts_sub_u32 s44, s44, s47\n\t" \
    "buffer_load_dwordx4 %[va], %[ra], s44 offen lds\n\ts_add_u32 m0, m0, 4096\n\ts_add_u32 s44, s44, 262144\n\ts_cmp_ge_u32 s44, s49\n\ts_cselect_b32 s47, s49, 0\n\t" \
    "s_sub_u32 s44, s44, s47\n\tbuffer_load_dwordx4 %[va], %[ra], s44 offen lds\n\ts_add_u32 m0, m0, 4096\n\ts_add_u32 s44, s44, 262144\n\ts_cmp_ge_u32 s44, s49\n\t" \
    "s_cselect_b32 s47, s49, 0\n\ts_sub_u32 s44, s44, s47\n\tbuffer_load_dwordx4 %[va], %[ra], s44 offen lds\n\ts_add_u32 s45, %[kq], 1\n\ts_and_b32 s45, s45, 3\n\t" \
    "s_sub_u32 s44, s44, s48\n\ts_lshl_b32 s48, s45, 7\n\ts_add_u32 s44, s44, s48\n\ts_mul_i32 s46, s45, 57344\n\ts_add_u32 s46, s46, %[sb]\n\ts_add_u32 m0, %[lm], 131072\n\ts_nop 0\n\t" \
    "buffer_load_dwordx4 %[vb0], %[rb], s46 offen lds\n\ts_add_u32 m0, m0, 4096\n\ts_nop 0\n\tbuffer_load_dwordx4 %[vb1], %[rb], s46 offen lds\n\ts_add_u32 m0, m0, 4096\n\ts_nop 0\n\t" \
    "buffer_load_dwordx4 %[vb2], %[rb], s46 offen lds\n\ts_add_u32 m0, m0, 4096\n\ts_nop 0\n\tbuffer_load_dwordx4 %[vb3], %[rb], s46 offen lds\n\ts_add_u32 m0, %[lm], 73728\n\t" \
    "s_add_u32 s44, s44, 262144\n\ts_cmp_ge_u32 s44, s49\n\ts_cselect_b32 s47, s49, 0\n\ts_sub_u32 s44, s44, s47\n\tbuffer_load_dwordx4 %[va], %[ra], s44 offen lds\n\t" \
    "s_add_u32 m0, m0, 4096\n\ts_add_u32 s44, s44, 262144\n\ts_cmp_ge_u32 s44, s49\n\ts_cselect_b32 s47, s49, 0\n\ts_sub_u32 s44, s44, s47\n\t" \
    "buffer_load_dwordx4 %[va], %[ra], s44 offen lds\n\ts_add_u32 m0, m0, 4096\n\ts_add_u32 s44, s44, 262144\n\ts_cmp_ge_u32 s44, s49\n\ts_cselect_b32 s47, s49, 0\n\t" \
    "s_sub_u32 s44, s44, s47\n\tbuffer_load_dwordx4 %[va], %[ra], s44 offen lds\n\ts_add_u32 m0, m0, 4096\n\ts_add_u32 s44, s44, 262144\n\ts_cmp_ge_u32 s44, s49\n\t" \
    "s_cselect_b32 s47, s49, 0\n\ts_sub_u32 s44, s44, s47\n\tbuffer_load_dwordx4 %[va], %[ra], s44 offen lds\n\ts_add_u32 m0, m0, 4096\n\ts_add_u32 s44, s44, 262144\n\t" \
    "s_cmp_ge_u32 s44, s49\n\ts_cselect_b32 s47, s49, 0\n\ts_sub_u32 s44, s44, s47\n\tbuffer_load_dwordx4 %[va], %[ra], s44 offen lds\n\ts_add_u32 m0, m0, 4096\n\t" \
    "s_add_u32 s44, s44, 262144\n\ts_cmp_ge_u32 s44, s49\n\ts_cselect_b32 s47, s49, 0\n\ts_sub_u32 s44, s44, s47\n\tbuffer_load_dwordx4 %[va], %[ra], s44 offen lds\n\t" \
    "s_add_u32 m0, m0, 4096\n\ts_add_u32 s44, s44, 262144\n\ts_cmp_ge_u32 s44, s49\n\ts_cselect_b32 s47, s49, 0\n\ts_sub_u32 s44, s44, s47\n\t" \
    "buffer_load_dwordx4 %[va], %[ra], s44 offen lds\n\ts_add_u32 m0, m0, 4096\n\ts_add_u32 s44, s44, 262144\n\ts_cmp_ge_u32 s44, s49\n\ts_cselect_b32 s47, s49, 0\n\t" \
    "s_sub_u32 s44, s44, s47\n\tbuffer_load_dwordx4 %[va], %[ra], s44 offen lds\n\ts_add_u32 m0, m0, 4096\n\ts_add_u32 s44, s44, 262144\n\ts_cmp_ge_u32 s44, s49\n\t" \
    "s_cselect_b32 s47, s49, 0\n\ts_sub_u32 s44, s44, s47\n\tbuffer_load_dwordx4 %[va], %[ra], s44 offen lds\n\ts_add_u32 m0, m0, 4096\n\ts_add_u32 s44, s44, 262144\n\t" \
    "s_cmp_ge_u32 s44, s49\n\ts_cselect_b32 s47, s49, 0\n\ts_sub_u32 s44, s44, s47\n\tbuffer_load_dwordx4 %[va], %[ra], s44 offen lds\n\ts_add_u32 m0, m0, 4096\n\t" \
    "s_add_u32 s44, s44, 262144\n\ts_cmp_ge_u32 s44, s49\n\ts_cselect_b32 s47, s49, 0\n\ts_sub_u32 s44, s44, s47\n\tbuffer_load_dwordx4 %[va], %[ra], s44 offen lds\n\t" \
    "s_add_u32 m0, m0, 4096\n\ts_add_u32 s44, s44, 262144\n\ts_cmp_ge_u32 s44, s49\n\ts_cselect_b32 s47, s49, 0\n\ts_sub_u32 s44, s44, s47\n\t" \
    "buffer_load_dwordx4 %[va], %[ra], s44 offen lds\n\ts_add_u32 m0, m0, 4096\n\ts_add_u32 s44, s44, 262144\n\ts_cmp_ge_u32 s44, s49\n\ts_cselect_b32 s47, s49, 0\n\t" \
    "s_sub_u32 s44, s44, s47\n\tbuffer_load_dwordx4 %[va], %[ra], s44 offen lds\n\ts_add_u32 m0, m0, 4096\n\ts_add_u32 s44, s44, 262144\n\ts_cmp_ge_u32 s44, s49\n\t" \
    "s_cselect_b32 s47, s49, 0\n\ts_sub_u32 s44, s44, s47\n\tbuffer_load_dwordx4 %[va], %[ra], s44 offen lds\n\tv_add_u32 v26, 73728, %[r0]\n\tv_add_u32 v27, 73728, %[r1]\n\t" \
    "v_add_u32 v28, 73728, %[q0]\n\t" W0T(0) "s_waitcnt vmcnt(18)\n\ts_barrier\n\t" W0T(1) "ds_read_b128 a[0:3], %[r0] offset:0\n\tds_read_b128 a[8:11], %[r0] offset:2048\n\t" \
    "ds_read_b64_tr_b16 a[112:113], %[q0] offset:0\n\tds_read_b64_tr_b16 a[114:115], %[q0] offset:896\n\tds_read_b64_tr_b16 a[116:117], %[q0] offset:32\n\t" \
    "ds_read_b64_tr_b16 a[118:119], %[q0] offset:928\n\tds_read_b64_tr_b16 a[120:121], %[q0] offset:64\n\tds_read_b64_tr_b16 a[122:123], %[q0] offset:960\n\t" \
    "ds_read_b64_tr_b16 a[124:125], %[q0] offset:96\n\tds_read_b64_tr_b16 a[126:127], %[q0] offset:992\n\tds_read_b128 a[4:7], %[r1] offset:0\n\t" \
    "ds_read_b128 a[12:15], %[r1] offset:2048\n\tds_read_b64_tr_b16 a[128:129], %[q0] offset:7680\n\tds_read_b64_tr_b16 a[130:131], %[q0] offset:8576\n\ts_waitcnt lgkmcnt(4)\n\t" \
    "v_mfma_f32_16x16x32_bf16 v[32:35], a[0:3], a[112:115], 0\n\tds_read_b64_tr_b16 a[132:133], %[q0] offset:7712\n\tv_mfma_f32_16x16x32_bf16 v[36:39], a[0:3], a[116:119], 0\n\t" \
    "ds_read_b64_tr_b16 a[134:135], %[q0] offset:8608\n\tv_mfma_f32_16x16x32_bf16 v[40:43], a[0:3], a[120:123], 0\n\tds_read_b64_tr_b16 a[136:137], %[q0] offset:7744\n\t" \
    "v_mfma_f32_16x16x32_bf16 v[44:47], a[0:3], a[124:127], 0\n\tds_read_b64_tr_b16 a[138:139], %[q0] offset:8640\n\tv_mfma_f32_16x16x32_bf16 v[48:51], a[8:11], a[112:115], 0\n\t" \
    "ds_read_b64_tr_b16 a[140:141], %[q0] offset:7776\n\tv_mfma_f32_16x16x32_bf16 v[52:55], a[8:11], a[116:119], 0\n\tds_read_b64_tr_b16 a[142:143], %[q0] offset:8672\n\t" \
    "v_mfma_f32_16x16x32_bf16 v[56:59], a[8:11], a[120:123], 0\n\tds_read_b128 a[16:19], %[r0] offset:4096\n\tv_mfma_f32_16x16x32_bf16 v[60:63], a[8:11], a[124:127], 0\n\t" \
    "ds_read_b128 a[24:27], %[r0] offset:6144\n\ts_waitcnt lgkmcnt(2)\n\tv_mfma_f32_16x16x32_bf16 v[32:35], a[4:7], a[128:131], v[32:35]\n\tds_read_b128 a[20:23], %[r1] offset:4096\n\t" \
    "v_mfma_f32_16x16x32_bf16 v[36:39], a[4:7], a[132:135], v[36:39]\n\tds_read_b128 a[28:31], %[r1] offset:6144\n\tv_mfma_f32_16x16x32_bf16 v[40:43], a[4:7], a[136:139], v[40:43]\n\t" \
    "ds_read_b128 a[32:35], %[r0] offset:8192\n\tv_mfma_f32_16x16x32_bf16 v[44:47], a[4:7], a[140:143], v[44:47]\n\tds_read_b128 a[40:43], %[r0] offset:10240\n\t" \
    "v_mfma_f32_16x16x32_bf16 v[48:51], a[12:15], a[128:131], v[48:51]\n\tds_read_b128 a[36:39], %[r1] offset:8192\n\t" \
    "v_mfma_f32_16x16x32_bf16 v[52:55], a[12:15], a[132:135], v[52:55]\n\tds_read_b128 a[44:47], %[r1] offset:10240\n\t" \
    "v_mfma_f32_16x16x32_bf16 v[56:59], a[12:15], a[136:139], v[56:59]\n\tds_read_b128 a[48:51], %[r0] offset:12288\n\t" \
    "v_mfma_f32_16x16x32_bf16 v[60:63], a[12:15], a[140:143], v[60:63]\n\tds_read_b128 a[56:59], %[r0] offset:14336\n\ts_waitcnt lgkmcnt(8)\n\t" \
    "v_mfma_f32_16x16x32_bf16 v[64:67], a[16:19], a[112:115], 0\n\tds_read_b128 a[52:55], %[r1] offset:12288\n\tv_mfma_f32_16x16x32_bf16 v[68:71], a[16:19], a[116:119], 0\n\t" \
    "ds_read_b128 a[60:63], %[r1] offset:14336\n\tv_mfma_f32_16x16x32_bf16 v[72:75], a[16:19], a[120:123], 0\n\tds_read_b128 a[64:67], %[r0] offset:16384\n\t" \
    "v_mfma_f32_16x16x32_bf16 v[76:79], a[16:19], a[124:127], 0\n\tds_read_b128 a[72:75], %[r0] offset:18432\n\tv_mfma_f32_16x16x32_bf16 v[80:83], a[24:27], a[112:115], 0\n\t" \
    "ds_read_b128 a[68:71], %[r1] offset:16384\n\tv_mfma_f32_16x16x32_bf16 v[84:87], a[24:27], a[116:119], 0\n\tds_read_b128 a[76:79], %[r1] offset:18432\n\t" \
    "v_mfma_f32_16x16x32_bf16 v[88:91], a[24:27], a[120:123], 0\n\tds_read_b128 a[80:83], %[r0] offset:20480\n\tv_mfma_f32_16x16x32_bf16 v[92:95], a[24:27], a[124:127], 0\n\t" \
    "ds_read_b128 a[88:91], %[r0] offset:22528\n\ts_waitcnt lgkmcnt(12)\n\tv_mfma_f32_16x16x32_bf16 v[64:67], a[20:23], a[128:131], v[64:67]\n\t" \
    "ds_read_b128 a[84:87], %[r1] offset:20480\n\tv_mfma_f32_16x16x32_bf16 v[68:71], a[20:23], a[132:135], v[68:71]\n\tds_read_b128 a[92:95], %[r1] offset:22528\n\t" \
    "v_mfma_f32_16x16x32_bf16 v[72:75], a[20:23], a[136:139], v[72:75]\n\tds_read_b128 a[96:99], %[r0] offset:24576\n\t" \
    "v_mfma_f32_16x16x32_bf16 v[76:79], a[20:23], a[140:143], v[76:79]\n\tds_read_b128 a[104:107], %[r0] offset:26624\n\t" \
    "v_mfma_f32_16x16x32_bf16 v[80:83], a[28:31], a[128:131], v[80:83]\n\tds_read_b128 a[100:103], %[r1] offset:24576\n\t" \
    "v_mfma_f32_16x16x32_bf16 v[84:87], a[28:31], a[132:135], v[84:87]\n\tds_read_b128 a[108:111], %[r1] offset:26624\n\t" \
    "v_mfma_f32_16x16x32_bf16 v[88:91], a[28:31], a[136:139], v[88:91]\n\tv_mfma_f32_16x16x32_bf16 v[92:95], a[28:31], a[140:143], v[92:95]\n\ts_waitcnt lgkmcnt(12)\n\t" \
    "v_mfma_f32_16x16x32_bf16 v[96:99], a[32:35], a[112:115], 0\n\tv_mfma_f32_16x16x32_bf16 v[100:103], a[32:35], a[116:119], 0\n\t" \
    "v_mfma_f32_16x16x32_bf16 v[104:107], a[32:35], a[120:123], 0\n\tv_mfma_f32_16x16x32_bf16 v[108:111], a[32:35], a[124:127], 0\n\t" \
    "v_mfma_f32_16x16x32_bf16 v[112:115], a[40:43], a[112:115], 0\n\tv_mfma_f32_16x16x32_bf16 v[116:119], a[40:43], a[116:119], 0\n\t" \
    "v_mfma_f32_16x16x32_bf16 v[120:123], a[40:43], a[120:123], 0\n\tv_mfma_f32_16x16x32_bf16 v[124:127], a[40:43], a[124:127], 0\n\ts_waitcnt lgkmcnt(0)\n\ts_barrier\n\t" \
    "v_mfma_f32_16x16x32_bf16 v[96:99], a[36:39], a[128:131], v[96:99]\n\tv_mfma_f32_16x16x32_bf16 v[100:103], a[36:39], a[132:135], v[100:103]\n\t" \
    "buffer_load_dword v30, %[bo], %[ws], 0 offen sc1\n\tv_mfma_f32_16x16x32_bf16 v[104:107], a[36:39], a[136:139], v[104:107]\n\t" \
    "v_mfma_f32_16x16x32_bf16 v[108:111], a[36:39], a[140:143], v[108:111]\n\tv_mfma_f32_16x16x32_bf16 v[112:115], a[44:47], a[128:131], v[112:115]\n\ts_add_u32 s45, %[kq], 2\n\t" \
    "s_and_b32 s45, s45, 3\n\ts_sub_u32 s44, s44, s48\n\ts_lshl_b32 s48, s45, 7\n\ts_add_u32 s44, s44, s48\n\ts_mul_i32 s46, s45, 57344\n\ts_add_u32 s46, s46, %[sb]\n\t" \
    "s_add_u32 m0, %[lm], 57344\n\ts_nop 0\n\tbuffer_load_dwordx4 %[vb0], %[rb], s46 offen lds\n\tv_mfma_f32_16x16x32_bf16 v[116:119], a[44:47], a[132:135], v[116:119]\n\t" \
    "v_mfma_f32_16x16x32_bf16 v[120:123], a[44:47], a[136:139], v[120:123]\n\tv_mfma_f32_16x16x32_bf16 v[124:127], a[44:47], a[140:143], v[124:127]\n\ts_add_u32 m0, m0, 4096\n\t" \
    "s_nop 0\n\tbuffer_load_dwordx4 %[vb1], %[rb], s46 offen lds\n\tv_mfma_f32_16x16x32_bf16 v[128:131], a[48:51], a[112:115], 0\n\t" \
    "v_mfma_f32_16x16x32_bf16 v[132:135], a[48:51], a[116:119], 0\n\tv_mfma_f32_16x16x32_bf16 v[136:139], a[48:51], a[120:123], 0\n\ts_add_u32 m0, m0, 4096\n\ts_nop 0\n\t" \
    "buffer_load_dwordx4 %[vb2], %[rb], s46 offen lds\n\tv_mfma_f32_16x16x32_bf16 v[140:143], a[48:51], a[124:127], 0\n\tv_mfma_f32_16x16x32_bf16 v[144:147], a[56:59], a[112:115], 0\n\t" \
    "v_mfma_f32_16x16x32_bf16 v[148:151], a[56:59], a[116:119], 0\n\ts_add_u32 m0, m0, 4096\n\ts_nop 0\n\tbuffer_load_dwordx4 %[vb3], %[rb], s46 offen lds\n\t" \
    "v_mfma_f32_16x16x32_bf16 v[152:155], a[56:59], a[120:123], 0\n\tv_mfma_f32_16x16x32_bf16 v[156:159], a[56:59], a[124:127], 0\n\t" \
    "v_mfma_f32_16x16x32_bf16 v[128:131], a[52:55], a[128:131], v[128:131]\n\ts_add_u32 m0, %[lm], 0\n\ts_add_u32 s44, s44, 262144\n\ts_cmp_ge_u32 s44, s49\n\t" \
    "s_cselect_b32 s47, s49, 0\n\ts_sub_u32 s44, s44, s47\n\tbuffer_load_dwordx4 %[va], %[ra], s44 offen lds\n\tv_mfma_f32_16x16x32_bf16 v[132:135], a[52:55], a[132:135], v[132:135]\n\t" \
    "v_mfma_f32_16x16x32_bf16 v[136:139], a[52:55], a[136:139], v[136:139]\n\tv_mfma_f32_16x16x32_bf16 v[140:143], a[52:55], a[140:143], v[140:143]\n\ts_add_u32 m0, m0, 4096\n\t" \
    "s_add_u32 s44, s44, 262144\n\ts_cmp_ge_u32 s44, s49\n\ts_cselect_b32 s47, s49, 0\n\ts_sub_u32 s44, s44, s47\n\tbuffer_load_dwordx4 %[va], %[ra], s44 offen lds\n\t" \
    "v_mfma_f32_16x16x32_bf16 v[144:147], a[60:63], a[128:131], v[144:147]\n\tv_mfma_f32_16x16x32_bf16 v[148:151], a[60:63], a[132:135], v[148:151]\n\t" \
    "v_mfma_f32_16x16x32_bf16 v[152:155], a[60:63], a[136:139], v[152:155]\n\ts_add_u32 m0, m0, 4096\n\ts_add_u32 s44, s44, 262144\n\ts_cmp_ge_u32 s44, s49\n\t" \
    "s_cselect_b32 s47, s49, 0\n\ts_sub_u32 s44, s44, s47\n\tbuffer_load_dwordx4 %[va], %[ra], s44 offen lds\n\tv_mfma_f32_16x16x32_bf16 v[156:159], a[60:63], a[140:143], v[156:159]\n\t" \
    "v_mfma_f32_16x16x32_bf16 v[160:163], a[64:67], a[112:115], 0\n\tv_mfma_f32_16x16x32_bf16 v[164:167], a[64:67], a[116:119], 0\n\ts_add_u32 m0, m0, 4096\n\t" \
    "s_add_u32 s44, s44, 262144\n\ts_cmp_ge_u32 s44, s49\n\ts_cselect_b32 s47, s49, 0\n\ts_sub_u32 s44, s44, s47\n\tbuffer_load_dwordx4 %[va], %[ra], s44 offen lds\n\t" \
    "v_mfma_f32_16x16x32_bf16 v[168:171], a[64:67], a[120:123], 0\n\tv_mfma_f32_16x16x32_bf16 v[172:175], a[64:67], a[124:127], 0\n\t" \
    "v_mfma_f32_16x16x32_bf16 v[176:179], a[72:75], a[112:115], 0\n\ts_add_u32 m0, m0, 4096\n\ts_add_u32 s44, s44, 262144\n\ts_cmp_ge_u32 s44, s49\n\ts_cselect_b32 s47, s49, 0\n\t" \
    "s_sub_u32 s44, s44, s47\n\tbuffer_load_dwordx4 %[va], %[ra], s44 offen lds\n\tv_mfma_f32_16x16x32_bf16 v[180:183], a[72:75], a[116:119], 0\n\t" \
    "v_mfma_f32_16x16x32_bf16 v[184:187], a[72:75], a[120:123], 0\n\tv_mfma_f32_16x16x32_bf16 v[188:191], a[72:75], a[124:127], 0\n\ts_add_u32 m0, m0, 4096\n\t" \
    "s_add_u32 s44, s44, 262144\n\ts_cmp_ge_u32 s44, s49\n\ts_cselect_b32 s47, s49, 0\n\ts_sub_u32 s44, s44, s47\n\tbuffer_load_dwordx4 %[va], %[ra], s44 offen lds\n\t" \
    "v_mfma_f32_16x16x32_bf16 v[160:163], a[68:71], a[128:131], v[160:163]\n\tv_mfma_f32_16x16x32_bf16 v[164:167], a[68:71], a[132:135], v[164:167]\n\t" \
    "v_mfma_f32_16x16x32_bf16 v[168:171], a[68:71], a[136:139], v[168:171]\n\ts_add_u32 m0, m0, 4096\n\ts_add_u32 s44, s44, 262144\n\ts_cmp_ge_u32 s44, s49\n\t" \
    "s_cselect_b32 s47, s49, 0\n\ts_sub_u32 s44, s44, s47\n\tbuffer_load_dwordx4 %[va], %[ra], s44 offen lds\n\tv_mfma_f32_16x16x32_bf16 v[172:175], a[68:71], a[140:143], v[172:175]\n\t" \
    "v_mfma_f32_16x16x32_bf16 v[176:179], a[76:79], a[128:131], v[176:179]\n\tv_mfma_f32_16x16x32_bf16 v[180:183], a[76:79], a[132:135], v[180:183]\n\ts_add_u32 m0, m0, 4096\n\t" \
    "s_add_u32 s44, s44, 262144\n\ts_cmp_ge_u32 s44, s49\n\ts_cselect_b32 s47, s49, 0\n\ts_sub_u32 s44, s44, s47\n\tbuffer_load_dwordx4 %[va], %[ra], s44 offen lds\n\t" \
    "v_mfma_f32_16x16x32_bf16 v[184:187], a[76:79], a[136:139], v[184:187]\n\tv_mfma_f32_16x16x32_bf16 v[188:191], a[76:79], a[140:143], v[188:191]\n\t" \
    "v_mfma_f32_16x16x32_bf16 v[192:195], a[80:83], a[112:115], 0\n\ts_add_u32 m0, m0, 4096\n\ts_add_u32 s44, s44, 262144\n\ts_cmp_ge_u32 s44, s49\n\ts_cselect_b32 s47, s49, 0\n\t" \
    "s_sub_u32 s44, s44, s47\n\tbuffer_load_dwordx4 %[va], %[ra], s44 offen lds\n\tv_mfma_f32_16x16x32_bf16 v[196:199], a[80:83], a[116:119], 0\n\t" \
    "v_mfma_f32_16x16x32_bf16 v[200:203], a[80:83], a[120:123], 0\n\tv_mfma_f32_16x16x32_bf16 v[204:207], a[80:83], a[124:127], 0\n\ts_add_u32 m0, m0, 4096\n\t" \
    "s_add_u32 s44, s44, 262144\n\ts_cmp_ge_u32 s44, s49\n\ts_cselect_b32 s47, s49, 0\n\ts_sub_u32 s44, s44, s47\n\tbuffer_load_dwordx4 %[va], %[ra], s44 offen lds\n\t" \
    "v_mfma_f32_16x16x32_bf16 v[208:211], a[88:91], a[112:115], 0\n\tv_mfma_f32_16x16x32_bf16 v[212:215], a[88:91], a[116:119], 0\n\t" \
    "v_mfma_f32_16x16x32_bf16 v[216:219], a[88:91], a[120:123], 0\n\ts_add_u32 m0, m0, 4096\n\ts_add_u32 s44, s44, 262144\n\ts_cmp_ge_u32 s44, s49\n\ts_cselect_b32 s47, s49, 0\n\t" \
    "s_sub_u32 s44, s44, s47\n\tbuffer_load_dwordx4 %[va], %[ra], s44 offen lds\n\tv_mfma_f32_16x16x32_bf16 v[220:223], a[88:91], a[124:127], 0\n\t" \
    "v_mfma_f32_16x16x32_bf16 v[192:195], a[84:87], a[128:131], v[192:195]\n\tv_mfma_f32_16x16x32_bf16 v[196:199], a[84:87], a[132:135], v[196:199]\n\ts_add_u32 m0, m0, 4096\n\t" \
    "s_add_u32 s44, s44, 262144\n\ts_cmp_ge_u32 s44, s49\n\ts_cselect_b32 s47, s49, 0\n\ts_sub_u32 s44, s44, s47\n\tbuffer_load_dwordx4 %[va], %[ra], s44 offen lds\n\t" \
    "v_mfma_f32_16x16x32_bf16 v[200:203], a[84:87], a[136:139], v[200:203]\n\tv_mfma_f32_16x16x32_bf16 v[204:207], a[84:87], a[140:143], v[204:207]\n\t" \
    "v_mfma_f32_16x16x32_bf16 v[208:211], a[92:95], a[128:131], v[208:211]\n\ts_add_u32 m0, m0, 4096\n\ts_add_u32 s44, s44, 262144\n\ts_cmp_ge_u32 s44, s49\n\t" \
    "s_cselect_b32 s47, s49, 0\n\ts_sub_u32 s44, s44, s47\n\tbuffer_load_dwordx4 %[va], %[ra], s44 offen lds\n\tv_mfma_f32_16x16x32_bf16 v[212:215], a[92:95], a[132:135], v[212:215]\n\t" \
    "v_mfma_f32_16x16x32_bf16 v[216:219], a[92:95], a[136:139], v[216:219]\n\tv_mfma_f32_16x16x32_bf16 v[220:223], a[92:95], a[140:143], v[220:223]\n\ts_add_u32 m0, m0, 4096\n\t" \
    "s_add_u32 s44, s44, 262144\n\ts_cmp_ge_u32 s44, s49\n\ts_cselect_b32 s47, s49, 0\n\ts_sub_u32 s44, s44, s47\n\tbuffer_load_dwordx4 %[va], %[ra], s44 offen lds\n\t" \
    "v_mfma_f32_16x16x32_bf16 v[224:227], a[96:99], a[112:115], 0\n\tv_mfma_f32_16x16x32_bf16 v[228:231], a[96:99], a[116:119], 0\n\t" \
    "v_mfma_f32_16x16x32_bf16 v[232:235], a[96:99], a[120:123], 0\n\tv_mfma_f32_16x16x32_bf16 v[236:239], a[96:99], a[124:127], 0\n\t" \
    "v_mfma_f32_16x16x32_bf16 v[240:243], a[104:107], a[112:115], 0\n\tv_mfma_f32_16x16x32_bf16 v[244:247], a[104:107], a[116:119], 0\n\t" \
    "v_mfma_f32_16x16x32_bf16 v[248:251], a[104:107], a[120:123], 0\n\tv_mfma_f32_16x16x32_bf16 v[252:255], a[104:107], a[124:127], 0\n\t" \
    "v_mfma_f32_16x16x32_bf16 v[224:227], a[100:103], a[128:131], v[224:227]\n\tv_mfma_f32_16x16x32_bf16 v[228:231], a[100:103], a[132:135], v[228:231]\n\t" \
    "v_mfma_f32_16x16x32_bf16 v[232:235], a[100:103], a[136:139], v[232:235]\n\tv_mfma_f32_16x16x32_bf16 v[236:239], a[100:103], a[140:143], v[236:239]\n\t" \
    "v_mfma_f32_16x16x32_bf16 v[240:243], a[108:111], a[128:131], v[240:243]\n\tv_mfma_f32_16x16x32_bf16 v[244:247], a[108:111], a[132:135], v[244:247]\n\t" \
    "v_mfma_f32_16x16x32_bf16 v[248:251], a[108:111], a[136:139], v[248:251]\n\tv_mfma_f32_16x16x32_bf16 v[252:255], a[108:111], a[140:143], v[252:255]\n\t" W0T(2) "" \
    "s_waitcnt vmcnt(18)\n\ts_barrier\n\t" W0T(3) "ds_read_b128 a[0:3], v26 offset:0\n\tds_read_b128 a[8:11], v26 offset:2048\n\tds_read_b64_tr_b16 a[112:113], v28 offset:0\n\t" \
    "ds_read_b64_tr_b16 a[114:115], v28 offset:896\n\tds_read_b64_tr_b16 a[116:117], v28 offset:32\n\tds_read_b64_tr_b16 a[118:119], v28 offset:928\n\t" \
    "ds_read_b64_tr_b16 a[120:121], v28 offset:64\n\tds_read_b64_tr_b16 a[122:123], v28 offset:960\n\tds_read_b64_tr_b16 a[124:125], v28 offset:96\n\t" \
    "ds_read_b64_tr_b16 a[126:127], v28 offset:992\n\tds_read_b128 a[4:7], v27 offset:0\n\tds_read_b128 a[12:15], v27 offset:2048\n\tds_read_b64_tr_b16 a[128:129], v28 offset:7680\n\t" \
    "ds_read_b64_tr_b16 a[130:131], v28 offset:8576\n\ts_waitcnt lgkmcnt(4)\n\tv_mfma_f32_16x16x32_bf16 v[32:35], a[0:3], a[112:115], v[32:35]\n\t" \
    "ds_read_b64_tr_b16 a[132:133], v28 offset:7712\n\tv_mfma_f32_16x16x32_bf16 v[36:39], a[0:3], a[116:119], v[36:39]\n\tds_read_b64_tr_b16 a[134:135], v28 offset:8608\n\t" \
    "v_mfma_f32_16x16x32_bf16 v[40:43], a[0:3], a[120:123], v[40:43]\n\tds_read_b64_tr_b16 a[136:137], v28 offset:7744\n\t" \
    "v_mfma_f32_16x16x32_bf16 v[44:47], a[0:3], a[124:127], v[44:47]\n\tds_read_b64_tr_b16 a[138:139], v28 offset:8640\n\t" \
    "v_mfma_f32_16x16x32_bf16 v[48:51], a[8:11], a[112:115], v[48:51]\n\tds_read_b64_tr_b16 a[140:141], v28 offset:7776\n\t" \
    "v_mfma_f32_16x16x32_bf16 v[52:55], a[8:11], a[116:119], v[52:55]\n\tds_read_b64_tr_b16 a[142:143], v28 offset:8672\n\t" \
    "v_mfma_f32_16x16x32_bf16 v[56:59], a[8:11], a[120:123], v[56:59]\n\tds_read_b128 a[16:19], v26 offset:4096\n\tv_mfma_f32_16x16x32_bf16 v[60:63], a[8:11], a[124:127], v[60:63]\n\t" \
    "ds_read_b128 a[24:27], v26 offset:6144\n\ts_waitcnt lgkmcnt(2)\n\tv_mfma_f32_16x16x32_bf16 v[32:35], a[4:7], a[128:131], v[32:35]\n\tds_read_b128 a[20:23], v27 offset:4096\n\t" \
    "v_mfma_f32_16x16x32_bf16 v[36:39], a[4:7], a[132:135], v[36:39]\n\tds_read_b128 a[28:31], v27 offset:6144\n\tv_mfma_f32_16x16x32_bf16 v[40:43], a[4:7], a[136:139], v[40:43]\n\t" \
    "ds_read_b128 a[32:35], v26 offset:8192\n\tv_mfma_f32_16x16x32_bf16 v[44:47], a[4:7], a[140:143], v[44:47]\n\tds_read_b128 a[40:43], v26 offset:10240\n\t" \
    "v_mfma_f32_16x16x32_bf16 v[48:51], a[12:15], a[128:131], v[48:51]\n\tds_read_b128 a[36:39], v27 offset:8192\n\tv_mfma_f32_16x16x32_bf16 v[52:55], a[12:15], a[132:135], v[52:55]\n\t" \
    "ds_read_b128 a[44:47], v27 offset:10240\n\tv_mfma_f32_16x16x32_bf16 v[56:59], a[12:15], a[136:139], v[56:59]\n\tds_read_b128 a[48:51], v26 offset:12288\n\t" \
    "v_mfma_f32_16x16x32_bf16 v[60:63], a[12:15], a[140:143], v[60:63]\n\tds_read_b128 a[56:59], v26 offset:14336\n\ts_waitcnt lgkmcnt(8)\n\t" \
    "v_mfma_f32_16x16x32_bf16 v[64:67], a[16:19], a[112:115], v[64:67]\n\tds_read_b128 a[52:55], v27 offset:12288\n\t" \
    "v_mfma_f32_16x16x32_bf16 v[68:71], a[16:19], a[116:119], v[68:71]\n\tds_read_b128 a[60:63], v27 offset:14336\n\t" \
    "v_mfma_f32_16x16x32_bf16 v[72:75], a[16:19], a[120:123], v[72:75]\n\tds_read_b128 a[64:67], v26 offset:16384\n\t" \
    "v_mfma_f32_16x16x32_bf16 v[76:79], a[16:19], a[124:127], v[76:79]\n\tds_read_b128 a[72:75], v26 offset:18432\n\t" \
    "v_mfma_f32_16x16x32_bf16 v[80:83], a[24:27], a[112:115], v[80:83]\n\tds_read_b128 a[68:71], v27 offset:16384\n\t" \
    "v_mfma_f32_16x16x32_bf16 v[84:87], a[24:27], a[116:119], v[84:87]\n\tds_read_b128 a[76:79], v27 offset:18432\n\t" \
    "v_mfma_f32_16x16x32_bf16 v[88:91], a[24:27], a[120:123], v[88:91]\n\tds_read_b128 a[80:83], v26 offset:20480\n\t" \
    "v_mfma_f32_16x16x32_bf16 v[92:95], a[24:27], a[124:127], v[92:95]\n\tds_read_b128 a[88:91], v26 offset:22528\n\ts_waitcnt lgkmcnt(12)\n\t" \
    "v_mfma_f32_16x16x32_bf16 v[64:67], a[20:23], a[128:131], v[64:67]\n\tds_read_b128 a[84:87], v27 offset:20480\n\t" \
    "v_mfma_f32_16x16x32_bf16 v[68:71], a[20:23], a[132:135], v[68:71]\n\tds_read_b128 a[92:95], v27 offset:22528\n\t" \
    "v_mfma_f32_16x16x32_bf16 v[72:75], a[20:23], a[136:139], v[72:75]\n\tds_read_b128 a[96:99], v26 offset:24576\n\t" \
    "v_mfma_f32_16x16x32_bf16 v[76:79], a[20:23], a[140:143], v[76:79]\n\tds_read_b128 a[104:107], v26 offset:26624\n\t" \
    "v_mfma_f32_16x16x32_bf16 v[80:83], a[28:31], a[128:131], v[80:83]\n\tds_read_b128 a[100:103], v27 offset:24576\n\t" \
    "v_mfma_f32_16x16x32_bf16 v[84:87], a[28:31], a[132:135], v[84:87]\n\tds_read_b128 a[108:111], v27 offset:26624\n\t" \
    "v_mfma_f32_16x16x32_bf16 v[88:91], a[28:31], a[136:139], v[88:91]\n\tv_mfma_f32_16x16x32_bf16 v[92:95], a[28:31], a[140:143], v[92:95]\n\ts_waitcnt lgkmcnt(12)\n\t" \
    "v_mfma_f32_16x16x32_bf16 v[96:99], a[32:35], a[112:115], v[96:99]\n\tv_mfma_f32_16x16x32_bf16 v[100:103], a[32:35], a[116:119], v[100:103]\n\t" \
    "v_mfma_f32_16x16x32_bf16 v[104:107], a[32:35], a[120:123], v[104:107]\n\tv_mfma_f32_16x16x32_bf16 v[108:111], a[32:35], a[124:127], v[108:111]\n\t" \
    "v_mfma_f32_16x16x32_bf16 v[112:115], a[40:43], a[112:115], v[112:115]\n\tv_mfma_f32_16x16x32_bf16 v[116:119], a[40:43], a[116:119], v[116:119]\n\t" \
    "v_mfma_f32_16x16x32_bf16 v[120:123], a[40:43], a[120:123], v[120:123]\n\tv_mfma_f32_16x16x32_bf16 v[124:127], a[40:43], a[124:127], v[124:127]\n\ts_waitcnt lgkmcnt(0)\n\t" \
    "s_barrier\n\tv_mfma_f32_16x16x32_bf16 v[96:99], a[36:39], a[128:131], v[96:99]\n\tv_mfma_f32_16x16x32_bf16 v[100:103], a[36:39], a[132:135], v[100:103]\n\t" \
    "s_add_u32 s45, %[kq], 3\n\ts_and_b32 s45, s45, 3\n\ts_sub_u32 s44, s44, s48\n\ts_lshl_b32 s48, s45, 7\n\ts_add_u32 s44, s44, s48\n\ts_mul_i32 s46, s45, 57344\n\t" \
    "s_add_u32 s46, s46, %[sb]\n\ts_add_u32 m0, %[lm], 131072\n\ts_nop 0\n\tbuffer_load_dwordx4 %[vb0], %[rb], s46 offen lds\n\t" \
    "v_mfma_f32_16x16x32_bf16 v[104:107], a[36:39], a[136:139], v[104:107]\n\tv_mfma_f32_16x16x32_bf16 v[108:111], a[36:39], a[140:143], v[108:111]\n\ts_add_u32 m0, m0, 4096\n\t" \
    "s_nop 0\n\tbuffer_load_dwordx4 %[vb1], %[rb], s46 offen lds\n\tv_mfma_f32_16x16x32_bf16 v[112:115], a[44:47], a[128:131], v[112:115]\n\t" \
    "v_mfma_f32_16x16x32_bf16 v[116:119], a[44:47], a[132:135], v[116:119]\n\ts_add_u32 m0, m0, 4096\n\ts_nop 0\n\tbuffer_load_dwordx4 %[vb2], %[rb], s46 offen lds\n\t" \
    "v_mfma_f32_16x16x32_bf16 v[120:123], a[44:47], a[136:139], v[120:123]\n\tv_mfma_f32_16x16x32_bf16 v[124:127], a[44:47], a[140:143], v[124:127]\n\ts_add_u32 m0, m0, 4096\n\t" \
    "s_nop 0\n\tbuffer_load_dwordx4 %[vb3], %[rb], s46 offen lds\n\tv_mfma_f32_16x16x32_bf16 v[128:131], a[48:51], a[112:115], v[128:131]\n\t" \
    "v_mfma_f32_16x16x32_bf16 v[132:135], a[48:51], a[116:119], v[132:135]\n\ts_add_u32 m0, %[lm], 73728\n\ts_add_u32 s44, s44, 262144\n\ts_cmp_ge_u32 s44, s49\n\t" \
    "s_cselect_b32 s47, s49, 0\n\ts_sub_u32 s44, s44, s47\n\tbuffer_load_dwordx4 %[va], %[ra], s44 offen lds\n\tv_mfma_f32_16x16x32_bf16 v[136:139], a[48:51], a[120:123], v[136:139]\n\t" \
    "v_mfma_f32_16x16x32_bf16 v[140:143], a[48:51], a[124:127], v[140:143]\n\ts_add_u32 m0, m0, 4096\n\ts_add_u32 s44, s44, 262144\n\ts_cmp_ge_u32 s44, s49\n\t" \
    "s_cselect_b32 s47, s49, 0\n\ts_sub_u32 s44, s44, s47\n\tbuffer_load_dwordx4 %[va], %[ra], s44 offen lds\n\tv_mfma_f32_16x16x32_bf16 v[144:147], a[56:59], a[112:115], v[144:147]\n\t" \
    "v_mfma_f32_16x16x32_bf16 v[148:151], a[56:59], a[116:119], v[148:151]\n\ts_add_u32 m0, m0, 4096\n\ts_add_u32 s44, s44, 262144\n\ts_cmp_ge_u32 s44, s49\n\t" \
    "s_cselect_b32 s47, s49, 0\n\ts_sub_u32 s44, s44, s47\n\tbuffer_load_dwordx4 %[va], %[ra], s44 offen lds\n\tv_mfma_f32_16x16x32_bf16 v[152:155], a[56:59], a[120:123], v[152:155]\n\t" \
    "v_mfma_f32_16x16x32_bf16 v[156:159], a[56:59], a[124:127], v[156:159]\n\ts_add_u32 m0, m0, 4096\n\ts_add_u32 s44, s44, 262144\n\ts_cmp_ge_u32 s44, s49\n\t" \
    "s_cselect_b32 s47, s49, 0\n\ts_sub_u32 s44, s44, s47\n\tbuffer_load_dwordx4 %[va], %[ra], s44 offen lds\n\tv_mfma_f32_16x16x32_bf16 v[128:131], a[52:55], a[128:131], v[128:131]\n\t" \
    "v_mfma_f32_16x16x32_bf16 v[132:135], a[52:55], a[132:135], v[132:135]\n\ts_add_u32 m0, m0, 4096\n\ts_add_u32 s44, s44, 262144\n\ts_cmp_ge_u32 s44, s49\n\t" \
    "s_cselect_b32 s47, s49, 0\n\ts_sub_u32 s44, s44, s47\n\tbuffer_load_dwordx4 %[va], %[ra], s44 offen lds\n\tv_mfma_f32_16x16x32_bf16 v[136:139], a[52:55], a[136:139], v[136:139]\n\t" \
    "v_mfma_f32_16x16x32_bf16 v[140:143], a[52:55], a[140:143], v[140:143]\n\ts_add_u32 m0, m0, 4096\n\ts_add_u32 s44, s44, 262144\n\ts_cmp_ge_u32 s44, s49\n\t" \
    "s_cselect_b32 s47, s49, 0\n\ts_sub_u32 s44, s44, s47\n\tbuffer_load_dwordx4 %[va], %[ra], s44 offen lds\n\tv_mfma_f32_16x16x32_bf16 v[144:147], a[60:63], a[128:131], v[144:147]\n\t" \
    "v_mfma_f32_16x16x32_bf16 v[148:151], a[60:63], a[132:135], v[148:151]\n\ts_add_u32 m0, m0, 4096\n\ts_add_u32 s44, s44, 262144\n\ts_cmp_ge_u32 s44, s49\n\t" \
    "s_cselect_b32 s47, s49, 0\n\ts_sub_u32 s44, s44, s47\n\tbuffer_load_dwordx4 %[va], %[ra], s44 offen lds\n\tv_mfma_f32_16x16x32_bf16 v[152:155], a[60:63], a[136:139], v[152:155]\n\t" \
    "v_mfma_f32_16x16x32_bf16 v[156:159], a[60:63], a[140:143], v[156:159]\n\ts_add_u32 m0, m0, 4096\n\ts_add_u32 s44, s44, 262144\n\ts_cmp_ge_u32 s44, s49\n\t" \
    "s_cselect_b32 s47, s49, 0\n\ts_sub_u32 s44, s44, s47\n\tbuffer_load_dwordx4 %[va], %[ra], s44 offen lds\n\tv_mfma_f32_16x16x32_bf16 v[160:163], a[64:67], a[112:115], v[160:163]\n\t" \
    "v_mfma_f32_16x16x32_bf16 v[164:167], a[64:67], a[116:119], v[164:167]\n\ts_add_u32 m0, m0, 4096\n\ts_add_u32 s44, s44, 262144\n\ts_cmp_ge_u32 s44, s49\n\t" \
    "s_cselect_b32 s47, s49, 0\n\ts_sub_u32 s44, s44, s47\n\tbuffer_load_dwordx4 %[va], %[ra], s44 offen lds\n\tv_mfma_f32_16x16x32_bf16 v[168:171], a[64:67], a[120:123], v[168:171]\n\t" \
    "v_mfma_f32_16x16x32_bf16 v[172:175], a[64:67], a[124:127], v[172:175]\n\ts_add_u32 m0, m0, 4096\n\ts_add_u32 s44, s44, 262144\n\ts_cmp_ge_u32 s44, s49\n\t" \
    "s_cselect_b32 s47, s49, 0\n\ts_sub_u32 s44, s44, s47\n\tbuffer_load_dwordx4 %[va], %[ra], s44 offen lds\n\tv_mfma_f32_16x16x32_bf16 v[176:179], a[72:75], a[112:115], v[176:179]\n\t" \
    "v_mfma_f32_16x16x32_bf16 v[180:183], a[72:75], a[116:119], v[180:183]\n\ts_add_u32 m0, m0, 4096\n\ts_add_u32 s44, s44, 262144\n\ts_cmp_ge_u32 s44, s49\n\t" \
    "s_cselect_b32 s47, s49, 0\n\ts_sub_u32 s44, s44, s47\n\tbuffer_load_dwordx4 %[va], %[ra], s44 offen lds\n\tv_mfma_f32_16x16x32_bf16 v[184:187], a[72:75], a[120:123], v[184:187]\n\t" \
    "v_mfma_f32_16x16x32_bf16 v[188:191], a[72:75], a[124:127], v[188:191]\n\ts_add_u32 m0, m0, 4096\n\ts_add_u32 s44, s44, 262144\n\ts_cmp_ge_u32 s44, s49\n\t" \
    "s_cselect_b32 s47, s49, 0\n\ts_sub_u32 s44, s44, s47\n\tbuffer_load_dwordx4 %[va], %[ra], s44 offen lds\n\tv_mfma_f32_16x16x32_bf16 v[160:163], a[68:71], a[128:131], v[160:163]\n\t" \
    "v_mfma_f32_16x16x32_bf16 v[164:167], a[68:71], a[132:135], v[164:167]\n\ts_add_u32 m0, m0, 4096\n\ts_add_u32 s44, s44, 262144\n\ts_cmp_ge_u32 s44, s49\n\t" \
    "s_cselect_b32 s47, s49, 0\n\ts_sub_u32 s44, s44, s47\n\tbuffer_load_dwordx4 %[va], %[ra], s44 offen lds\n\tv_mfma_f32_16x16x32_bf16 v[168:171], a[68:71], a[136:139], v[168:171]\n\t" \
    "v_mfma_f32_16x16x32_bf16 v[172:175], a[68:71], a[140:143], v[172:175]\n\ts_add_u32 m0, m0, 4096\n\ts_add_u32 s44, s44, 262144\n\ts_cmp_ge_u32 s44, s49\n\t" \
    "s_cselect_b32 s47, s49, 0\n\ts_sub_u32 s44, s44, s47\n\tbuffer_load_dwordx4 %[va], %[ra], s44 offen lds\n\tv_mfma_f32_16x16x32_bf16 v[176:179], a[76:79], a[128:131], v[176:179]\n\t" \
    "v_mfma_f32_16x16x32_bf16 v[180:183], a[76:79], a[132:135], v[180:183]\n\tv_mfma_f32_16x16x32_bf16 v[184:187], a[76:79], a[136:139], v[184:187]\n\t" \
    "v_mfma_f32_16x16x32_bf16 v[188:191], a[76:79], a[140:143], v[188:191]\n\tv_mfma_f32_16x16x32_bf16 v[192:195], a[80:83], a[112:115], v[192:195]\n\t" \
    "v_mfma_f32_16x16x32_bf16 v[196:199], a[80:83], a[116:119], v[196:199]\n\tv_mfma_f32_16x16x32_bf16 v[200:203], a[80:83], a[120:123], v[200:203]\n\t" \
    "v_mfma_f32_16x16x32_bf16 v[204:207], a[80:83], a[124:127], v[204:207]\n\tv_mfma_f32_16x16x32_bf16 v[208:211], a[88:91], a[112:115], v[208:211]\n\t" \
    "v_mfma_f32_16x16x32_bf16 v[212:215], a[88:91], a[116:119], v[212:215]\n\tv_mfma_f32_16x16x32_bf16 v[216:219], a[88:91], a[120:123], v[216:219]\n\t" \
    "v_mfma_f32_16x16x32_bf16 v[220:223], a[88:91], a[124:127], v[220:223]\n\tv_mfma_f32_16x16x32_bf16 v[192:195], a[84:87], a[128:131], v[192:195]\n\t" \
    "v_mfma_f32_16x16x32_bf16 v[196:199], a[84:87], a[132:135], v[196:199]\n\tv_mfma_f32_16x16x32_bf16 v[200:203], a[84:87], a[136:139], v[200:203]\n\t" \
    "v_mfma_f32_16x16x32_bf16 v[204:207], a[84:87], a[140:143], v[204:207]\n\tv_mfma_f32_16x16x32_bf16 v[208:211], a[92:95], a[128:131], v[208:211]\n\t" \
    "v_mfma_f32_16x16x32_bf16 v[212:215], a[92:95], a[132:135], v[212:215]\n\tv_mfma_f32_16x16x32_bf16 v[216:219], a[92:95], a[136:139], v[216:219]\n\t" \
    "v_mfma_f32_16x16x32_bf16 v[220:223], a[92:95], a[140:143], v[220:223]\n\tv_mfma_f32_16x16x32_bf16 v[224:227], a[96:99], a[112:115], v[224:227]\n\t" \
    "v_mfma_f32_16x16x32_bf16 v[228:231], a[96:99], a[116:119], v[228:231]\n\tv_mfma_f32_16x16x32_bf16 v[232:235], a[96:99], a[120:123], v[232:235]\n\t" \
    "v_mfma_f32_16x16x32_bf16 v[236:239], a[96:99], a[124:127], v[236:239]\n\tv_mfma_f32_16x16x32_bf16 v[240:243], a[104:107], a[112:115], v[240:243]\n\t" \
    "v_mfma_f32_16x16x32_bf16 v[244:247], a[104:107], a[116:119], v[244:247]\n\tv_mfma_f32_16x16x32_bf16 v[248:251], a[104:107], a[120:123], v[248:251]\n\t" \
    "v_mfma_f32_16x16x32_bf16 v[252:255], a[104:107], a[124:127], v[252:255]\n\tv_mfma_f32_16x16x32_bf16 v[224:227], a[100:103], a[128:131], v[224:227]\n\t" \
    "v_mfma_f32_16x16x32_bf16 v[228:231], a[100:103], a[132:135], v[228:231]\n\tv_mfma_f32_16x16x32_bf16 v[232:235], a[100:103], a[136:139], v[232:235]\n\t" \
    "v_mfma_f32_16x16x32_bf16 v[236:239], a[100:103], a[140:143], v[236:239]\n\tv_mfma_f32_16x16x32_bf16 v[240:243], a[108:111], a[128:131], v[240:243]\n\t" \
    "v_mfma_f32_16x16x32_bf16 v[244:247], a[108:111], a[132:135], v[244:247]\n\tv_mfma_f32_16x16x32_bf16 v[248:251], a[108:111], a[136:139], v[248:251]\n\t" \
    "v_mfma_f32_16x16x32_bf16 v[252:255], a[108:111], a[140:143], v[252:255]\n\t" W0T(4) "s_waitcnt vmcnt(0)\n\ts_barrier\n\t" W0T(5) "ds_read_b128 a[0:3], %[r0] offset:0\n\t" \
    "ds_read_b128 a[16:19], %[r0] offset:2048\n\tds_read_b64_tr_b16 a[112:113], %[q0] offset:0\n\tds_read_b64_tr_b16 a[114:115], %[q0] offset:896\n\t" \
    "ds_read_b64_tr_b16 a[116:117], %[q0] offset:32\n\tds_read_b64_tr_b16 a[118:119], %[q0] offset:928\n\tds_read_b64_tr_b16 a[120:121], %[q0] offset:64\n\t" \
    "ds_read_b64_tr_b16 a[122:123], %[q0] offset:960\n\tds_read_b64_tr_b16 a[124:125], %[q0] offset:96\n\tds_read_b64_tr_b16 a[126:127], %[q0] offset:992\n\t" \
    "ds_read_b128 a[4:7], %[r1] offset:0\n\tds_read_b128 a[20:23], %[r1] offset:2048\n\tds_read_b64_tr_b16 a[128:129], %[q0] offset:7680\n\t" \
    "ds_read_b64_tr_b16 a[130:131], %[q0] offset:8576\n\tbuffer_load_dword v31, %[fx], %[ws], 0 offen sc1\n\ts_waitcnt lgkmcnt(4)\n\t" \
    "v_mfma_f32_16x16x32_bf16 v[32:35], a[0:3], a[112:115], v[32:35]\n\tds_read_b64_tr_b16 a[132:133], %[q0] offset:7712\n\tds_read_b64_tr_b16 a[134:135], %[q0] offset:8608\n\t" \
    "v_mfma_f32_16x16x32_bf16 v[36:39], a[0:3], a[116:119], v[36:39]\n\tds_read_b64_tr_b16 a[136:137], %[q0] offset:7744\n\tds_read_b64_tr_b16 a[138:139], %[q0] offset:8640\n\t" \
    "v_mfma_f32_16x16x32_bf16 v[40:43], a[0:3], a[120:123], v[40:43]\n\tds_read_b64_tr_b16 a[140:141], %[q0] offset:7776\n\tds_read_b64_tr_b16 a[142:143], %[q0] offset:8672\n\t" \
    "v_mfma_f32_16x16x32_bf16 v[44:47], a[0:3], a[124:127], v[44:47]\n\tds_read_b128 a[8:11], v26 offset:0\n\tds_read_b128 a[24:27], v26 offset:2048\n\t" \
    "v_mfma_f32_16x16x32_bf16 v[48:51], a[16:19], a[112:115], v[48:51]\n\tds_read_b64_tr_b16 a[144:145], v28 offset:0\n\tds_read_b64_tr_b16 a[146:147], v28 offset:896\n\t" \
    "v_mfma_f32_16x16x32_bf16 v[52:55], a[16:19], a[116:119], v[52:55]\n\tds_read_b64_tr_b16 a[148:149], v28 offset:32\n\tds_read_b64_tr_b16 a[150:151], v28 offset:928\n\t" \
    "v_mfma_f32_16x16x32_bf16 v[56:59], a[16:19], a[120:123], v[56:59]\n\tds_read_b64_tr_b16 a[152:153], v28 offset:64\n\tds_read_b64_tr_b16 a[154:155], v28 offset:960\n\t" \
    "v_mfma_f32_16x16x32_bf16 v[60:63], a[16:19], a[124:127], v[60:63]\n\tds_read_b64_tr_b16 a[156:157], v28 offset:96\n\tds_read_b64_tr_b16 a[158:159], v28 offset:992\n\t" \
    "s_waitcnt lgkmcnt(10)\n\tv_mfma_f32_16x16x32_bf16 v[32:35], a[4:7], a[128:131], v[32:35]\n\tds_read_b128 a[12:15], v27 offset:0\n\tds_read_b128 a[28:31], v27 offset:2048\n\t" \
    "v_mfma_f32_16x16x32_bf16 v[36:39], a[4:7], a[132:135], v[36:39]\n\tds_read_b64_tr_b16 a[160:161], v28 offset:7680\n\tds_read_b64_tr_b16 a[162:163], v28 offset:8576\n\t" \
    "v_mfma_f32_16x16x32_bf16 v[40:43], a[4:7], a[136:139], v[40:43]\n\tds_read_b64_tr_b16 a[164:165], v28 offset:7712\n\tds_read_b64_tr_b16 a[166:167], v28 offset:8608\n\t" \
    "v_mfma_f32_16x16x32_bf16 v[44:47], a[4:7], a[140:143], v[44:47]\n\tds_read_b64_tr_b16 a[168:169], v28 offset:7744\n\tds_read_b64_tr_b16 a[170:171], v28 offset:8640\n\t" \
    "v_mfma_f32_16x16x32_bf16 v[48:51], a[20:23], a[128:131], v[48:51]\n\tds_read_b64_tr_b16 a[172:173], v28 offset:7776\n\tds_read_b64_tr_b16 a[174:175], v28 offset:8672\n\t" \
    "v_mfma_f32_16x16x32_bf16 v[52:55], a[20:23], a[132:135], v[52:55]\n\tds_read_b128 a[32:35], %[r0] offset:4096\n\tds_read_b128 a[48:51], %[r0] offset:6144\n\t" \
    "v_mfma_f32_16x16x32_bf16 v[56:59], a[20:23], a[136:139], v[56:59]\n\tds_read_b128 a[36:39], %[r1] offset:4096\n\tds_read_b128 a[52:55], %[r1] offset:6144\n\t" \
    "v_mfma_f32_16x16x32_bf16 v[60:63], a[20:23], a[140:143], v[60:63]\n\tds_read_b128 a[40:43], v26 offset:4096\n\tds_read_b128 a[56:59], v26 offset:6144\n\ts_waitcnt lgkmcnt(12)\n\t" \
    "v_mfma_f32_16x16x32_bf16 v[32:35], a[8:11], a[144:147], v[32:35]\n\tds_read_b128 a[44:47], v27 offset:4096\n\tds_read_b128 a[60:63], v27 offset:6144\n\t" \
    "v_mfma_f32_16x16x32_bf16 v[36:39], a[8:11], a[148:151], v[36:39]\n\tds_read_b128 a[64:67], %[r0] offset:8192\n\tds_read_b128 a[80:83], %[r0] offset:10240\n\t" \
    "v_mfma_f32_16x16x32_bf16 v[40:43], a[8:11], a[152:155], v[40:43]\n\tds_read_b128 a[68:71], %[r1] offset:8192\n\tds_read_b128 a[84:87], %[r1] offset:10240\n\t" \
    "v_mfma_f32_16x16x32_bf16 v[44:47], a[8:11], a[156:159], v[44:47]\n\tds_read_b128 a[72:75], v26 offset:8192\n\tds_read_b128 a[88:91], v26 offset:10240\n\t" \
    "v_mfma_f32_16x16x32_bf16 v[48:51], a[24:27], a[144:147], v[48:51]\n\tds_read_b128 a[76:79], v27 offset:8192\n\tds_read_b128 a[92:95], v27 offset:10240\n\t" \
    "v_mfma_f32_16x16x32_bf16 v[52:55], a[24:27], a[148:151], v[52:55]\n\tv_mfma_f32_16x16x32_bf16 v[56:59], a[24:27], a[152:155], v[56:59]\n\t" \
    "v_mfma_f32_16x16x32_bf16 v[60:63], a[24:27], a[156:159], v[60:63]\n\ts_waitcnt lgkmcnt(12)\n\tv_mfma_f32_16x16x32_bf16 v[32:35], a[12:15], a[160:163], v[32:35]\n\t" \
    "v_mfma_f32_16x16x32_bf16 v[36:39], a[12:15], a[164:167], v[36:39]\n\tv_mfma_f32_16x16x32_bf16 v[40:43], a[12:15], a[168:171], v[40:43]\n\t" \
    "v_mfma_f32_16x16x32_bf16 v[44:47], a[12:15], a[172:175], v[44:47]\n\tv_mfma_f32_16x16x32_bf16 v[48:51], a[28:31], a[160:163], v[48:51]\n\t" \
    "v_mfma_f32_16x16x32_bf16 v[52:55], a[28:31], a[164:167], v[52:55]\n\tv_mfma_f32_16x16x32_bf16 v[56:59], a[28:31], a[168:171], v[56:59]\n\t" \
    "v_mfma_f32_16x16x32_bf16 v[60:63], a[28:31], a[172:175], v[60:63]\n\ts_waitcnt lgkmcnt(8)\n\ts_barrier\n\tv_mfma_f32_16x16x32_bf16 v[64:67], a[32:35], a[112:115], v[64:67]\n\t" \
    "ds_read_b128 a[0:3], %[r0] offset:12288\n\tv_mfma_f32_16x16x32_bf16 v[68:71], a[32:35], a[116:119], v[68:71]\n\tds_read_b128 a[16:19], %[r0] offset:14336\n\t" \
    "v_mfma_f32_16x16x32_bf16 v[72:75], a[32:35], a[120:123], v[72:75]\n\tds_read_b128 a[4:7], %[r1] offset:12288\n\t" \
    "v_mfma_f32_16x16x32_bf16 v[76:79], a[32:35], a[124:127], v[76:79]\n\tds_read_b128 a[20:23], %[r1] offset:14336\n\t" \
    "v_mfma_f32_16x16x32_bf16 v[80:83], a[48:51], a[112:115], v[80:83]\n\tds_read_b128 a[8:11], v26 offset:12288\n\tv_mfma_f32_16x16x32_bf16 v[84:87], a[48:51], a[116:119], v[84:87]\n\t" \
    "ds_read_b128 a[24:27], v26 offset:14336\n\tv_mfma_f32_16x16x32_bf16 v[88:91], a[48:51], a[120:123], v[88:91]\n\tds_read_b128 a[12:15], v27 offset:12288\n\t" \
    "v_mfma_f32_16x16x32_bf16 v[92:95], a[48:51], a[124:127], v[92:95]\n\tds_read_b128 a[28:31], v27 offset:14336\n\t" \
    "v_mfma_f32_16x16x32_bf16 v[64:67], a[36:39], a[128:131], v[64:67]\n\tv_cvt_pk_bf16_f32 v32, v32, v33\n\tv_cvt_pk_bf16_f32 v33, v34, v35\n\t" \
    "v_mfma_f32_16x16x32_bf16 v[68:71], a[36:39], a[132:135], v[68:71]\n\tv_cvt_pk_bf16_f32 v34, v48, v49\n\tv_cvt_pk_bf16_f32 v35, v50, v51\n\t" \
    "v_mfma_f32_16x16x32_bf16 v[72:75], a[36:39], a[136:139], v[72:75]\n\ts_add_u32 s47, %[sm], 0\n\tbuffer_store_dwordx4 v[32:35], %[so], %[ws], s47 offen sc1\n\t" \
    "v_mfma_f32_16x16x32_bf16 v[76:79], a[36:39], a[140:143], v[76:79]\n\tds_write_b128 %[iw], v[32:35] offset:0\n\tv_cvt_pk_bf16_f32 v36, v36, v37\n\t" \
    "v_mfma_f32_16x16x32_bf16 v[80:83], a[52:55], a[128:131], v[80:83]\n\tv_cvt_pk_bf16_f32 v37, v38, v39\n\tv_cvt_pk_bf16_f32 v38, v52, v53\n\t" \
    "v_mfma_f32_16x16x32_bf16 v[84:87], a[52:55], a[132:135], v[84:87]\n\tv_cvt_pk_bf16_f32 v39, v54, v55\n\ts_add_u32 s47, %[sm], 229376\n\t" \
    "v_mfma_f32_16x16x32_bf16 v[88:91], a[52:55], a[136:139], v[88:91]\n\tbuffer_store_dwordx4 v[36:39], %[so], %[ws], s47 offen sc1\n\tds_write_b128 %[iw], v[36:39] offset:1024\n\t" \
    "v_mfma_f32_16x16x32_bf16 v[92:95], a[52:55], a[140:143], v[92:95]\n\tv_cvt_pk_bf16_f32 v40, v40, v41\n\tv_cvt_pk_bf16_f32 v41, v42, v43\n\t" \
    "v_mfma_f32_16x16x32_bf16 v[64:67], a[40:43], a[144:147], v[64:67]\n\tv_cvt_pk_bf16_f32 v42, v56, v57\n\tv_cvt_pk_bf16_f32 v43, v58, v59\n\t" \
    "v_mfma_f32_16x16x32_bf16 v[68:71], a[40:43], a[148:151], v[68:71]\n\ts_add_u32 s47, %[sm], 458752\n\tbuffer_store_dwordx4 v[40:43], %[so], %[ws], s47 offen sc1\n\t" \
    "v_mfma_f32_16x16x32_bf16 v[72:75], a[40:43], a[152:155], v[72:75]\n\tds_write_b128 %[iw], v[40:43] offset:2048\n\tv_cvt_pk_bf16_f32 v44, v44, v45\n\t" \
    "v_mfma_f32_16x16x32_bf16 v[76:79], a[40:43], a[156:159], v[76:79]\n\tv_cvt_pk_bf16_f32 v45, v46, v47\n\tv_cvt_pk_bf16_f32 v46, v60, v61\n\t" \
    "v_mfma_f32_16x16x32_bf16 v[80:83], a[56:59], a[144:147], v[80:83]\n\tv_cvt_pk_bf16_f32 v47, v62, v63\n\ts_add_u32 s47, %[sm], 688128\n\t" \
    "v_mfma_f32_16x16x32_bf16 v[84:87], a[56:59], a[148:151], v[84:87]\n\tbuffer_store_dwordx4 v[44:47], %[so3], %[ws], s47 offen sc1\n\tds_write_b128 %[iw], v[44:47] offset:3072\n\t" \
    "v_mfma_f32_16x16x32_bf16 v[88:91], a[56:59], a[152:155], v[88:91]\n\tv_mfma_f32_16x16x32_bf16 v[92:95], a[56:59], a[156:159], v[92:95]\n\t" \
    "v_mfma_f32_16x16x32_bf16 v[64:67], a[44:47], a[160:163], v[64:67]\n\tv_mfma_f32_16x16x32_bf16 v[68:71], a[44:47], a[164:167], v[68:71]\n\t" \
    "v_mfma_f32_16x16x32_bf16 v[72:75], a[44:47], a[168:171], v[72:75]\n\tv_mfma_f32_16x16x32_bf16 v[76:79], a[44:47], a[172:175], v[76:79]\n\t" \
    "v_mfma_f32_16x16x32_bf16 v[80:83], a[60:63], a[160:163], v[80:83]\n\tv_mfma_f32_16x16x32_bf16 v[84:87], a[60:63], a[164:167], v[84:87]\n\t" \
    "v_mfma_f32_16x16x32_bf16 v[88:91], a[60:63], a[168:171], v[88:91]\n\tv_mfma_f32_16x16x32_bf16 v[92:95], a[60:63], a[172:175], v[92:95]\n\ts_waitcnt lgkmcnt(12)\n\ts_barrier\n\t" \
    "v_mfma_f32_16x16x32_bf16 v[96:99], a[64:67], a[112:115], v[96:99]\n\tds_read_b128 a[32:35], %[r0] offset:16384\n\t" \
    "v_mfma_f32_16x16x32_bf16 v[100:103], a[64:67], a[116:119], v[100:103]\n\tds_read_b128 a[48:51], %[r0] offset:18432\n\t" \
    "v_mfma_f32_16x16x32_bf16 v[104:107], a[64:67], a[120:123], v[104:107]\n\tds_read_b128 a[36:39], %[r1] offset:16384\n\t" \
    "v_mfma_f32_16x16x32_bf16 v[108:111], a[64:67], a[124:127], v[108:111]\n\tds_read_b128 a[52:55], %[r1] offset:18432\n\t" \
    "v_mfma_f32_16x16x32_bf16 v[112:115], a[80:83], a[112:115], v[112:115]\n\tds_read_b128 a[40:43], v26 offset:16384\n\t" \
    "v_mfma_f32_16x16x32_bf16 v[116:119], a[80:83], a[116:119], v[116:119]\n\tds_read_b128 a[56:59], v26 offset:18432\n\t" \
    "v_mfma_f32_16x16x32_bf16 v[120:123], a[80:83], a[120:123], v[120:123]\n\tds_read_b128 a[44:47], v27 offset:16384\n\t" \
    "v_mfma_f32_16x16x32_bf16 v[124:127], a[80:83], a[124:127], v[124:127]\n\tds_read_b128 a[60:63], v27 offset:18432\n\t" \
    "v_mfma_f32_16x16x32_bf16 v[96:99], a[68:71], a[128:131], v[96:99]\n\tv_cvt_pk_bf16_f32 v64, v64, v65\n\tv_cvt_pk_bf16_f32 v65, v66, v67\n\t" \
    "v_mfma_f32_16x16x32_bf16 v[100:103], a[68:71], a[132:135], v[100:103]\n\tv_cvt_pk_bf16_f32 v66, v80, v81\n\tv_cvt_pk_bf16_f32 v67, v82, v83\n\t" \
    "v_mfma_f32_16x16x32_bf16 v[104:107], a[68:71], a[136:139], v[104:107]\n\ts_add_u32 s47, %[sm], 1024\n\tbuffer_store_dwordx4 v[64:67], %[so], %[ws], s47 offen sc1\n\t" \
    "v_mfma_f32_16x16x32_bf16 v[108:111], a[68:71], a[140:143], v[108:111]\n\tds_write_b128 %[iw], v[64:67] offset:4096\n\tv_cvt_pk_bf16_f32 v68, v68, v69\n\t" \
    "v_mfma_f32_16x16x32_bf16 v[112:115], a[84:87], a[128:131], v[112:115]\n\tv_cvt_pk_bf16_f32 v69, v70, v71\n\tv_cvt_pk_bf16_f32 v70, v84, v85\n\t" \
    "v_mfma_f32_16x16x32_bf16 v[116:119], a[84:87], a[132:135], v[116:119]\n\tv_cvt_pk_bf16_f32 v71, v86, v87\n\ts_add_u32 s47, %[sm], 230400\n\t" \
    "v_mfma_f32_16x16x32_bf16 v[120:123], a[84:87], a[136:139], v[120:123]\n\tbuffer_store_dwordx4 v[68:71], %[so], %[ws], s47 offen sc1\n\tds_write_b128 %[iw], v[68:71] offset:5120\n\t" \
    "v_mfma_f32_16x16x32_bf16 v[124:127], a[84:87], a[140:143], v[124:127]\n\tv_cvt_pk_bf16_f32 v72, v72, v73\n\tv_cvt_pk_bf16_f32 v73, v74, v75\n\t" \
    "v_mfma_f32_16x16x32_bf16 v[96:99], a[72:75], a[144:147], v[96:99]\n\tv_cvt_pk_bf16_f32 v74, v88, v89\n\tv_cvt_pk_bf16_f32 v75, v90, v91\n\t" \
    "v_mfma_f32_16x16x32_bf16 v[100:103], a[72:75], a[148:151], v[100:103]\n\ts_add_u32 s47, %[sm], 459776\n\tbuffer_store_dwordx4 v[72:75], %[so], %[ws], s47 offen sc1\n\t" \
    "v_mfma_f32_16x16x32_bf16 v[104:107], a[72:75], a[152:155], v[104:107]\n\tds_write_b128 %[iw], v[72:75] offset:6144\n\tv_cvt_pk_bf16_f32 v76, v76, v77\n\t" \
    "v_mfma_f32_16x16x32_bf16 v[108:111], a[72:75], a[156:159], v[108:111]\n\tv_cvt_pk_bf16_f32 v77, v78, v79\n\tv_cvt_pk_bf16_f32 v78, v92, v93\n\t" \
    "v_mfma_f32_16x16x32_bf16 v[112:115], a[88:91], a[144:147], v[112:115]\n\tv_cvt_pk_bf16_f32 v79, v94, v95\n\ts_add_u32 s47, %[sm], 689152\n\t" \
    "v_mfma_f32_16x16x32_bf16 v[116:119], a[88:91], a[148:151], v[116:119]\n\tbuffer_store_dwordx4 v[76:79], %[so3], %[ws], s47 offen sc1\n\t" \
    "ds_write_b128 %[iw], v[76:79] offset:7168\n\tv_mfma_f32_16x16x32_bf16 v[120:123], a[88:91], a[152:155], v[120:123]\n\t" \
    "v_mfma_f32_16x16x32_bf16 v[124:127], a[88:91], a[156:159], v[124:127]\n\tv_mfma_f32_16x16x32_bf16 v[96:99], a[76:79], a[160:163], v[96:99]\n\t" \
    "v_mfma_f32_16x16x32_bf16 v[100:103], a[76:79], a[164:167], v[100:103]\n\tv_mfma_f32_16x16x32_bf16 v[104:107], a[76:79], a[168:171], v[104:107]\n\t" \
    "v_mfma_f32_16x16x32_bf16 v[108:111], a[76:79], a[172:175], v[108:111]\n\tv_mfma_f32_16x16x32_bf16 v[112:115], a[92:95], a[160:163], v[112:115]\n\t" \
    "v_mfma_f32_16x16x32_bf16 v[116:119], a[92:95], a[164:167], v[116:119]\n\tv_mfma_f32_16x16x32_bf16 v[120:123], a[92:95], a[168:171], v[120:123]\n\t" \
    "v_mfma_f32_16x16x32_bf16 v[124:127], a[92:95], a[172:175], v[124:127]\n\ts_waitcnt vmcnt(8)\n\tv_cmp_eq_u32 vcc, %[h0], v30\n\tv_cmp_gt_i32 s[46:47], 0, v31\n\t" \
    "s_or_b64 vcc, vcc, s[46:47]\n\ts_cselect_b32 %[ok], 0, 1\n\ts_cbranch_scc1 np%=\n\tbuffer_load_dword v24, %[px], %[ws], 0 offen offset:0 sc1\n\t" \
    "buffer_load_dword v24, %[px], %[ws], 0 offen offset:128 sc1\n\tbuffer_load_dword v24, %[px], %[ws], 0 offen offset:256 sc1\n\t" \
    "buffer_load_dword v24, %[px], %[ws], 0 offen offset:384 sc1\n\tnp%=:\n\ts_waitcnt lgkmcnt(12)\n\ts_barrier\n\t" \
    "v_mfma_f32_16x16x32_bf16 v[128:131], a[0:3], a[112:115], v[128:131]\n\tds_read_b128 a[64:67], %[r0] offset:20480\n\t" \
    "v_mfma_f32_16x16x32_bf16 v[132:135], a[0:3], a[116:119], v[132:135]\n\tds_read_b128 a[80:83], %[r0] offset:22528\n\t" \
    "v_mfma_f32_16x16x32_bf16 v[136:139], a[0:3], a[120:123], v[136:139]\n\tds_read_b128 a[68:71], %[r1] offset:20480\n\t" \
    "v_mfma_f32_16x16x32_bf16 v[140:143], a[0:3], a[124:127], v[140:143]\n\tds_read_b128 a[84:87], %[r1] offset:22528\n\t" \
    "v_mfma_f32_16x16x32_bf16 v[144:147], a[16:19], a[112:115], v[144:147]\n\tds_read_b128 a[72:75], v26 offset:20480\n\t" \
    "v_mfma_f32_16x16x32_bf16 v[148:151], a[16:19], a[116:119], v[148:151]\n\tds_read_b128 a[88:91], v26 offset:22528\n\t" \
    "v_mfma_f32_16x16x32_bf16 v[152:155], a[16:19], a[120:123], v[152:155]\n\tds_read_b128 a[76:79], v27 offset:20480\n\t" \
    "v_mfma_f32_16x16x32_bf16 v[156:159], a[16:19], a[124:127], v[156:159]\n\tds_read_b128 a[92:95], v27 offset:22528\n\t" \
    "v_mfma_f32_16x16x32_bf16 v[128:131], a[4:7], a[128:131], v[128:131]\n\tv_cvt_pk_bf16_f32 v96, v96, v97\n\tv_cvt_pk_bf16_f32 v97, v98, v99\n\t" \
    "v_mfma_f32_16x16x32_bf16 v[132:135], a[4:7], a[132:135], v[132:135]\n\tv_cvt_pk_bf16_f32 v98, v112, v113\n\tv_cvt_pk_bf16_f32 v99, v114, v115\n\t" \
    "v_mfma_f32_16x16x32_bf16 v[136:139], a[4:7], a[136:139], v[136:139]\n\ts_add_u32 s47, %[sm], 2048\n\tbuffer_store_dwordx4 v[96:99], %[so], %[ws], s47 offen sc1\n\t" \
    "v_mfma_f32_16x16x32_bf16 v[140:143], a[4:7], a[140:143], v[140:143]\n\tds_write_b128 %[iw], v[96:99] offset:8192\n\tv_cvt_pk_bf16_f32 v100, v100, v101\n\t" \
    "v_mfma_f32_16x16x32_bf16 v[144:147], a[20:23], a[128:131], v[144:147]\n\tv_cvt_pk_bf16_f32 v101, v102, v103\n\tv_cvt_pk_bf16_f32 v102, v116, v117\n\t" \
    "v_mfma_f32_16x16x32_bf16 v[148:151], a[20:23], a[132:135], v[148:151]\n\tv_cvt_pk_bf16_f32 v103, v118, v119\n\ts_add_u32 s47, %[sm], 231424\n\t" \
    "v_mfma_f32_16x16x32_bf16 v[152:155], a[20:23], a[136:139], v[152:155]\n\tbuffer_store_dwordx4 v[100:103], %[so], %[ws], s47 offen sc1\n\t" \
    "ds_write_b128 %[iw], v[100:103] offset:9216\n\tv_mfma_f32_16x16x32_bf16 v[156:159], a[20:23], a[140:143], v[156:159]\n\tv_cvt_pk_bf16_f32 v104, v104, v105\n\t" \
    "v_cvt_pk_bf16_f32 v105, v106, v107\n\tv_mfma_f32_16x16x32_bf16 v[128:131], a[8:11], a[144:147], v[128:131]\n\tv_cvt_pk_bf16_f32 v106, v120, v121\n\t" \
    "v_cvt_pk_bf16_f32 v107, v122, v123\n\tv_mfma_f32_16x16x32_bf16 v[132:135], a[8:11], a[148:151], v[132:135]\n\ts_add_u32 s47, %[sm], 460800\n\t" \
    "buffer_store_dwordx4 v[104:107], %[so], %[ws], s47 offen sc1\n\tv_mfma_f32_16x16x32_bf16 v[136:139], a[8:11], a[152:155], v[136:139]\n\t" \
    "ds_write_b128 %[iw], v[104:107] offset:10240\n\tv_cvt_pk_bf16_f32 v108, v108, v109\n\tv_mfma_f32_16x16x32_bf16 v[140:143], a[8:11], a[156:159], v[140:143]\n\t" \
    "v_cvt_pk_bf16_f32 v109, v110, v111\n\tv_cvt_pk_bf16_f32 v110, v124, v125\n\tv_mfma_f32_16x16x32_bf16 v[144:147], a[24:27], a[144:147], v[144:147]\n\t" \
    "v_cvt_pk_bf16_f32 v111, v126, v127\n\ts_add_u32 s47, %[sm], 690176\n\tv_mfma_f32_16x16x32_bf16 v[148:151], a[24:27], a[148:151], v[148:151]\n\t" \
    "buffer_store_dwordx4 v[108:111], %[so3], %[ws], s47 offen sc1\n\tds_write_b128 %[iw], v[108:111] offset:11264\n\t" \
    "v_mfma_f32_16x16x32_bf16 v[152:155], a[24:27], a[152:155], v[152:155]\n\tv_mfma_f32_16x16x32_bf16 v[156:159], a[24:27], a[156:159], v[156:159]\n\t" \
    "v_mfma_f32_16x16x32_bf16 v[128:131], a[12:15], a[160:163], v[128:131]\n\tv_mfma_f32_16x16x32_bf16 v[132:135], a[12:15], a[164:167], v[132:135]\n\t" \
    "v_mfma_f32_16x16x32_bf16 v[136:139], a[12:15], a[168:171], v[136:139]\n\tv_mfma_f32_16x16x32_bf16 v[140:143], a[12:15], a[172:175], v[140:143]\n\t" \
    "v_mfma_f32_16x16x32_bf16 v[144:147], a[28:31], a[160:163], v[144:147]\n\tv_mfma_f32_16x16x32_bf16 v[148:151], a[28:31], a[164:167], v[148:151]\n\t" \
    "v_mfma_f32_16x16x32_bf16 v[152:155], a[28:31], a[168:171], v[152:155]\n\tv_mfma_f32_16x16x32_bf16 v[156:159], a[28:31], a[172:175], v[156:159]\n\ts_waitcnt lgkmcnt(12)\n\t" \
    "s_barrier\n\tv_mfma_f32_16x16x32_bf16 v[160:163], a[32:35], a[112:115], v[160:163]\n\tds_read_b128 a[0:3], %[r0] offset:24576\n\t" \
    "v_mfma_f32_16x16x32_bf16 v[164:167], a[32:35], a[116:119], v[164:167]\n\tds_read_b128 a[16:19], %[r0] offset:26624\n\t" \
    "v_mfma_f32_16x16x32_bf16 v[168:171], a[32:35], a[120:123], v[168:171]\n\tds_read_b128 a[4:7], %[r1] offset:24576\n\t" \
    "v_mfma_f32_16x16x32_bf16 v[172:175], a[32:35], a[124:127], v[172:175]\n\tds_read_b128 a[20:23], %[r1] offset:26624\n\t" \
    "v_mfma_f32_16x16x32_bf16 v[176:179], a[48:51], a[112:115], v[176:179]\n\tds_read_b128 a[8:11], v26 offset:24576\n\t" \
    "v_mfma_f32_16x16x32_bf16 v[180:183], a[48:51], a[116:119], v[180:183]\n\tds_read_b128 a[24:27], v26 offset:26624\n\t" \
    "v_mfma_f32_16x16x32_bf16 v[184:187], a[48:51], a[120:123], v[184:187]\n\tds_read_b128 a[12:15], v27 offset:24576\n\t" \
    "v_mfma_f32_16x16x32_bf16 v[188:191], a[48:51], a[124:127], v[188:191]\n\tds_read_b128 a[28:31], v27 offset:26624\n\t" \
    "v_mfma_f32_16x16x32_bf16 v[160:163], a[36:39], a[128:131], v[160:163]\n\tv_cvt_pk_bf16_f32 v128, v128, v129\n\tv_cvt_pk_bf16_f32 v129, v130, v131\n\t" \
    "v_mfma_f32_16x16x32_bf16 v[164:167], a[36:39], a[132:135], v[164:167]\n\tv_cvt_pk_bf16_f32 v130, v144, v145\n\tv_cvt_pk_bf16_f32 v131, v146, v147\n\t" \
    "v_mfma_f32_16x16x32_bf16 v[168:171], a[36:39], a[136:139], v[168:171]\n\ts_add_u32 s47, %[sm], 3072\n\tbuffer_store_dwordx4 v[128:131], %[so], %[ws], s47 offen sc1\n\t" \
    "v_mfma_f32_16x16x32_bf16 v[172:175], a[36:39], a[140:143], v[172:175]\n\tds_write_b128 %[iw], v[128:131] offset:12288\n\tv_cvt_pk_bf16_f32 v132, v132, v133\n\t" \
    "v_mfma_f32_16x16x32_bf16 v[176:179], a[52:55], a[128:131], v[176:179]\n\tv_cvt_pk_bf16_f32 v133, v134, v135\n\tv_cvt_pk_bf16_f32 v134, v148, v149\n\t" \
    "v_mfma_f32_16x16x32_bf16 v[180:183], a[52:55], a[132:135], v[180:183]\n\tv_cvt_pk_bf16_f32 v135, v150, v151\n\ts_add_u32 s47, %[sm], 232448\n\t" \
    "v_mfma_f32_16x16x32_bf16 v[184:187], a[52:55], a[136:139], v[184:187]\n\tbuffer_store_dwordx4 v[132:135], %[so], %[ws], s47 offen sc1\n\t" \
    "ds_write_b128 %[iw], v[132:135] offset:13312\n\tv_mfma_f32_16x16x32_bf16 v[188:191], a[52:55], a[140:143], v[188:191]\n\tv_cvt_pk_bf16_f32 v136, v136, v137\n\t" \
    "v_cvt_pk_bf16_f32 v137, v138, v139\n\tv_mfma_f32_16x16x32_bf16 v[160:163], a[40:43], a[144:147], v[160:163]\n\tv_cvt_pk_bf16_f32 v138, v152, v153\n\t" \
    "v_cvt_pk_bf16_f32 v139, v154, v155\n\tv_mfma_f32_16x16x32_bf16 v[164:167], a[40:43], a[148:151], v[164:167]\n\ts_add_u32 s47, %[sm], 461824\n\t" \
    "buffer_store_dwordx4 v[136:139], %[so], %[ws], s47 offen sc1\n\tv_mfma_f32_16x16x32_bf16 v[168:171], a[40:43], a[152:155], v[168:171]\n\t" \
    "ds_write_b128 %[iw], v[136:139] offset:14336\n\tv_cvt_pk_bf16_f32 v140, v140, v141\n\tv_mfma_f32_16x16x32_bf16 v[172:175], a[40:43], a[156:159], v[172:175]\n\t" \
    "v_cvt_pk_bf16_f32 v141, v142, v143\n\tv_cvt_pk_bf16_f32 v142, v156, v157\n\tv_mfma_f32_16x16x32_bf16 v[176:179], a[56:59], a[144:147], v[176:179]\n\t" \
    "v_cvt_pk_bf16_f32 v143, v158, v159\n\ts_add_u32 s47, %[sm], 691200\n\tv_mfma_f32_16x16x32_bf16 v[180:183], a[56:59], a[148:151], v[180:183]\n\t" \
    "buffer_store_dwordx4 v[140:143], %[so3], %[ws], s47 offen sc1\n\tds_write_b128 %[iw], v[140:143] offset:15360\n\t" \
    "v_mfma_f32_16x16x32_bf16 v[184:187], a[56:59], a[152:155], v[184:187]\n\tv_mfma_f32_16x16x32_bf16 v[188:191], a[56:59], a[156:159], v[188:191]\n\t" \
    "v_mfma_f32_16x16x32_bf16 v[160:163], a[44:47], a[160:163], v[160:163]\n\tv_mfma_f32_16x16x32_bf16 v[164:167], a[44:47], a[164:167], v[164:167]\n\t" \
    "v_mfma_f32_16x16x32_bf16 v[168:171], a[44:47], a[168:171], v[168:171]\n\tv_mfma_f32_16x16x32_bf16 v[172:175], a[44:47], a[172:175], v[172:175]\n\t" \
    "v_mfma_f32_16x16x32_bf16 v[176:179], a[60:63], a[160:163], v[176:179]\n\tv_mfma_f32_16x16x32_bf16 v[180:183], a[60:63], a[164:167], v[180:183]\n\t" \
    "v_mfma_f32_16x16x32_bf16 v[184:187], a[60:63], a[168:171], v[184:187]\n\tv_mfma_f32_16x16x32_bf16 v[188:191], a[60:63], a[172:175], v[188:191]\n\ts_waitcnt lgkmcnt(12)\n\t" \
    "s_barrier\n\tv_mfma_f32_16x16x32_bf16 v[192:195], a[64:67], a[112:115], v[192:195]\n\tv_cvt_pk_bf16_f32 v160, v160, v161\n\tv_cvt_pk_bf16_f32 v161, v162, v163\n\t" \
    "v_mfma_f32_16x16x32_bf16 v[196:199], a[64:67], a[116:119], v[196:199]\n\tv_cvt_pk_bf16_f32 v162, v176, v177\n\tv_cvt_pk_bf16_f32 v163, v178, v179\n\t" \
    "v_mfma_f32_16x16x32_bf16 v[200:203], a[64:67], a[120:123], v[200:203]\n\ts_add_u32 s47, %[sm], 4096\n\tbuffer_store_dwordx4 v[160:163], %[so], %[ws], s47 offen sc1\n\t" \
    "v_mfma_f32_16x16x32_bf16 v[204:207], a[64:67], a[124:127], v[204:207]\n\tds_write_b128 %[iw], v[160:163] offset:16384\n\tv_cvt_pk_bf16_f32 v164, v164, v165\n\t" \
    "v_mfma_f32_16x16x32_bf16 v[208:211], a[80:83], a[112:115], v[208:211]\n\tv_cvt_pk_bf16_f32 v165, v166, v167\n\tv_cvt_pk_bf16_f32 v166, v180, v181\n\t" \
    "v_mfma_f32_16x16x32_bf16 v[212:215], a[80:83], a[116:119], v[212:215]\n\tv_cvt_pk_bf16_f32 v167, v182, v183\n\ts_add_u32 s47, %[sm], 233472\n\t" \
    "v_mfma_f32_16x16x32_bf16 v[216:219], a[80:83], a[120:123], v[216:219]\n\tbuffer_store_dwordx4 v[164:167], %[so], %[ws], s47 offen sc1\n\t" \
    "ds_write_b128 %[iw], v[164:167] offset:17408\n\tv_mfma_f32_16x16x32_bf16 v[220:223], a[80:83], a[124:127], v[220:223]\n\tv_cvt_pk_bf16_f32 v168, v168, v169\n\t" \
    "v_cvt_pk_bf16_f32 v169, v170, v171\n\tv_mfma_f32_16x16x32_bf16 v[192:195], a[68:71], a[128:131], v[192:195]\n\tv_cvt_pk_bf16_f32 v170, v184, v185\n\t" \
    "v_cvt_pk_bf16_f32 v171, v186, v187\n\tv_mfma_f32_16x16x32_bf16 v[196:199], a[68:71], a[132:135], v[196:199]\n\ts_add_u32 s47, %[sm], 462848\n\t" \
    "buffer_store_dwordx4 v[168:171], %[so], %[ws], s47 offen sc1\n\tv_mfma_f32_16x16x32_bf16 v[200:203], a[68:71], a[136:139], v[200:203]\n\t" \
    "ds_write_b128 %[iw], v[168:171] offset:18432\n\tv_cvt_pk_bf16_f32 v172, v172, v173\n\tv_mfma_f32_16x16x32_bf16 v[204:207], a[68:71], a[140:143], v[204:207]\n\t" \
    "v_cvt_pk_bf16_f32 v173, v174, v175\n\tv_cvt_pk_bf16_f32 v174, v188, v189\n\tv_mfma_f32_16x16x32_bf16 v[208:211], a[84:87], a[128:131], v[208:211]\n\t" \
    "v_cvt_pk_bf16_f32 v175, v190, v191\n\ts_add_u32 s47, %[sm], 692224\n\tv_mfma_f32_16x16x32_bf16 v[212:215], a[84:87], a[132:135], v[212:215]\n\t" \
    "buffer_store_dwordx4 v[172:175], %[so3], %[ws], s47 offen sc1\n\tds_write_b128 %[iw], v[172:175] offset:19456\n\t" \
    "v_mfma_f32_16x16x32_bf16 v[216:219], a[84:87], a[136:139], v[216:219]\n\tv_mfma_f32_16x16x32_bf16 v[220:223], a[84:87], a[140:143], v[220:223]\n\t" \
    "v_mfma_f32_16x16x32_bf16 v[192:195], a[72:75], a[144:147], v[192:195]\n\tv_mfma_f32_16x16x32_bf16 v[196:199], a[72:75], a[148:151], v[196:199]\n\t" \
    "v_mfma_f32_16x16x32_bf16 v[200:203], a[72:75], a[152:155], v[200:203]\n\tv_mfma_f32_16x16x32_bf16 v[204:207], a[72:75], a[156:159], v[204:207]\n\t" \
    "v_mfma_f32_16x16x32_bf16 v[208:211], a[88:91], a[144:147], v[208:211]\n\tv_mfma_f32_16x16x32_bf16 v[212:215], a[88:91], a[148:151], v[212:215]\n\t" \
    "v_mfma_f32_16x16x32_bf16 v[216:219], a[88:91], a[152:155], v[216:219]\n\tv_mfma_f32_16x16x32_bf16 v[220:223], a[88:91], a[156:159], v[220:223]\n\t" \
    "v_mfma_f32_16x16x32_bf16 v[192:195], a[76:79], a[160:163], v[192:195]\n\tv_mfma_f32_16x16x32_bf16 v[196:199], a[76:79], a[164:167], v[196:199]\n\t" \
    "v_mfma_f32_16x16x32_bf16 v[200:203], a[76:79], a[168:171], v[200:203]\n\tv_mfma_f32_16x16x32_bf16 v[204:207], a[76:79], a[172:175], v[204:207]\n\t" \
    "v_mfma_f32_16x16x32_bf16 v[208:211], a[92:95], a[160:163], v[208:211]\n\tv_mfma_f32_16x16x32_bf16 v[212:215], a[92:95], a[164:167], v[212:215]\n\t" \
    "v_mfma_f32_16x16x32_bf16 v[216:219], a[92:95], a[168:171], v[216:219]\n\tv_mfma_f32_16x16x32_bf16 v[220:223], a[92:95], a[172:175], v[220:223]\n\ts_waitcnt lgkmcnt(8)\n\t" \
    "s_barrier\n\tv_mfma_f32_16x16x32_bf16 v[224:227], a[0:3], a[112:115], v[224:227]\n\tv_cvt_pk_bf16_f32 v192, v192, v193\n\tv_cvt_pk_bf16_f32 v193, v194, v195\n\t" \
    "v_mfma_f32_16x16x32_bf16 v[228:231], a[0:3], a[116:119], v[228:231]\n\tv_cvt_pk_bf16_f32 v194, v208, v209\n\tv_cvt_pk_bf16_f32 v195, v210, v211\n\t" \
    "v_mfma_f32_16x16x32_bf16 v[232:235], a[0:3], a[120:123], v[232:235]\n\ts_add_u32 s47, %[sm], 5120\n\tbuffer_store_dwordx4 v[192:195], %[so], %[ws], s47 offen sc1\n\t" \
    "v_mfma_f32_16x16x32_bf16 v[236:239], a[0:3], a[124:127], v[236:239]\n\tds_write_b128 %[iw], v[192:195] offset:20480\n\tv_cvt_pk_bf16_f32 v196, v196, v197\n\t" \
    "v_mfma_f32_16x16x32_bf16 v[240:243], a[16:19], a[112:115], v[240:243]\n\tv_cvt_pk_bf16_f32 v197, v198, v199\n\tv_cvt_pk_bf16_f32 v198, v212, v213\n\t" \
    "v_mfma_f32_16x16x32_bf16 v[244:247], a[16:19], a[116:119], v[244:247]\n\tv_cvt_pk_bf16_f32 v199, v214, v215\n\ts_add_u32 s47, %[sm], 234496\n\t" \
    "v_mfma_f32_16x16x32_bf16 v[248:251], a[16:19], a[120:123], v[248:251]\n\tbuffer_store_dwordx4 v[196:199], %[so], %[ws], s47 offen sc1\n\t" \
    "ds_write_b128 %[iw], v[196:199] offset:21504\n\tv_mfma_f32_16x16x32_bf16 v[252:255], a[16:19], a[124:127], v[252:255]\n\tv_cvt_pk_bf16_f32 v200, v200, v201\n\t" \
    "v_cvt_pk_bf16_f32 v201, v202, v203\n\tv_mfma_f32_16x16x32_bf16 v[224:227], a[4:7], a[128:131], v[224:227]\n\tv_cvt_pk_bf16_f32 v202, v216, v217\n\t" \
    "v_cvt_pk_bf16_f32 v203, v218, v219\n\tv_mfma_f32_16x16x32_bf16 v[228:231], a[4:7], a[132:135], v[228:231]\n\ts_add_u32 s47, %[sm], 463872\n\t" \
    "buffer_store_dwordx4 v[200:203], %[so], %[ws], s47 offen sc1\n\tv_mfma_f32_16x16x32_bf16 v[232:235], a[4:7], a[136:139], v[232:235]\n\t" \
    "ds_write_b128 %[iw], v[200:203] offset:22528\n\tv_cvt_pk_bf16_f32 v204, v204, v205\n\tv_mfma_f32_16x16x32_bf16 v[236:239], a[4:7], a[140:143], v[236:239]\n\t" \
    "v_cvt_pk_bf16_f32 v205, v206, v207\n\tv_cvt_pk_bf16_f32 v206, v220, v221\n\tv_mfma_f32_16x16x32_bf16 v[240:243], a[20:23], a[128:131], v[240:243]\n\t" \
    "v_cvt_pk_bf16_f32 v207, v222, v223\n\ts_add_u32 s47, %[sm], 693248\n\tv_mfma_f32_16x16x32_bf16 v[244:247], a[20:23], a[132:135], v[244:247]\n\t" \
    "buffer_store_dwordx4 v[204:207], %[so3], %[ws], s47 offen sc1\n\tds_write_b128 %[iw], v[204:207] offset:23552\n\t" \
    "v_mfma_f32_16x16x32_bf16 v[248:251], a[20:23], a[136:139], v[248:251]\n\tv_mfma_f32_16x16x32_bf16 v[252:255], a[20:23], a[140:143], v[252:255]\n\t" \
    "v_mfma_f32_16x16x32_bf16 v[224:227], a[8:11], a[144:147], v[224:227]\n\tv_mfma_f32_16x16x32_bf16 v[228:231], a[8:11], a[148:151], v[228:231]\n\t" \
    "v_mfma_f32_16x16x32_bf16 v[232:235], a[8:11], a[152:155], v[232:235]\n\tv_mfma_f32_16x16x32_bf16 v[236:239], a[8:11], a[156:159], v[236:239]\n\t" \
    "v_mfma_f32_16x16x32_bf16 v[240:243], a[24:27], a[144:147], v[240:243]\n\tv_mfma_f32_16x16x32_bf16 v[244:247], a[24:27], a[148:151], v[244:247]\n\t" \
    "v_mfma_f32_16x16x32_bf16 v[248:251], a[24:27], a[152:155], v[248:251]\n\tv_mfma_f32_16x16x32_bf16 v[252:255], a[24:27], a[156:159], v[252:255]\n\t" \
    "v_mfma_f32_16x16x32_bf16 v[224:227], a[12:15], a[160:163], v[224:227]\n\tv_mfma_f32_16x16x32_bf16 v[228:231], a[12:15], a[164:167], v[228:231]\n\t" \
    "v_mfma_f32_16x16x32_bf16 v[232:235], a[12:15], a[168:171], v[232:235]\n\tv_mfma_f32_16x16x32_bf16 v[236:239], a[12:15], a[172:175], v[236:239]\n\t" \
    "v_mfma_f32_16x16x32_bf16 v[240:243], a[28:31], a[160:163], v[240:243]\n\tv_mfma_f32_16x16x32_bf16 v[244:247], a[28:31], a[164:167], v[244:247]\n\t" \
    "v_mfma_f32_16x16x32_bf16 v[248:251], a[28:31], a[168:171], v[248:251]\n\tv_mfma_f32_16x16x32_bf16 v[252:255], a[28:31], a[172:175], v[252:255]\n\ts_nop 3\n\t" \
    "v_cvt_pk_bf16_f32 v224, v224, v225\n\tv_cvt_pk_bf16_f32 v225, v226, v227\n\tv_cvt_pk_bf16_f32 v226, v240, v241\n\tv_cvt_pk_bf16_f32 v227, v242, v243\n\t" \
    "s_add_u32 s47, %[sm], 6144\n\tbuffer_store_dwordx4 v[224:227], %[so], %[ws], s47 offen sc1\n\tds_write_b128 %[iw], v[224:227] offset:24576\n\tv_cvt_pk_bf16_f32 v228, v228, v229\n\t" \
    "v_cvt_pk_bf16_f32 v229, v230, v231\n\tv_cvt_pk_bf16_f32 v230, v244, v245\n\tv_cvt_pk_bf16_f32 v231, v246, v247\n\ts_add_u32 s47, %[sm], 235520\n\t" \
    "buffer_store_dwordx4 v[228:231], %[so], %[ws], s47 offen sc1\n\tds_write_b128 %[iw], v[228:231] offset:25600\n\tv_cvt_pk_bf16_f32 v232, v232, v233\n\t" \
    "v_cvt_pk_bf16_f32 v233, v234, v235\n\tv_cvt_pk_bf16_f32 v234, v248, v249\n\tv_cvt_pk_bf16_f32 v235, v250, v251\n\ts_add_u32 s47, %[sm], 464896\n\t" \
    "buffer_store_dwordx4 v[232:235], %[so], %[ws], s47 offen sc1\n\tds_write_b128 %[iw], v[232:235] offset:26624\n\tv_cvt_pk_bf16_f32 v236, v236, v237\n\t" \
    "v_cvt_pk_bf16_f32 v237, v238, v239\n\tv_cvt_pk_bf16_f32 v238, v252, v253\n\tv_cvt_pk_bf16_f32 v239, v254, v255\n\ts_add_u32 s47, %[sm], 694272\n\t" \
    "buffer_store_dwordx4 v[236:239], %[so3], %[ws], s47 offen sc1\n\tds_write_b128 %[iw], v[236:239] offset:27648\n\t" W0T(6) "s_waitcnt vmcnt(0) lgkmcnt(0)\n\ts_barrier\n\t" W0T(7) ""
#define CL_W0 "v24","v25","v26","v27","v28","v29","v30","v31","v32","v33","v34","v35","v36","v37","v38","v39","v40","v41","v42","v43","v44","v45","v46","v47","v48","v49","v50","v51","v52","v53","v54","v55","v56","v57","v58","v59","v60","v61","v62","v63","v64","v65","v66","v67","v68","v69","v70","v71","v72","v73","v74","v75","v76","v77","v78","v79","v80","v81","v82","v83","v84","v85","v86","v87","v88","v89","v90","v91","v92","v93","v94","v95","v96","v97","v98","v99","v100","v101","v102","v103","v104","v105","v106","v107","v108","v109","v110","v111","v112","v113","v114","v115","v116","v117","v118","v119","v120","v121","v122","v123","v124","v125","v126","v127","v128","v129","v130","v131","v132","v133","v134","v135","v136","v137","v138","v139","v140","v141","v142","v143","v144","v145","v146","v147","v148","v149","v150","v151","v152","v153","v154","v155","v156","v157","v158","v159","v160","v161","v162","v163","v164","v165","v166","v167","v168","v169","v170","v171","v172","v173","v174","v175","v176","v177","v178","v179","v180","v181","v182","v183","v184","v185","v186","v187","v188","v189","v190","v191","v192","v193","v194","v195","v196","v197","v198","v199","v200","v201","v202","v203","v204","v205","v206","v207","v208","v209","v210","v211","v212","v213","v214","v215","v216","v217","v218","v219","v220","v221","v222","v223","v224","v225","v226","v227","v228","v229","v230","v231","v232","v233","v234","v235","v236","v237","v238","v239","v240","v241","v242","v243","v244","v245","v246","v247","v248","v249","v250","v251","v252","v253","v254","v255","a0","a1","a2","a3","a4","a5","a6","a7","a8","a9","a10","a11","a12","a13","a14","a15","a16","a17","a18","a19","a20","a21","a22","a23","a24","a25","a26","a27","a28","a29","a30","a31","a32","a33","a34","a35","a36","a37","a38","a39","a40","a41","a42","a43","a44","a45","a46","a47","a48","a49","a50","a51","a52","a53","a54","a55","a56","a57","a58","a59","a60","a61","a62","a63","a64","a65","a66","a67","a68","a69","a70","a71","a72","a73","a74","a75","a76","a77","a78","a79","a80","a81","a82","a83","a84","a85","a86","a87","a88","a89","a90","a91","a92","a93","a94","a95","a96","a97","a98","a99","a100","a101","a102","a103","a104","a105","a106","a107","a108","a109","a110","a111","a112","a113","a114","a115","a116","a117","a118","a119","a120","a121","a122","a123","a124","a125","a126","a127","a128","a129","a130","a131","a132","a133","a134","a135","a136","a137","a138","a139","a140","a141","a142","a143","a144","a145","a146","a147","a148","a149","a150","a151","a152","a153","a154","a155","a156","a157","a158","a159","a160","a161","a162","a163","a164","a165","a166","a167","a168","a169","a170","a171","a172","a173","a174","a175",W0CL "s44","s45","s46","s47","s48","s49","s54","s55","vcc","scc","memory"
template <int TYPE>
DEV u32 w0(const Params& p, LDS char* lds, int h, int q, int rot, int wave, u32 fx, u32 px, u32& h0) {
    const int lane = fresh_lane();
    const int x = lane & 15, g = lane >> 4, tid = wave * 64 + lane, hf = wave & 1, wr = wave >> 1;
    rsrc_t ra = mkrs(TYPE == 0 ? p.Wk : p.Wv, 448u * 4096u * 2u);
    rsrc_t rb = mkrs(TYPE == 0 ? p.Wq : p.Wo, 448u * 4096u * 2u);
    rsrc_t ws = mkrs(p.ws, WS_END);
    const u32 lb = (u32)(uintptr_t)lds;
    const u32 sw = (u32)(((lane & 7) ^ (4 * hf + g)) << 4);
    const u32 va = (u32)(8 * (lane >> 5) + ((lane >> 3) & 3)) * 8192u + sw;
    const u32 r0 = lb + x * 128 + ((g ^ (x >> 1)) << 4);
    u32 ok;
#define W0_IN [ra] "s"(ra), [rb] "s"(rb), [ws] "s"(ws), [sa] "s"(__builtin_amdgcn_readfirstlane((rot + 16 * hf + 4 * wr) * 8192 + h * 512)), [lm] "s"(lb + wave * 1024), \
        [va] "v"(va), [r0] "v"(r0 + wr * 28672), [r1] "v"((r0 ^ 64u) + wr * 28672), [iw] "v"(lb + hf * L_BUF + wr * 28672 + lane * 16), \
        [kq] "s"(KQ), [bo] "v"(beat_off(lane)), [fx] "v"(fx), [px] "v"(px) W0DBG
#define W0_H0 "s_nop 4\n\tbuffer_load_dword %[h0], %[bo], %[ws], 0 offen sc1\n\t"
    if (TYPE == 0) {
        asm volatile(W0_H0 W0_ASM0 W0END : [ok] "=&s"(ok), [h0] "=&v"(h0)
            : W0_IN, [sb] "s"(__builtin_amdgcn_readfirstlane((112 * q + 8 * wave) * 8192 + h * 512)), [vb0] "v"((u32)(lane >> 3) * 8192u + sw),
              [q0] "v"(r0 + 57344 + hf * 8192), [q1] "v"((r0 ^ 64u) + 57344 + hf * 8192) : CL_W0);
    } else {
        u32 vb[4];
#pragma unroll
        for (int i = 0; i < 4; ++i) {
            const u32 n = (u32)(tid + 256 * i), gr = __umul24(n, 8739u) >> 20, r = n - __umul24(gr, 120u);
            vb[i] = (__umul24(gr, 7168u) + __umul24(__umul24(r, 4682u) >> 16, 672u) + r * 16u) | (r >= 112u || gr >= 8u ? OOB : 0u);
        }
        asm volatile(W0_H0 W0_ASM1 W0END : [ok] "=&s"(ok), [h0] "=&v"(h0)
            : W0_IN, [sb] "s"((u32)(h * 256 * 896 + 224 * q)), [vb0] "v"(vb[0]), [vb1] "v"(vb[1]), [vb2] "v"(vb[2]), [vb3] "v"(vb[3]),
              [q0] "v"(lb + 57344 + g * 1920 + (x >> 2) * 224 + (x & 3) * 8 + hf * 128),
              [sm] "s"(O_BM + (u32)(((7 * q + 4 * hf) * 16 + h) * 14 + 7 * wr) * 1024u), [so] "v"(lane * 16), [so3] "v"(hf ? OOB : (u32)lane * 16u) : CL_W0);
    }
    return ok;
}

struct Cls { u32 c[4], fb; };
DEV Cls get_cls(rsrc_t ws, int lane, int& budget) {
    const u32 vo = (lane & 31) < 4 ? F_K + 128 + (lane & 3) * 1024 + (blockIdx.x & 3) * 128 : F_ARM + (lane & 31) * 128;
    int v;
    while (true) {
        MEMBAR();
        v = (int)ld1c(ws, vo, 0);
        if (__builtin_amdgcn_ballot_w64(v < 0) == 0ull || --budget < 0) break;
        __builtin_amdgcn_s_sleep(16);
    }
    MEMBAR();
    Cls r;
#pragma unroll
    for (int i = 0; i < 4; ++i) r.c[i] = (((u32)__builtin_amdgcn_readlane(v, i) & 3u) << 13) | ((u32)(i + 1) << 21);
    r.fb = ((u32)__builtin_amdgcn_readlane(v, 0) & 4u) << 11;
    return r;
}
DEV u32 pick(const Cls& c, int i) { return i == 0 ? c.c[0] : i == 1 ? c.c[1] : i == 2 ? c.c[2] : c.c[3]; }

DEV u32 gnow() { return (u32)__builtin_amdgcn_s_memrealtime() * 24u; }
DEV u32 rtt(char* a) {
    u64 t0, t1; u32 v, zero = 0;
    asm volatile("s_memtime %0\n\ts_waitcnt lgkmcnt(0)\n\tglobal_atomic_or %2, %3, %4, off sc0 sc1\n\ts_waitcnt vmcnt(0)\n\ts_memtime %1\n\ts_waitcnt lgkmcnt(0)"
                 : "=&s"(t0), "=&s"(t1), "=&v"(v) : "v"(a), "v"(zero) : "memory");
    return (u32)(t1 - t0);
}
#define FSX 0x2000u
#define T_PROBE 25000
DEV void probe(const Params& p, int xc, int j, int wave, int lane, u32 tb) {
    if (lane == 0) {
        char* a = p.ws + (j < 7 ? ((size_t)(xc + 1) << 21) + 60 * GRP + j * 128 : 4 * GRP + xc * 128) + wave * 2048;
        u32 m[4] = {~0u, ~0u, ~0u, ~0u};
        for (int r = 0; r < 64 && (r < 4 || gnow() - tb < T_PROBE); ++r) {
            const u32 d = rtt(a + ((r & 3) ^ 2) * 8192);
#pragma unroll
            for (int k = 0; k < 4; ++k) m[k] = ((r & 3) == k && d < m[k]) ? d : m[k];
        }
        st4c((u4){m[0], m[1], m[2], m[3]}, mkrs(p.ws, WS_END), 0, F_R + xc * 512 + (j * 4 + wave) * 16);
    }
}

#define CL_S "s44","s45","s46","s47","s48","s49","s50","s51","s52","s53","s54","s55","s56","s57","s58","s59","s60","s61","s62","s63","s64","s65","s66","s67","s68","s69","s70","s71","s72","s73","s74","s75","s76","s77","s78","s79","s80","s81","s82","s83","s84","s85","s86","s87","s88","s89","s90","s91","s92","s93","s94","s95","vcc","scc","memory"
#define CL_V8_63 "v8","v9","v10","v11","v12","v13","v14","v15","v16","v17","v18","v19","v20","v21","v22","v23","v24","v25","v26","v27","v28","v29","v30","v31","v32","v33","v34","v35","v36","v37","v38","v39","v40","v41","v42","v43","v44","v45","v46","v47","v48","v49","v50","v51","v52","v53","v54","v55","v56","v57","v58","v59","v60","v61","v62","v63"
#define CL_V64_119 "v64","v65","v66","v67","v68","v69","v70","v71","v72","v73","v74","v75","v76","v77","v78","v79","v80","v81","v82","v83","v84","v85","v86","v87","v88","v89","v90","v91","v92","v93","v94","v95","v96","v97","v98","v99","v100","v101","v102","v103","v104","v105","v106","v107","v108","v109","v110","v111","v112","v113","v114","v115","v116","v117","v118","v119"
#define CL_V120_175 "v120","v121","v122","v123","v124","v125","v126","v127","v128","v129","v130","v131","v132","v133","v134","v135","v136","v137","v138","v139","v140","v141","v142","v143","v144","v145","v146","v147","v148","v149","v150","v151","v152","v153","v154","v155","v156","v157","v158","v159","v160","v161","v162","v163","v164","v165","v166","v167","v168","v169","v170","v171","v172","v173","v174","v175"
#define CL_A "a0","a1","a2","a3","a4","a5","a6","a7","a8","a9","a10","a11","a12","a13","a14","a15","a16","a17","a18","a19","a20","a21","a22","a23","a24","a25","a26","a27","a28","a29","a30","a31","a32","a33","a34","a35","a36","a37","a38","a39","a40","a41","a42","a43","a44","a45","a46","a47","a48","a49","a50","a51","a52","a53","a54","a55","a56","a57","a58","a59","a60","a61","a62","a63","a64","a65","a66","a67","a68","a69","a70","a71","a72","a73","a74","a75","a76","a77","a78","a79","a80","a81","a82","a83","a84","a85","a86","a87","a88","a89","a90","a91","a92","a93","a94","a95","a96","a97","a98","a99","a100","a101","a102","a103","a104","a105","a106","a107","a108","a109","a110","a111","a112","a113","a114","a115","a116","a117","a118","a119","a120","a121","a122","a123","a124","a125","a126","a127","a128","a129","a130","a131","a132","a133","a134","a135","a136","a137","a138","a139","a140","a141","a142","a143","a144","a145","a146","a147","a148","a149","a150","a151","a152","a153","a154","a155","a156","a157","a158","a159","a160","a161","a162","a163","a164","a165","a166","a167","a168","a169","a170","a171","a172","a173","a174","a175","a176","a177","a178","a179","a180","a181","a182","a183","a184","a185","a186","a187","a188","a189","a190","a191","a192","a193","a194","a195","a196","a197","a198","a199","a200","a201","a202","a203","a204","a205","a206","a207","a208","a209","a210","a211","a212","a213","a214","a215","a216","a217","a218","a219","a220","a221","a222","a223"
#define STR2(x) #x
#define STR(x) STR2(x)
#define T_P "(" STR(T_A) "+" STR(T_B) ")"
#define BURST \
    "s_memtime s[64:65]\n\ts_memrealtime s[52:53]\n\ts_nop 2\n\ts_memrealtime s[54:55]\n\ts_nop 2\n\ts_memrealtime s[56:57]\n\ts_nop 2\n\t" \
    "s_memrealtime s[58:59]\n\ts_nop 2\n\ts_memrealtime s[60:61]\n\ts_nop 2\n\ts_memrealtime s[62:63]\n\ts_waitcnt lgkmcnt(0)\n\t" \
    "s_add_u32 s52, s52, s54\n\ts_add_u32 s56, s56, s58\n\ts_add_u32 s60, s60, s62\n\ts_add_u32 s52, s52, s56\n\ts_add_u32 s52, s52, s60\n\t" \
    "s_lshl_b32 s52, s52, 2\n\ts_sub_u32 s68, s52, s64\n\t"
#define SLED_INIT(R0, R1, L) "s_getpc_b64 s[" R0 ":" R1 "]\n\ts_add_u32 s" R0 ", s" R0 ", " L "%=-.\n\ts_addc_u32 s" R1 ", s" R1 ", 0\n\t"
#define SLED(R0, R1, L) \
    "s_max_i32 s50, s50, 0\n\ts_min_i32 s50, s50, 400\n\ts_and_b32 s50, s50, -4\n\ts_sub_u32 s50, s" R0 ", s50\n\ts_subb_u32 s51, s" R1 ", 0\n\t" \
    "s_setpc_b64 s[50:51]\n\t.rept 100\n\ts_nop 0\n\t.endr\n" L "%=:\n\t"
#define GW(L, TOL) \
    L "a%=:\n\ts_memtime s[50:51]\n\ts_waitcnt lgkmcnt(0)\n\ts_sub_u32 s50, s48, s50\n\ts_cmp_lt_i32 s50, -" TOL "\n\ts_cbranch_scc1 recal%=\n\t" \
    "s_sub_u32 s51, s50, 400\n\ts_cmp_lt_u32 s51, 8000\n\ts_cbranch_scc0 " L "b%=\n\ts_sleep 2\n\ts_branch " L "a%=\n" L "b%=:\n\t"
#define T_RE 200
#define T_ST 3400
template <int SLP>
DEV u32 grid0(rsrc_t ws, u32 fb, int lane, int& budget) {
    u32 v;
    while (true) {
        MEMBAR();
        v = ld1c(ws, F_L + (fb ^ 0x2000u) + (lane & 15) * 128, 0);
        if (__builtin_amdgcn_ballot_w64(v == ~0u) == 0ull || --budget < 0) break;
        __builtin_amdgcn_s_sleep(SLP);
    }
    const u32 v0 = __builtin_amdgcn_readfirstlane(v);
    int d = 0;
#pragma unroll
    for (int i = 1; i < 16; ++i) { const int e = (int)(__builtin_amdgcn_readlane(v, i) - v0); d = e > d ? e : d; }
    return __builtin_amdgcn_readfirstlane(v0 + (u32)d);
}
#define PW0(L) L "a%=:\n\ts_memrealtime s[50:51]\n\ts_waitcnt lgkmcnt(0)\n\ts_mul_i32 s50, s50, 24\n\ts_sub_u32 s50, s48, s50\n\t"
#define PW1(L) "s_sub_u32 s51, s50, 410\n\ts_cmp_lt_u32 s51, 8000\n\ts_cbranch_scc0 " L "b%=\n\ts_sleep 1\n\ts_branch " L "a%=\n"
#define PW2(L, OFF) L "b%=:\n\ts_cmp_lt_i32 s50, 170\n\ts_cbranch_scc1 " L "d%=\n\t" \
    BURST "s_sub_u32 s50, s69, s52\n\ts_add_u32 s50, s50, " OFF "\n" L "d%=:\n\t"
#define PW(L, OFF) PW0(L) PW1(L) PW2(L, OFF)
#define GW2(L, TOL, OFF) PW0(L) "s_cmp_lt_i32 s50, -" TOL "\n\ts_cbranch_scc1 " L "c%=\n\t" PW1(L) \
    L "c%=:\n\ts_add_u32 s48, s48, " T_P "\n\ts_add_u32 s69, s69, " T_P "\n\ts_branch " L "a%=\n" PW2(L, OFF)
#define KH(R) \
    "s_lshr_b32 s50, " R ", 7\n\ts_and_b32 s50, s50, 0x800\n\ts_lshr_b32 s51, " R ", 5\n\ts_and_b32 s51, s51, 0x4000\n\ts_or_b32 s50, s50, s51\n\t" \
    "s_lshr_b32 s51, " R ", 8\n\ts_and_b32 s51, s51, 0x1000\n\ts_or_b32 s50, s50, s51\n\ts_or_b32 " R ", " R ", s50\n\t"
#define KMEM(R, S) "s_and_b32 " R ", " S ", 7\n\ts_lshl_b32 " R ", " R ", 17\n\t" KH(R)
#define KSC(R, S) "s_and_b32 " R ", " S ", 3\n\ts_lshl_b32 " R ", " R ", 18\n\ts_or_b32 " R ", " R ", 0x100000\n\t" KH(R)
#define X4(K) "s_xor_b32 s70, %[c0], " K "\n\ts_xor_b32 s71, %[c1], " K "\n\ts_xor_b32 s72, %[c2], " K "\n\ts_xor_b32 s73, %[c3], " K "\n\t"
#define FLIP4 "s_xor_b32 s70, s70, s76\n\ts_xor_b32 s71, s71, s76\n\ts_xor_b32 s72, s72, s76\n\ts_xor_b32 s73, s73, s76\n\t"
#define SMF64(D, A, W) "v_smfmac_f32_16x16x64_bf16 v[" D ":" D "+3], v[" A ":" A "+3], " W ", %[idx]\n\t"
#define LGK(n) "s_waitcnt lgkmcnt(" #n ")\n\t"
#define READ_A \
    ".irp k,0,1,2,3,4,5,6\n\tds_read_b128 v[8+4*\\k:11+4*\\k], %[la] offset:1024*\\k\n\t.endr\n\t"
#define MAX_A \
    "v_max3_u32 v44, v8, v9, v10\n\t.irp k,11,13,15,17,19,21,23,25,27,29,31,33\n\tv_max3_u32 v44, v44, v\\k, v[\\k+1]\n\t.endr\n\t" \
    "v_max_u32 v44, v44, v35\n\tv_cmp_eq_u32 vcc, -1, v44\n\t"
#define DLOSS
#define DT(n)
#define DCNT
#define DOUT(T)
#define DTGT(R)
#define DINIT
#define DBGOP

#define S_HEAD(EXTRA) \
    "s_mov_b32 s45, %[s0]\n\ts_mov_b32 s47, 20000\n\ts_mov_b32 m0, %[m0v]\n\ts_mov_b32 s69, %[t0]\n\ts_mov_b32 s76, 0x84000\n\t" DINIT SLED_INIT("66", "67", "we") \
    LGK(0) BURST \
    "retry%=:\n\t" \
    ".irp k,36,37,38,39,40,41,42,43\n\tv_mov_b32 v\\k, 0\n\t.endr\n\t" \
    KMEM("s44", "s45") "s_xor_b32 s44, s44, %[cm]\n\ts_sub_u32 s49, s44, 1024\n\t" EXTRA \
    "s_add_u32 s69, s69, " T_P "\n\ts_sub_u32 s48, s69, 190\n\t" \
    GW2("w", "600", "-212") SLED("66", "67", "we") DT(0) DTGT("s69")
#define S_SLOW \
    "slow%=:\n\t" MAX_A "s_cbranch_vccz cont%=\n\ts_sub_u32 s47, s47, 1\n\ts_cbranch_scc1 dead%=\n\t" DCNT \
    LGK(0) "s_mov_b32 s76, 0\n\ts_barrier\n\ts_branch retry%=\n" \
    "dead%=:\n\ts_mov_b32 s47, 0\n\ts_branch cont%=\n"
#define S_NEXT "s_mov_b32 s76, 0x84000\n\ts_add_u32 s45, s45, 1\n\ts_and_b32 s50, s45, 62\n\ts_cbranch_scc1 retry%=\n\ts_branch done%=\n"
#define FOLD2(D, T) \
    "v_permlane32_swap_b32 v" D ", v[" D "+2]\n\tv_permlane32_swap_b32 v[" D "+1], v[" D "+3]\n\t" \
    "v_pk_add_f32 v[" D ":" D "+1], v[" D ":" D "+1], v[" D "+2:" D "+3]\n\t"

DEV void role_s(const Params& p, LDS char* lds, int xc, int h, int q, int wave) {
    u32 h0;
    const u32 ok = w0<0>(p, lds, h, q, 112 * q, wave, OOB, OOB, h0);
    const int lane = fresh_lane();
    const int x = lane & 15, g = lane >> 4;
    rsrc_t ws = mkrs(p.ws, WS_END);
    int budget = 400000;
    constexpr int L_UM = 8192;
    if (wave == 0) *(LDS u32*)(lds + 57344 + L_UM + 112 * 16 + lane * 4) = 0u;
    STAMP(0, 0, 6);
    if (!ok) wait_beat(ws, lane, h0, budget);
    const Cls cl = get_cls(ws, lane, budget);
    const u32 cm = pick(cl, xc);
    int b0 = 7 * q + 8 * wave + g, b1 = b0 + 4;
    b0 = b0 >= 28 ? b0 - 28 : b0; b1 = b1 >= 28 ? b1 - 28 : b1;
    const u32 g0 = (b0 >> 3) * GRP + (b0 & 7) * 256 + x * 16, g1 = (b1 >> 3) * GRP + (b1 & 7) * 256 + x * 16;
    const u32 lb = (u32)(uintptr_t)lds + 57344;
    const u32 la = lb + lane * 16, wa = (u32)(uintptr_t)lds + (wave >> 1) * L_BUF + (wave & 1) * 2048 + lane * 16;
    const int idx = (lane & 8) ? 0xEEEE : 0x4444;
    const int kk = x >> 2, nbq = x & 3;
    const u32 umw = lb + L_UM + wave * 512 + x * 16 + (2 * (g & 1) + (g >> 1)) * 4;
    const u32 m0v = lb + wave * 2048;
    const u32 so = (lane & 8) ? OOB : (h >> 2) * 2 * GRP + (q >> 1) * 1024 + (h & 3) * 256 + (q & 1) * 128 + (lane & 7) * 16 + ((g ^ q) & 1) * 8 + (g >> 1) * 4;
    if (wave == 3)
#pragma unroll
        for (int k = 0; k < 8; ++k) st1c(~0u, ws, so, cl.c[k & 3] ^ kgrp(32 + 8 * (k >> 2)));
#pragma clang loop unroll(disable)
    for (int s0 = 0; s0 < 2; ++s0) {
    const u32 t0 = s0 ? grid0<40>(ws, cl.fb, lane, budget) : gnow() + 800u - (T_A + T_B);
    STAMP(0, 0, s0);
    if (wave < 3) {
        const u32 trp = lb + L_UM + (16 * g + 8 * (nbq >> 1) + kk) * 16 + (nbq & 1) * 8;
        const u32 u0 = O_UM + h * 7168 + q * 1792 + lane * 16, u1 = g < 3 ? u0 + 1024 : OOB;
        asm volatile(
            "s_nop 4\n\tv_add_u32 v44, 28672, %[wa]\n\t"
            ".irp m,0,1,2,3,4,5,6\n\t"
            "ds_read_b128 v[64+4*\\m:67+4*\\m], %[wa] offset:4096*\\m\n\tds_read_b128 v[120+4*\\m:123+4*\\m], %[wa] offset:4096*\\m+1024\n\t"
            "ds_read_b128 v[92+4*\\m:95+4*\\m], v44 offset:4096*\\m\n\tds_read_b128 v[148+4*\\m:151+4*\\m], v44 offset:4096*\\m+1024\n\t"
            "s_waitcnt lgkmcnt(4)\n\t"
            ".endr\n\t"
            S_HEAD("")
            "buffer_load_dwordx4 %[g0], %[ws], s44 offen sc1 lds\n\t"
            "buffer_load_dwordx4 %[g1], %[ws], s49 offen offset:1024 sc1 lds\n\t"
            "s_mul_i32 s77, s45, 16*7168\n\tv_mov_b32 v56, s45\n\t"
            "s_waitcnt vmcnt(0)\n\t" DT(1) "s_barrier\n\t"
            READ_A
            LGK(6) SMF64("36", "8", "v[64:71]") SMF64("40", "8", "v[120:127]")
            LGK(5) SMF64("36", "12", "v[72:79]") SMF64("40", "12", "v[128:135]")
            LGK(4) SMF64("36", "16", "v[80:87]") SMF64("40", "16", "v[136:143]")
            LGK(3) SMF64("36", "20", "v[88:95]") SMF64("40", "20", "v[144:151]")
            LGK(2) SMF64("36", "24", "v[96:103]") SMF64("40", "24", "v[152:159]")
            LGK(0) SMF64("36", "28", "v[104:111]") SMF64("36", "32", "v[112:119]")
            SMF64("40", "28", "v[160:167]") SMF64("40", "32", "v[168:175]")
            "s_nop 0\n\t"
            FOLD2("36", "")
            "v_cvt_pk_bf16_f32 v44, v36, v37\n\tv_cmp_u_f32 vcc, v36, v37\n\tds_write_b32 %[umw], v44\n\ts_nop 0\n\t"
            FOLD2("40", "")
            "v_cvt_pk_bf16_f32 v45, v40, v41\n\tds_write_b32 %[umw], v45 offset:256\n\t"
            "s_cbranch_vccnz slow%=\n"
            "cont%=:\n\t" LGK(0) "s_barrier\n\t" DT(2)
            "buffer_store_dword v56, %[fs], %[ws], 0 offen sc1\n\t"
            "s_cmp_eq_u32 %[wv], 0\n\ts_cbranch_scc0 nopub%=\n\ts_sleep 2\n\t"
            "ds_read_b64_tr_b16 v[48:49], %[trp]\n\tds_read_b64_tr_b16 v[50:51], %[trp] offset:64\n\t"
            "ds_read_b64_tr_b16 v[52:53], %[trp] offset:1024\n\tds_read_b64_tr_b16 v[54:55], %[trp] offset:1088\n\t"
            LGK(0) "s_nop 15\n\t"
            "buffer_store_dwordx4 v[48:51], %[u0], %[ws], s77 offen sc1\n\t"
            "buffer_store_dwordx4 v[52:55], %[u1], %[ws], s77 offen sc1\n\ts_nop 1\n"
            "nopub%=:\n\t" DOUT("48")
            S_NEXT S_SLOW
            "done%=:\n\tv_mov_b32 v56, 64\n\ts_waitcnt vmcnt(0)\n\ts_barrier\n\tbuffer_store_dword v56, %[ff], %[ws], 0 offen sc1\n\ts_waitcnt vmcnt(0)"
            :
            : [ws] "s"(ws), [cm] "s"(cm), [m0v] "s"(m0v), [wv] "s"(wave), [t0] "s"(t0), [s0] "s"(s0),
              [g0] "v"(g0), [g1] "v"(g1), [la] "v"(la), [wa] "v"(wa), [idx] "v"(idx), [umw] "v"(umw),
              [trp] "v"(trp), [u0] "v"(u0), [u1] "v"(u1), [fs] "v"((wave != 0 && lane == 0) ? (wave == 1 ? F_S + (cl.fb ^ FSX) : F_U + cl.fb) + h * 128 + q * 4 : OOB),
              [ff] "v"((wave == 0 && lane == 0 && s0) ? F_U + cl.fb + h * 128 + q * 4 : OOB) DBGOP
            : CL_S, CL_V8_63, CL_V64_119, CL_V120_175);
    } else {
        const u32 trf = lb + L_UM + (16 * g + 4 * (kk >> 1) + 2 * (nbq >> 1) + (kk & 1)) * 16 + (nbq & 1) * 8;
        asm volatile(
            "s_nop 4\n\tv_add_u32 v44, 28672, %[wa]\n\t"
            ".irp m,0,1,2,3,4,5,6\n\t"
            "ds_read_b128 v[64+4*\\m:67+4*\\m], %[wa] offset:4096*\\m\n\tds_read_b128 v[92+4*\\m:95+4*\\m], v44 offset:4096*\\m\n\t"
            ".endr\n\t"
            "v_mov_b32 v60, -1\n\tv_mov_b32 v61, -1\n\t"
            S_HEAD(KSC("s75", "s45") X4("s75"))
            "buffer_load_dwordx4 %[g0], %[ws], s44 offen sc1 lds\n\t"
            FLIP4 "s_sleep 2\n\t"
            ".irp k,70,71,72,73\n\tbuffer_store_dword v60, %[so], %[ws], s\\k offen sc1\n\t.endr\n\t" FLIP4
            "s_waitcnt vmcnt(4)\n\t" DT(1) "s_barrier\n\t"
            READ_A
            LGK(6) SMF64("36", "8", "v[64:71]") LGK(5) SMF64("36", "12", "v[72:79]") LGK(4) SMF64("36", "16", "v[80:87]")
            LGK(3) SMF64("36", "20", "v[88:95]") LGK(2) SMF64("36", "24", "v[96:103]") LGK(1) SMF64("36", "28", "v[104:111]")
            LGK(0) SMF64("36", "32", "v[112:119]")
            "s_nop 7\n\t"
            FOLD2("36", "")
            "v_cvt_pk_bf16_f32 v44, v36, v37\n\tv_cmp_u_f32 vcc, v36, v37\n\tds_write_b32 %[umw], v44\n\t"
            "s_nop 1\n\ts_cbranch_vccnz slow%=\n"
            "cont%=:\n\ts_cmp_eq_u32 s45, 63\n\t" LGK(0) "s_barrier\n\t"
            "ds_read_b64_tr_b16 v[48:49], %[trf]\n\tds_read_b64_tr_b16 v[50:51], %[trf] offset:128\n\t"
            "ds_read_b64_tr_b16 v[52:53], %[trf] offset:1024\n\tds_read_b64_tr_b16 v[54:55], %[trf] offset:1152\n\t"
            "s_cbranch_scc1 done%=\n\t"
            LGK(2) "v_mfma_f32_16x16x32_bf16 v[56:59], v[48:51], v[8:11], 0\n\t"
            LGK(0) "v_mfma_f32_16x16x32_bf16 v[56:59], v[52:55], v[12:15], v[56:59]\n\t"
            "s_nop 7\n\t"
            "v_permlane32_swap_b32 v56, v58\n\tv_permlane32_swap_b32 v57, v59\n\ts_nop 0\n\t"
            "v_add_f32_dpp v56, v58, v56 row_ror:8 row_mask:0xf bank_mask:0xf\n\t"
            "v_add_f32_dpp v57, v59, v57 row_ror:8 row_mask:0xf bank_mask:0xf\n\t"
            "v_cvt_pkrtz_f16_f32 v56, v56, v57\n\t"
            ".irp k,70,71,72,73\n\tbuffer_store_dword v56, %[so], %[ws], s\\k offen sc1\n\t.endr\n\t" DT(2)
            DOUT("48")
            S_NEXT S_SLOW
            "done%=:\n\ts_waitcnt vmcnt(0) lgkmcnt(0)\n\ts_barrier"
            :
            : [ws] "s"(ws), [cm] "s"(cm), [m0v] "s"(m0v), [c0] "s"(cl.c[0]), [c1] "s"(cl.c[1]), [c2] "s"(cl.c[2]), [c3] "s"(cl.c[3]), [g0] "v"(g0), [la] "v"(la), [wa] "v"(wa),
              [idx] "v"(idx), [umw] "v"(umw), [trf] "v"(trf), [so] "v"(so), [t0] "s"(t0), [s0] "s"(s0) DBGOP
            : CL_S, CL_V8_63, CL_V64_119);
    }
    }
}

#define V_LOADS \
    "buffer_load_dwordx4 v[64:67], %[sof], %[ws], s75 offen sc1\n\tbuffer_load_dwordx4 v[68:71], %[sof], %[ws], s75 offen offset:1024 sc1\n\t" \
    "buffer_load_dwordx2 v[76:77], %[sto], %[ws], s93 offen sc1\n\t"
#define MIX2(D, A) "v_fma_mix_f32 v" D ", v" A ", 1.0, v[" A "+4] op_sel_hi:[1,0,1]\n\tv_fma_mix_f32 v[" D "+1], v" A ", 1.0, v[" A "+4] op_sel:[1,0,1] op_sel_hi:[1,0,1]\n\t"
#define V_LOADY "buffer_load_dwordx4 v[56:59], %[yo], %[ws], s92 offen sc1\n\t"
#define V_MM(D0, T0, D1, T1, K) SMF64(D0, "8+4*" K, "a[56*" T0 "+8*" K ":56*" T0 "+8*" K "+7]") SMF64(D1, "8+4*" K, "a[56*" T1 "+8*" K ":56*" T1 "+8*" K "+7]")
#define VMX(K) "v_max3_u32 v78, v78, v" K ", v[" K "+1]\n\tv_max3_u32 v78, v78, v[" K "+2], v[" K "+3]\n\t"
#define SWA(K) "v_permlane32_swap_b32 v[36+" K "], v[44+" K "]\n\t"
#define V_HALF2(MID) \
    V_MM("40", "1", "48", "3", "0") V_MM("40", "1", "48", "3", "1") V_MM("40", "1", "48", "3", "2") V_MM("40", "1", "48", "3", "3") \
    V_MM("40", "1", "48", "3", "4") V_MM("40", "1", "48", "3", "5") V_MM("40", "1", "48", "3", "6") MID \
    "s_nop 7\n\t" SWA("4") SWA("5") SWA("6") SWA("7") \
    ".irp k,4,6\n\tv_pk_add_f32 v[36+\\k:37+\\k], v[36+\\k:37+\\k], v[44+\\k:45+\\k]\n\t.endr\n\t" \
    "v_cvt_pk_bf16_f32 v53, v40, v41\n\tv_cvt_pk_bf16_f32 v55, v42, v43\n\t" \
    "s_nop 1\n\tv_permlane16_swap_b32 v52, v53\n\tv_permlane16_swap_b32 v54, v55\n\t" \
    "buffer_store_dwordx4 v[52:55], %[l16], %[ws], s77 offen sc1\n\t"

DEV void role_v(const Params& p, LDS char* lds, int xc, int vl, int wave) {
    const u32 tb = gnow();
    const u32 h0 = beat0(p);
    const int v = 7 * xc + vl;
    const int lane = fresh_lane();
    const int x = lane & 15, g = lane >> 4, i = lane & 7;
    rsrc_t ws = mkrs(p.ws, WS_END);
    int budget = 400000;
    for (int u = vl * 4 + wave; u < 256; u += 28) {
        const int ch = u & 63;
        if (ch < 4) continue;
        const u32 cb = ((u32)(u >> 6) << 13) | ((u32)(xc + 1) << 21);
        const u32 o = ch < 16 ? cb ^ kgrp(ch) : ch < 32 ? cb ^ kgrp(ch + 16) : (cb ^ kgrp((ch - 32) >> 1) ^ 0x1000u ^ ((ch & 1) << 11));
        st4c((u4){~0u, ~0u, ~0u, ~0u}, ws, lane * 16, o);
        st4c((u4){~0u, ~0u, ~0u, ~0u}, ws, lane * 16 + 1024, o);
    }
    const int osel = lane >> 5, oi = (lane >> 2) & 7, op = lane & 3, oc = 4 * op + 2 * osel;
    constexpr int L_R = 16384;
    const u32 lb = (u32)(uintptr_t)lds;
    const int idx = (lane & 8) ? 0xEEEE : 0x4444;
    const int idxy = (2 * (i >> 2) + (i & 1)) * 0x55;
    const f2 mk = {(!(lane & 8) && ((i >> 1) & 1) == 0) ? 1.f : 0.f, (!(lane & 8) && ((i >> 1) & 1) == 1) ? 1.f : 0.f};
    const u32 la = lb + lane * 16;
    const u32 rw = lb + L_R + wave * 512 + lane * 4;
    const u32 rr = lb + L_R + ((oi & 1) * 64 + (((oi >> 2) & 1) + 2 * ((oi >> 1) & 1)) * 16 + oc) * 4;
#pragma clang loop unroll(disable)
    for (int ps = 0; ps < 2; ++ps) {
    const u32 ob = ps ? 0u : OOB;
    const u32 mo = ((v >> 3) * GRP + (v & 7) * 256 + lane * 4) | ob;
    Cls cl = {{0u, 0u, 0u, 0u}, 0u};
    u32 t0 = gnow() + 1000000u;
    if (ps) {
    VMCNT(0);
    bar();
    wait_beat(ws, lane, h0, budget);
    if (wave == 0 && lane == 0) st1c(0u, ws, F_ARM + (4 + v) * 128, 0);
    STAMP(1, 0, 6);
    VMCNT(0);
    bar();
    probe(p, xc, vl, wave, lane, tb);
    asm volatile("" :: "v"(ld1c(ws, lane < 5 ? (lane ? O_BM + (lane << 21) : O_MB) - 128u : OOB, 0)));
    wait_flags(ws, F_B + (x * 4 + xc) * 128, true, 0, budget, 1);
    STAMP(1, 0, 0);
    asm volatile(
        "s_nop 4\n\ts_mov_b32 s77, %[wb]\n\t"
        ".irp j,0,1,2,3,4,5,6,7,8,9,10,11,12,13\n\t"
        ".irp k,0,1,2,3\n\tbuffer_load_dwordx4 a[16*\\j+4*\\k:16*\\j+4*\\k+3], %[l16], %[ws], s77 offen offset:1024*\\k sc1\n\t.endr\n\t"
        "s_add_u32 s77, s77, 4096\n\t"
        ".endr"
        :: [ws] "s"(ws), [wb] "s"(O_BM + (u32)(v * 16 + 4 * wave) * 14336u), [l16] "v"(lane * 16) : "s77", "scc", "memory", CL_A);
    cl = get_cls(ws, lane, budget);
#pragma unroll
    for (int k = 1; k < 4; ++k) st1c(~0u, ws, mo, pick(cl, wave) ^ kgrp(4 * k));
    t0 = grid0<4>(ws, cl.fb, lane, budget);
    }
    t0 -= T_A + T_B;
    const u32 cm = pick(cl, xc);
    const u32 l16 = (lane * 16) | ob, gl = wave * GRP + l16;
    const u32 sof = wave * 2 * GRP + l16;
    const u32 sto = (7 * GRP + (4 * wave + g) * 128 + i * 8) | ob;
    const u32 yo = vl * GRP + wave * 1024 + l16;
    const u32 fm = lane == 0 ? F_M + cl.fb + v * 128 + wave * 4 : OOB;
    STAMP(1, 0, 5 - ps);
    asm volatile(
        "s_nop 4\n\t"
        "s_mov_b32 s45, 0\n\ts_mov_b32 s47, 20000\n\ts_or_b32 s74, %[ob], 20000\n\ts_mov_b32 m0, %[m0v]\n\t" DINIT
        SLED_INIT("78", "79", "e2")
        "s_mov_b32 s69, %[t0]\n\ts_mov_b32 s76, 0\n\ts_mov_b32 s77, %[mbb]\n\ts_mov_b32 s44, %[cm]\n\t"
        "v_mov_b32 v100, -1\n\ts_waitcnt vmcnt(0)\n"
        "recal%=:\n\t" BURST
        "rc%=:\n\ts_sub_u32 s50, s69, s52\n\ts_cmp_lt_i32 s50, -300-" T_P "\n\ts_cbranch_scc0 top%=\n\ts_add_u32 s69, s69, " T_P "\n\ts_branch rc%=\n"
        "top%=:\n\t"
        "s_add_u32 s69, s69, " T_P "\n\ts_add_u32 s48, s69, " STR(T_VM) "-60\n"
        PW0("m1") "s_cmp_lt_i32 s50, -400\n\ts_cbranch_scc1 recal%=\n\ts_cmp_lt_u32 s50, 8000\n\ts_cbranch_scc1 m1a%=\n\t" DT(0) DTGT("s69")
        "buffer_load_dwordx4 %[gl], %[ws], s44 offen sc1 lds\n\t"
        "buffer_load_dwordx4 %[gl2], %[ws], s44 offen offset:1024 sc1 lds\n\t"
        "s_sleep 2\n\t"
        "s_xor_b32 s70, s44, %[cx]\n\ts_xor_b32 s70, s70, 0x84000\n\tbuffer_store_dword v100, %[mo], %[ws], s70 offen sc1\n\t"
        ".irp k,36,37,38,39,40,41,42,43,44,45,46,47,48,49,50,51,96,97,98,99\n\tv_mov_b32 v\\k, 0\n\t.endr\n\t"
        "s_add_u32 s49, s45, 1\n\t" KMEM("s49", "s49") "s_xor_b32 s49, s49, %[cm]\n\ts_xor_b32 s70, s49, %[cx]\n\t"
        KSC("s75", "s45") "s_xor_b32 s75, s75, %[cm]\n\t"
        "s_mov_b32 s93, s76\n\t" KH("s93") "s_xor_b32 s93, s93, %[cm]\n\ts_xor_b32 s93, s93, 0x1000\n\ts_andn2_b32 s92, s93, 0x800\n\t"
        "s_waitcnt vmcnt(1)\n\t" DT(1) "s_barrier\n\t"
        READ_A
        "s_add_u32 s48, s69, " STR(T_A) "-190\n\t"
        LGK(6) V_MM("36", "0", "44", "2", "0") "v_max3_u32 v78, v8, v9, v10\n\tv_max_u32 v78, v78, v11\n\t"
        LGK(5) V_MM("36", "0", "44", "2", "1") VMX("12") LGK(4) V_MM("36", "0", "44", "2", "2") VMX("16")
        LGK(3) V_MM("36", "0", "44", "2", "3") VMX("20") LGK(2) V_MM("36", "0", "44", "2", "4") VMX("24")
        LGK(1) V_MM("36", "0", "44", "2", "5") VMX("28") LGK(0) V_MM("36", "0", "44", "2", "6") VMX("32")
        "v_cmp_eq_u32 vcc, -1, v78\n\ts_nop 4\n\t" SWA("0") SWA("1") SWA("2") SWA("3")
        "v_pk_add_f32 v[36:37], v[36:37], v[44:45]\n\tv_pk_add_f32 v[38:39], v[38:39], v[46:47]\n\t"
        "v_cvt_pk_bf16_f32 v52, v36, v37\n\tv_cvt_pk_bf16_f32 v54, v38, v39\n\t"
        "s_cbranch_vccnz slowm%=\n"
        "contm%=:\n\ts_cmp_eq_u32 s45, 63\n\ts_cbranch_scc1 last%=\n\t"
        PW("m2", STR(T_A) "-224")
        "s_add_u32 s51, s50, 120\n\ts_cmp_gt_u32 s51, 600\n\ts_cselect_b64 s[56:57], -1, 0\n\t"
        SLED("78", "79", "e2") DT(2)
        V_LOADS
        V_HALF2(V_LOADY)
        "soft%=:\n\t"
        "s_waitcnt vmcnt(3)\n\t" DT(3)
        MIX2("72", "66") MIX2("74", "67") MIX2("60", "64") MIX2("62", "65")
        ".irp k,0,1,2,3\n\tv_add_f32_dpp v[60+\\k], v[72+\\k], v[60+\\k] row_ror:8 row_mask:0xf bank_mask:0xf\n\t.endr\n\t"
        "v_max3_f32 v80, v60, v61, v62\n\ts_waitcnt vmcnt(2)\n\tv_max3_f32 v80, v76, v63, v80\n\tv_mul_f32 v77, 0.5, v77\n\ts_nop 0\n\t"
        "v_max_f32_dpp v80, v80, v80 row_ror:8 row_mask:0xf bank_mask:0xf\n\t"
        "v_sub_f32 v82, v76, v80\n\t"
        "v_pk_add_f32 v[60:61], v[60:61], v[80:81] op_sel_hi:[1,0] neg_lo:[0,1] neg_hi:[0,1]\n\t"
        "v_pk_add_f32 v[62:63], v[62:63], v[80:81] op_sel_hi:[1,0] neg_lo:[0,1] neg_hi:[0,1]\n\t"
        "v_exp_f32 v82, v82\n\tv_exp_f32 v60, v60\n\tv_exp_f32 v61, v61\n\tv_exp_f32 v62, v62\n\tv_exp_f32 v63, v63\n\t"
        "v_fma_f32 v84, v82, v77, v60\n\tv_add_f32 v84, v84, v61\n\tv_add_f32 v85, v62, v63\n\tv_add_f32 v84, v84, v85\n\t"
        "v_pk_mul_f32 v[86:87], %[mk], v[82:83] op_sel_hi:[1,0]\n\ts_nop 0\n\t"
        "v_add_f32_dpp v84, v84, v84 row_ror:8 row_mask:0xf bank_mask:0xf\n\t"
        "v_rcp_f32 v84, v84\n\ts_nop 0\n\t"
        "v_pk_mul_f32 v[60:61], v[60:61], v[84:85] op_sel_hi:[1,0]\n\tv_pk_mul_f32 v[62:63], v[62:63], v[84:85] op_sel_hi:[1,0]\n\t"
        "v_cvt_pk_bf16_f32 v88, v60, v61\n\tv_cvt_pk_bf16_f32 v89, v62, v63\n\t"
        "v_pk_mul_f32 v[86:87], v[86:87], v[84:85] op_sel_hi:[1,0]\n\tv_cvt_pk_bf16_f32 v90, v86, 0\n\t"
        "v_smfmac_f32_16x16x32_bf16 v[96:99], v[88:89], v[52:55], %[idx]\n\t"
        "v_cvt_pk_bf16_f32 v91, v87, 0\n\ts_waitcnt vmcnt(1)\n\ts_nop 0\n\t"
        "v_smfmac_f32_16x16x32_bf16 v[96:99], v[90:91], v[56:59], %[idy]\n\t"
        "s_nop 7\n\t"
        FOLD2("96", "")
        "ds_write2st64_b32 %[rw], v96, v97 offset1:1\n\t"
        "v_cmp_u_f32 vcc, v96, v97\n\ts_or_b64 vcc, vcc, s[56:57]\n\ts_cbranch_scc1 slows%=\n"
        "conts%=:\n\t" LGK(0) "s_barrier\n\t"
        "ds_read_b64 v[60:61], %[rr]\n\tds_read_b64 v[62:63], %[rr] offset:512\n\tds_read_b64 v[64:65], %[rr] offset:1024\n\tds_read_b64 v[66:67], %[rr] offset:1536\n\t"
        LGK(2) "v_pk_add_f32 v[60:61], v[60:61], v[62:63]\n\t"
        LGK(1) "v_pk_add_f32 v[60:61], v[60:61], v[64:65]\n\t" LGK(0) "v_pk_add_f32 v[60:61], v[60:61], v[66:67]\n\t"
        "v_cvt_pk_bf16_f32 v60, v60, v61\n\t"
        "buffer_store_dword v60, %[mo], %[ws], s70 offen sc1\n\t" DT(4)
        DOUT("80")
        "s_add_u32 s45, s45, 1\n\ts_mov_b32 s44, s49\n\ts_add_u32 s77, s77, 28*4096\n\t"
        "s_add_u32 s76, s76, 0x40000\n\ts_cmp_eq_u32 s76, 0x180000\n\ts_cselect_b32 s76, 0, s76\n\t"
        "s_bitset0_b32 s74, 31\n\t"
        "s_cmp_eq_u32 %[ob], 0\n\ts_cbranch_scc1 top%=\n\ts_branch out%=\n"
        "slowm%=:\n\ts_sub_u32 s47, s47, 1\n\ts_cbranch_scc1 deadm%=\n\t" DCNT
        "s_barrier\n\ts_branch recal%=\n"
        "deadm%=:\n\ts_mov_b32 s47, 0\n\ts_branch contm%=\n"
        "slows%=:\n\ts_bitcmp1_b32 s74, 31\n\ts_cbranch_scc1 conts%=\n"
        "rl%=:\n\ts_add_u32 s69, s69, " T_P "\n\ts_add_u32 s48, s69, 600\n"
        PW0("r") "s_cmp_lt_i32 s50, -600\n\ts_cbranch_scc1 rl%=\n\ts_cmp_lt_u32 s50, 8000\n\ts_cbranch_scc1 ra%=\n\t"
        "s_xor_b32 s50, s49, %[cm]\n\t"
        "s_xor_b32 s51, s50, %[c0]\n\tbuffer_store_dword v100, %[mo], %[ws], s51 offen sc1\n\t"
        "s_xor_b32 s51, s50, %[c1]\n\tbuffer_store_dword v100, %[mo], %[ws], s51 offen sc1\n\t"
        "s_xor_b32 s51, s50, %[c2]\n\tbuffer_store_dword v100, %[mo], %[ws], s51 offen sc1\n\t"
        "s_xor_b32 s51, s50, %[c3]\n\tbuffer_store_dword v100, %[mo], %[ws], s51 offen sc1\n\t"
        "s_add_u32 s48, s69, " STR(T_A) "-" STR(T_RE) "\n"
        PW0("q") "s_cmp_lt_u32 s50, 8000\n\ts_cbranch_scc1 qa%=\n\t"
        DCNT DCNT DCNT DCNT DCNT DCNT DCNT DCNT
        V_LOADS V_LOADY
        "s_waitcnt vmcnt(0)\n\t"
        "v_max_u32 v78, v56, v57\n\t.irp k,58,64,66,68,70,76\n\tv_max3_u32 v78, v78, v\\k, v[\\k+1]\n\t.endr\n\t"
        "v_cmp_eq_u32 vcc, -1, v78\n\ts_cbranch_vccz rok%=\n\t"
        "s_cmp_eq_u32 s74, 0\n\ts_cbranch_scc1 rok%=\n\ts_sub_u32 s74, s74, 1\n\ts_branch rl%=\n"
        "rok%=:\n\ts_bitset1_b32 s74, 31\n\t"
        ".irp k,96,97,98,99\n\tv_mov_b32 v\\k, 0\n\t.endr\n\t"
        "s_branch soft%=\n"
        "last%=:\n\t" V_HALF2("")
        "v_mov_b32 v79, 63\n\ts_waitcnt vmcnt(0)\n\tbuffer_store_dword v79, %[fm], %[ws], 0 offen sc1\n"
        "out%=:\n\ts_waitcnt vmcnt(0)"
        :
        : [ws] "s"(ws), [cm] "s"(cm), [cx] "s"(cm ^ pick(cl, wave)), [c0] "s"(cl.c[0]), [c1] "s"(cl.c[1]), [c2] "s"(cl.c[2]), [c3] "s"(cl.c[3]), [m0v] "s"(__builtin_amdgcn_readfirstlane(lb + wave * 2048)),
          [mbb] "s"(O_MB + (u32)(v * 4 + wave) * 1024u),
          [l16] "v"(l16), [la] "v"(la), [gl] "v"(gl), [gl2] "v"(wave < 3 ? gl : gl - 1024), [sof] "v"(sof), [sto] "v"(sto), [yo] "v"(yo), [rw] "v"(rw), [rr] "v"(rr),
          [fm] "v"(fm), [t0] "s"(t0), [ob] "s"(__builtin_amdgcn_readfirstlane(ob)), [mo] "v"(mo), [idx] "v"(idx), [idy] "v"(idxy), [mk] "v"(mk) DBGOP
        : CL_S, CL_V8_63, CL_V64_119, CL_A);
    }
}

#define XAS 3
#define XSLP 1
#define CL_SX "s45","s48","s50","s51","s52","s53","s54","s55","s56","s57","s58","s59","s60","s61","s62","s63","s64","s65","s66","s67","s68","s69","vcc","scc","memory"
#define XCAL
#define XDBG
#define MOVD(D, S, C, M) "v_mov_b32_dpp v" D ", v" S " " C " row_mask:0xf bank_mask:" M "\n\t"
#define MF(D, A, B, C) "v_mfma_f32_16x16x32_bf16 v[" D ":" D "+3], v[" A ":" A "+3], " B ", " C "\n\t"
DEV float max3(float a, float b, float c) { float r; asm("v_max3_f32 %0, %1, %2, %3" : "=v"(r) : "v"(a), "v"(b), "v"(c)); return r; }
#define HEAT "v_mov_b32 v30, 0\n\ts_nop 1\n\t"
#define SW32(A, B) "v_permlane32_swap_b32 v" A ", v" B "\n\t"
#define PKADD(A, B) "v_pk_add_f32 v[" A ":" A "+1], v[" A ":" A "+1], v[" B ":" B "+1]\n\t"
DEV void role_xp(const Params& p, LDS char* lds, int xc, int h, int par, int wave) {
    const u32 h0 = beat0(p);
    const int lane = fresh_lane();
    const int x = lane & 15, g = lane >> 4, i = lane & 7, sel = (lane >> 3) & 1;
    rsrc_t ws = mkrs(p.ws, WS_END);
    rsrc_t rx = mkrs(p.Bx, 2048u * 896u);
    int budget = 400000;
    constexpr int L_XS = 0;
    const u32 lb = (u32)(uintptr_t)lds;
    const int idx = (lane & 8) ? 0xEEEE : 0x4444;
    const bool full = wave < 3;
    const u32 l16 = lane * 16;
    const u32 m0 = wave * GRP + l16, m1 = full ? m0 + 1024 : OOB;
    const u32 uo = wave * 4096 + l16, uo2 = full ? uo + 2048 : OOB;
    const u32 wr = lb + L_XS + wave * 1280 + g * 160 + (x >> 1) * 16 + (x & 1) * 4;
    const u32 rd = lb + L_XS + (i & 1) * 640 + (((i >> 2) & 1) + 2 * ((i >> 1) & 1)) * 160 + (2 * g + sel) * 16;
    const u32 y0 = g * GRP + h * 256 + x * 16;
    const u32 y1 = g < 3 ? y0 + 4 * GRP : OOB;
    {
        const int sg = (h * 2 + par) * 2 + (wave >> 1);
        u4 t[14];
#pragma unroll
        for (int m = 0; m < 14; ++m) t[m] = ld4(rx, (u32)x * 896u + g * 16 + m * 64, (u32)sg * 28672u + (wave & 1) * 14336);
#pragma unroll
        for (int m = 0; m < 14; ++m) st4c(t[m], ws, l16 + (m & 3) * 1024, O_XP + (u32)sg * 28672u + (wave & 1) * 14336 + (m >> 2) * 4096);
        VMCNT(0);
        bar();
    }
    wait_beat(ws, lane, h0, budget);
    if (wave == 0 && lane == 0) st1c(0u, ws, F_X + (h * 2 + par) * 128, 0);
    STAMP(2, 0, 6);
    {
        rsrc_t rw = mkrs(p.Awqkv, 448u * 1344u * 2u);
        rsrc_t ra = mkrs(p.Ax, 2048u * 896u);
        const u32 tl = (u32)((h * 2 + par) * 4 + wave) * 14336u;
        u4 v[14], st[11];
#pragma unroll
        for (int m = 0; m < 14; ++m) v[m] = ld4(ra, (u32)x * 896u + g * 16 + m * 64, tl);
        const int ir = lane >> 1, kl = 8 * ((ir >> 2) & 3) + 4 * (ir >> 4) + (ir & 3);
        const u32 tro = (u32)((4 * g + (x >> 2)) * 32 + (x & 3) * 8);
        LDS char* lw = lds + wave * 11 * 1024;
        const int f0 = par * 42 + (wave * 21 >> 1);
#pragma unroll
        for (int j = 0; j < 11; ++j) {
            const int f = f0 + j, m = f / 6, c = f % 6;
            st[j] = ld4(rw, (u32)(kl * 1344 + ((c & 1) ? 16 + (lane & 1) * 4 : (lane & 1) * 8)) * 2u, (u32)(32 * m * 1344 + (c >> 1) * 448 + h * 28) * 2u);
            if ((c & 1) && (lane & 1)) st[j] = (u4){st[j].z, st[j].w, 0u, 0u};
        }
#pragma unroll
        for (int m = 0; m < 14; ++m) st4c(v[m], ws, l16 + (m & 3) * 1024, O_AX + tl + (m & ~3) * 1024);
#pragma unroll
        for (int j = 0; j < 11; ++j) *(LDS u4*)(lw + j * 1024 + l16) = st[j];
        MEMBAR();
#pragma unroll
        for (int j = 0; j < 11; ++j)
            st4c(ldtr2(lw, j * 1024 + tro, j * 1024 + tro + 512), ws, l16 + (j & 3) * 1024, O_AW + (u32)(h * 84 + f0 + (j & ~3)) * 1024u);
        VMCNT(0);
        bar();
        if (wave == 0 && lane == 0) st1c(0u, ws, F_K + xc * 1024 + ((h & 3) * 2 + par) * 4, 0);
    }
    STAMP(2, 0, 5);
    u32 cm, cw, cg, fb;
    { const Cls cl = get_cls(ws, lane, budget); cm = pick(cl, xc); cw = pick(cl, wave) ^ 0x1000u; cg = pick(cl, g) ^ 0x1000u; fb = cl.fb; }
    const u32 sto = wave == 3 ? cg + 7 * GRP + h * 128 + x * 8 : OOB;
    const u32 cn = F_S + (fb ^ FSX) + h * 128 + (lane & 3) * 4;
    u32 tgt = 0;
    asm volatile("" :: "v"(ld1c(ws, lane < 2 ? (lane ? O_VO : O_U) + (u32)(49 + h) * 28672u - 128u : OOB, 0)));
    {
        const u4 ones = {~0u, ~0u, ~0u, ~0u};
        const u32 k0 = par ? kgrp(8) : 0u;
        st4c(ones, ws, y0, (cw ^ k0) & ~0x800u); st4c(ones, ws, y1, (cw ^ k0) & ~0x800u); st2c((u2){~0u, ~0u}, ws, sto ^ k0, 0);
    }
    int xb = 20000, fw = -1;
    const u32 fo = F_W - 8192 + h * 128 + (lane & 31) * 4;
    const int fq = x >> 2;
    u4 U[2][4] = {}, V[7] = {};
    int yt = par;
    STAMP(2, 0, 0);
    for (int s = par - 2; s < NSEG - 1; s += 2) {
        {
            const u32 ob = s < 0 ? OOB : 0u;
            const u32 ky = kgrp(8 * yt), kr = kgrp(yt < 4 ? 8 * yt + 16 : 8 * yt - 32);
            { const u32 now = gnow(); if (s <= 0) tgt = now; while ((int)(tgt - now) < -300) tgt += T_A + T_B; }
            const int xb0 = xb;
            asm volatile("" :: "v"(fw));
            asm volatile(
                "s_nop 4\n\ts_mov_b32 s69, %[tg]\n\t" XCAL DINIT SLED_INIT("66", "67", "we")
                ".irp k,48,49,50,51,52,53,54,55\n\tv_mov_b32 v\\k, 0\n\t.endr\n\t.irp k,56,57,58,59\n\tv_mov_b32 v\\k, -1\n\t.endr\n"
                "retry%=:\n\ts_sub_u32 s48, s69, " T_P "-" STR(T_XG) "\n"
                PW0("g") "s_cmp_lt_u32 s50, 8000\n\ts_cbranch_scc0 gb%=\n\ts_sleep 1\n\ts_branch ga%=\n"
                "gb%=:\n\tbuffer_load_dword v31, %[cn], %[ws], 0 offen sc1\n\t"
                ".irp k,16,17,18,19,20,21,22,23\n\tv_mov_b32 v\\k, 0\n\t.endr\n\t"
                "s_sub_u32 s48, s69, 200\n\t" DTGT("s69")
                PW("w", "-232") SLED("66", "67", "we")
                "s_waitcnt vmcnt(0)\n\tv_cmp_gt_i32 vcc, %[sp], v31\n\ts_cbranch_vccnz skip%=\n"
                "go%=:\n\t" DT(0)
                "buffer_load_dwordx4 v[8:11], %[m0], %[ws], %[mm] offen sc1\n\t"
                "buffer_load_dwordx4 v[12:15], %[m1], %[ws], %[mm] offen sc1\n\t"
                "s_sleep " STR(XAS) "\n\t"
                "buffer_store_dwordx4 v[56:59], %[y0], %[ws], %[yr] offen sc1\n\t"
                "buffer_store_dwordx4 v[56:59], %[y1], %[ws], %[yr] offen sc1\n\t"
                "buffer_store_dwordx2 v[56:57], %[str], %[ws], 0 offen sc1\n\t"
                "s_waitcnt vmcnt(4)\n\t" SMF64("16", "8", "%[u00]") SMF64("20", "8", "%[u10]")
                "s_waitcnt vmcnt(3)\n\t" DT(1) SMF64("16", "12", "%[u01]") SMF64("20", "12", "%[u11]")
                "s_nop 3\n\t"
                FOLD2("16", "")
                "ds_write2_b32 %[wr], v16, v17 offset1:160\n\tv_cmp_u_f32 vcc, v16, v17\n\t"
                FOLD2("20", "")
                "ds_write2_b32 %[wr], v20, v21 offset0:2 offset1:162\n\t"
                "s_cbranch_vccnz slow%=\n"
                "cont%=:\n\t" HEAT HEAT HEAT "v_mov_b32 v30, 0\n\t" LGK(0) "v_mov_b32 v30, 0\n\ts_barrier\n\tv_mov_b32 v30, 0\n\t"
                "ds_read_b128 v[8:11], %[rd]\n\tds_read_b128 v[12:15], %[rd] offset:1280\n\tv_mov_b32 v30, 0\n\t"
                "ds_read_b128 v[16:19], %[rd] offset:2560\n\tds_read_b128 v[20:23], %[rd] offset:3840\n\t"
                HEAT HEAT HEAT "v_mov_b32 v30, 0\n\t"
                LGK(2) PKADD("8", "12") PKADD("10", "14") LGK(1) PKADD("8", "16") PKADD("10", "18") LGK(0) PKADD("8", "20") PKADD("10", "22")
                "v_max_f32 v24, v8, v9\n\tv_max3_f32 v24, v24, v10, v11\n\ts_nop 1\n\t"
                "v_max_f32_dpp v25, v24, v24 row_ror:8 row_mask:0xf bank_mask:0xf\n\t"
                "v_max_f32_dpp v24, v24, v24 row_ror:8 row_mask:0xf bank_mask:0xf\n\ts_nop 1\n\t"
                "v_permlane16_swap_b32 v24, v25\n\tv_max_f32 v28, v24, v25\n\tv_max_f32 v24, v24, v25\n\ts_nop 1\n\t"
                "v_permlane32_swap_b32 v24, v28\n\tv_max_f32 v24, v24, v28\n\t"
                "v_pk_add_f32 v[8:9], v[8:9], v[24:25] op_sel_hi:[1,0] neg_lo:[0,1] neg_hi:[0,1]\n\t"
                "v_pk_add_f32 v[10:11], v[10:11], v[24:25] op_sel_hi:[1,0] neg_lo:[0,1] neg_hi:[0,1]\n\t"
                "v_exp_f32 v8, v8\n\tv_exp_f32 v9, v9\n\tv_exp_f32 v10, v10\n\tv_exp_f32 v11, v11\n\t"
                "v_cvt_pk_bf16_f32 v26, v8, v9\n\tv_cvt_pk_bf16_f32 v27, v10, v11\n\t"
                PKADD("8", "10")
                MOVD("48", "26", "quad_perm:[0,1,2,3]", "0x3") MOVD("49", "26", "row_ror:8", "0x3")
                MOVD("50", "27", "quad_perm:[0,1,2,3]", "0x3") MOVD("51", "27", "row_ror:8", "0x3")
                "v_add_f32 v25, v8, v9\n\t" MOVD("52", "26", "row_ror:8", "0xc")
                MF("32", "48", "%[v0]", "0")
                MOVD("53", "26", "quad_perm:[0,1,2,3]", "0xc") MOVD("54", "27", "row_ror:8", "0xc") MOVD("55", "27", "quad_perm:[0,1,2,3]", "0xc")
                MF("36", "48", "%[v1]", "0")
                "v_add_f32_dpp v28, v25, v25 row_ror:8 row_mask:0xf bank_mask:0xf\n\t"
                "v_add_f32_dpp v25, v25, v25 row_ror:8 row_mask:0xf bank_mask:0xf\n\ts_nop 0\n\t"
                MF("32", "52", "%[v2]", "v[32:35]")
                "v_permlane16_swap_b32 v25, v28\n\tv_add_f32 v29, v25, v28\n\t"
                MF("36", "52", "%[v3]", "v[36:39]")
                "v_add_f32 v25, v25, v28\n\ts_waitcnt vmcnt(0)\n\t"
                MF("40", "48", "%[v4]", "0")
                "v_permlane32_swap_b32 v25, v29\n\tv_add_f32 v25, v25, v29\n\t"
                MF("44", "48", "%[v5]", "0")
                "buffer_store_dwordx2 v[24:25], %[sto], %[ws], 0 offen sc1\n\t" DT(2)
                "v_cvt_pk_bf16_f32 v8, v32, v33\n\tv_cvt_pk_bf16_f32 v10, v34, v35\n\t"
                MF("40", "52", "%[v6]", "v[40:43]")
                "v_cvt_pk_bf16_f32 v9, v36, v37\n\tv_cvt_pk_bf16_f32 v11, v38, v39\n\t"
                "s_nop 0\n\t"
                "v_permlane16_swap_b32 v8, v9\n\tv_permlane16_swap_b32 v10, v11\n\t"
                "buffer_store_dwordx4 v[8:11], %[y0], %[ws], %[ys] offen sc1\n\t"
                "v_cvt_pk_bf16_f32 v13, v44, v45\n\tv_cvt_pk_bf16_f32 v15, v46, v47\n\t"
                "v_cvt_pk_bf16_f32 v12, v40, v41\n\tv_cvt_pk_bf16_f32 v14, v42, v43\n\t"
                "s_nop 0\n\t"
                "v_permlane16_swap_b32 v12, v13\n\tv_permlane16_swap_b32 v14, v15\n\t"
                "buffer_store_dwordx4 v[12:15], %[y1], %[ws], %[ys] offen sc1\n\t" DT(3)
                DOUT("32")
                "s_branch done%=\n"
                "slow%=:\n\tv_max3_u32 v24, v8, v9, v10\n\tv_max3_u32 v24, v24, v11, v12\n\tv_max3_u32 v24, v24, v13, v14\n\tv_max_u32 v24, v24, v15\n\t"
                "v_cmp_eq_u32 vcc, -1, v24\n\ts_cbranch_vccz cont%=\n\ts_cmp_eq_u32 %[xb], 0\n\ts_cbranch_scc1 cont%=\n\t" DCNT
                "nxt%=:\n\ts_sub_u32 %[xb], %[xb], 1\n\ts_add_u32 s69, s69, " T_P "\n\t" LGK(0) "s_branch retry%=\n"
                "skip%=:\n\ts_cmp_eq_u32 %[xb], 0\n\ts_cbranch_scc0 nxt%=\n\ts_branch go%=\n"
                "done%=:\n\ts_nop 1"
                : [xb] "+s"(xb)
                : [ws] "s"(ws), [mm] "s"(s < 0 ? cm : cm ^ kgrp(4 * (s & 7))), [tg] "s"(tgt), [sp] "s"(s - 1), [cn] "v"(s > 0 ? cn : OOB), [ys] "s"((cw ^ ky) & ~0x800u), [yr] "s"((cw ^ kr) & ~0x800u),
                  [u00] "v"(cat(U[0][0], U[0][1])), [u01] "v"(cat(U[0][2], U[0][3])), [u10] "v"(cat(U[1][0], U[1][1])), [u11] "v"(cat(U[1][2], U[1][3])),
                  [v0] "v"(V[0]), [v1] "v"(V[1]), [v2] "v"(V[2]), [v3] "v"(V[3]), [v4] "v"(V[4]), [v5] "v"(V[5]), [v6] "v"(V[6]),
                  [m0] "v"(m0), [m1] "v"(m1), [wr] "v"(wr), [rd] "v"(rd), [sto] "v"((sto ^ ky) | ob), [str] "v"((sto ^ kr) | ob), [y0] "v"(y0 | ob), [y1] "v"(y1 | ob), [idx] "v"(idx) XDBG
                : CL_SX, CL_V8_63);
            tgt += (u32)(2 + xb0 - xb) * (T_A + T_B);
            if (s >= 2 && wave == 2 && lane == 0) st1c((u32)(s - 2), ws, F_Q + fb + (h * 2 + par) * 128, 0);
            if (s >= 0) yt = yt < 4 ? yt + 2 : yt - 4;
        }
        const int sn = s + 2;
        if (sn < NSEG - 1) {
            while (__builtin_amdgcn_ballot_w64(fq == (sn & 3) && fw < 0) != 0ull && --budget >= 0) { MEMBAR(); fw = (int)ld1c(ws, fo, (sn < 4 ? sn : 4 + (sn >> 2)) * 2048); MEMBAR(); __builtin_amdgcn_s_sleep(XSLP); }
            if (sn == 0 && wave == 0) st1c((gnow() + T_ST) & ~1u, ws, lane == 0 ? F_L + (fb ^ 0x2000u) + h * 128 : OOB, 0);
            if (s < 0) STAMP(2, 0, 4);
            const u32 sb = (u32)(sn * 16 + h) * 28672u;
#pragma unroll
            for (int t = 0; t < 2; ++t)
#pragma unroll
                for (int k = 0; k < 4; ++k) U[t][k] = ld4c(ws, (k < 2 ? uo : uo2) + (k & 1) * 1024, O_U + sb + t * 14336);
#pragma unroll
            for (int k = 0; k < 7; ++k) V[k] = ld4c(ws, l16 + (k & 3) * 1024, O_VO + sb + wave * 7168 + (k >> 2) * 4096);
            if ((sn & 2) || sn < 2 || __builtin_amdgcn_ballot_w64(fq == (sn & 3) + 2 && fw < 0) != 0ull) { MEMBAR(); fw = (int)ld1c(ws, fo, (sn < 2 ? sn + 2 : 4 + ((sn + 2) >> 2)) * 2048); MEMBAR(); }
            if (s == -par) {
                tgt = grid0<4>(ws, fb, lane, budget) + sn * (T_A + T_B);
                while ((int)(tgt - gnow()) > 6000 && --budget >= 0) __builtin_amdgcn_s_sleep(4);
            }
        }
    }
}

#define GEMM_ASM0 \
    "s_nop 4\n\ts_mov_b32 s44, %[xb]\n\ts_mov_b32 s46, %[ob]\n\ts_add_u32 s47, s46, 4096\n\ts_mov_b32 s45, 15\n\tbuffer_load_dwordx4 v[8:11], %[x0], %[ws], s44 offen sc1\n\t" \
    "buffer_load_dwordx4 v[12:15], %[x1], %[ws], s44 offen sc1\n\ts_add_u32 s44, s44, 1024\n\tbuffer_load_dwordx4 v[16:19], %[x0], %[ws], s44 offen sc1\n\t" \
    "buffer_load_dwordx4 v[20:23], %[x1], %[ws], s44 offen sc1\n\ts_add_u32 s44, s44, 1024\n\tbuffer_load_dwordx4 v[24:27], %[x0], %[ws], s44 offen sc1\n\t" \
    "buffer_load_dwordx4 v[28:31], %[x1], %[ws], s44 offen sc1\n\ts_add_u32 s44, s44, 1024\n\tbuffer_load_dwordx4 v[32:35], %[x0], %[ws], s44 offen sc1\n\t" \
    "buffer_load_dwordx4 v[36:39], %[x1], %[ws], s44 offen sc1\n\ts_add_u32 s44, s44, 1024\n\tbuffer_load_dwordx4 v[40:43], %[x0], %[ws], s44 offen sc1\n\t" \
    "buffer_load_dwordx4 v[44:47], %[x1], %[ws], s44 offen sc1\n\ts_add_u32 s44, s44, 1024\n\tbuffer_load_dwordx4 v[48:51], %[x0], %[ws], s44 offen sc1\n\t" \
    "buffer_load_dwordx4 v[52:55], %[x1], %[ws], s44 offen sc1\n\ts_add_u32 s44, s44, 1024\n\tbuffer_load_dwordx4 v[56:59], %[x0], %[ws], s44 offen sc1\n\t" \
    "buffer_load_dwordx4 v[60:63], %[x1], %[ws], s44 offen sc1\n\ts_add_u32 s44, s44, 1024\n\tds_read_b128 a[0:3], %[wa] offset:0\n\tds_read_b128 a[4:7], %[wa] offset:1024\n\t" \
    "ds_read_b128 a[8:11], %[wa] offset:2048\n\tds_read_b128 a[12:15], %[wa] offset:3072\n\tds_read_b128 a[16:19], %[wb] offset:0\n\tds_read_b128 a[20:23], %[wb] offset:1024\n\t" \
    "ds_read_b128 a[24:27], %[wb] offset:2048\n\ts_waitcnt vmcnt(12)\n\tds_read_b128 a[28:31], %[wa] offset:4096\n\tds_read_b128 a[32:35], %[wa] offset:5120\n\t" \
    "ds_read_b128 a[36:39], %[wa] offset:6144\n\tds_read_b128 a[40:43], %[wa] offset:7168\n\tds_read_b128 a[44:47], %[wb] offset:4096\n\tds_read_b128 a[48:51], %[wb] offset:5120\n\t" \
    "ds_read_b128 a[52:55], %[wb] offset:6144\n\ts_waitcnt lgkmcnt(7)\n\tv_mfma_f32_16x16x32_bf16 v[64:67], a[0:3], v[8:11], 0\n\t" \
    "v_mfma_f32_16x16x32_bf16 v[68:71], a[0:3], v[12:15], 0\n\tv_mfma_f32_16x16x32_bf16 v[72:75], a[4:7], v[8:11], 0\n\tv_mfma_f32_16x16x32_bf16 v[76:79], a[4:7], v[12:15], 0\n\t" \
    "v_mfma_f32_16x16x32_bf16 v[80:83], a[8:11], v[8:11], 0\n\tv_mfma_f32_16x16x32_bf16 v[84:87], a[8:11], v[12:15], 0\n\tv_mfma_f32_16x16x32_bf16 v[88:91], a[12:15], v[8:11], 0\n\t" \
    "v_mfma_f32_16x16x32_bf16 v[92:95], a[12:15], v[12:15], 0\n\tv_mfma_f32_16x16x32_bf16 v[96:99], a[16:19], v[8:11], 0\n\t" \
    "v_mfma_f32_16x16x32_bf16 v[100:103], a[16:19], v[12:15], 0\n\tv_mfma_f32_16x16x32_bf16 v[104:107], a[20:23], v[8:11], 0\n\t" \
    "v_mfma_f32_16x16x32_bf16 v[108:111], a[20:23], v[12:15], 0\n\tv_mfma_f32_16x16x32_bf16 v[112:115], a[24:27], v[8:11], 0\n\t" \
    "v_mfma_f32_16x16x32_bf16 v[116:119], a[24:27], v[12:15], 0\n\tbuffer_load_dwordx4 v[8:11], %[x0], %[ws], s44 offen sc1\n\t" \
    "buffer_load_dwordx4 v[12:15], %[x1], %[ws], s44 offen sc1\n\ts_add_u32 s44, s44, 1024\n\ts_waitcnt vmcnt(12)\n\tds_read_b128 a[56:59], %[wa] offset:8192\n\t" \
    "ds_read_b128 a[60:63], %[wa] offset:9216\n\tds_read_b128 a[64:67], %[wa] offset:10240\n\tds_read_b128 a[68:71], %[wa] offset:11264\n\tds_read_b128 a[72:75], %[wb] offset:8192\n\t" \
    "ds_read_b128 a[76:79], %[wb] offset:9216\n\tds_read_b128 a[80:83], %[wb] offset:10240\n\ts_waitcnt lgkmcnt(7)\n\tv_mfma_f32_16x16x32_bf16 v[64:67], a[28:31], v[16:19], v[64:67]\n\t" \
    "v_mfma_f32_16x16x32_bf16 v[68:71], a[28:31], v[20:23], v[68:71]\n\tv_mfma_f32_16x16x32_bf16 v[72:75], a[32:35], v[16:19], v[72:75]\n\t" \
    "v_mfma_f32_16x16x32_bf16 v[76:79], a[32:35], v[20:23], v[76:79]\n\tv_mfma_f32_16x16x32_bf16 v[80:83], a[36:39], v[16:19], v[80:83]\n\t" \
    "v_mfma_f32_16x16x32_bf16 v[84:87], a[36:39], v[20:23], v[84:87]\n\tv_mfma_f32_16x16x32_bf16 v[88:91], a[40:43], v[16:19], v[88:91]\n\t" \
    "v_mfma_f32_16x16x32_bf16 v[92:95], a[40:43], v[20:23], v[92:95]\n\tv_mfma_f32_16x16x32_bf16 v[96:99], a[44:47], v[16:19], v[96:99]\n\t" \
    "v_mfma_f32_16x16x32_bf16 v[100:103], a[44:47], v[20:23], v[100:103]\n\tv_mfma_f32_16x16x32_bf16 v[104:107], a[48:51], v[16:19], v[104:107]\n\t" \
    "v_mfma_f32_16x16x32_bf16 v[108:111], a[48:51], v[20:23], v[108:111]\n\tv_mfma_f32_16x16x32_bf16 v[112:115], a[52:55], v[16:19], v[112:115]\n\t" \
    "v_mfma_f32_16x16x32_bf16 v[116:119], a[52:55], v[20:23], v[116:119]\n\tbuffer_load_dwordx4 v[16:19], %[x0], %[ws], s44 offen sc1\n\t" \
    "buffer_load_dwordx4 v[20:23], %[x1], %[ws], s44 offen sc1\n\ts_add_u32 s44, s44, 1024\n\ts_waitcnt vmcnt(12)\n\tds_read_b128 a[84:87], %[wa] offset:12288\n\t" \
    "ds_read_b128 a[88:91], %[wa] offset:13312\n\tds_read_b128 a[92:95], %[wa] offset:14336\n\tds_read_b128 a[96:99], %[wa] offset:15360\n\t" \
    "ds_read_b128 a[100:103], %[wb] offset:12288\n\tds_read_b128 a[104:107], %[wb] offset:13312\n\tds_read_b128 a[108:111], %[wb] offset:14336\n\ts_waitcnt lgkmcnt(7)\n\t" \
    "v_mfma_f32_16x16x32_bf16 v[64:67], a[56:59], v[24:27], v[64:67]\n\tv_mfma_f32_16x16x32_bf16 v[68:71], a[56:59], v[28:31], v[68:71]\n\t" \
    "v_mfma_f32_16x16x32_bf16 v[72:75], a[60:63], v[24:27], v[72:75]\n\tv_mfma_f32_16x16x32_bf16 v[76:79], a[60:63], v[28:31], v[76:79]\n\t" \
    "v_mfma_f32_16x16x32_bf16 v[80:83], a[64:67], v[24:27], v[80:83]\n\tv_mfma_f32_16x16x32_bf16 v[84:87], a[64:67], v[28:31], v[84:87]\n\t" \
    "v_mfma_f32_16x16x32_bf16 v[88:91], a[68:71], v[24:27], v[88:91]\n\tv_mfma_f32_16x16x32_bf16 v[92:95], a[68:71], v[28:31], v[92:95]\n\t" \
    "v_mfma_f32_16x16x32_bf16 v[96:99], a[72:75], v[24:27], v[96:99]\n\tv_mfma_f32_16x16x32_bf16 v[100:103], a[72:75], v[28:31], v[100:103]\n\t" \
    "v_mfma_f32_16x16x32_bf16 v[104:107], a[76:79], v[24:27], v[104:107]\n\tv_mfma_f32_16x16x32_bf16 v[108:111], a[76:79], v[28:31], v[108:111]\n\t" \
    "v_mfma_f32_16x16x32_bf16 v[112:115], a[80:83], v[24:27], v[112:115]\n\tv_mfma_f32_16x16x32_bf16 v[116:119], a[80:83], v[28:31], v[116:119]\n\t" \
    "buffer_load_dwordx4 v[24:27], %[x0], %[ws], s44 offen sc1\n\tbuffer_load_dwordx4 v[28:31], %[x1], %[ws], s44 offen sc1\n\ts_add_u32 s44, s44, 1024\n\ts_waitcnt vmcnt(12)\n\t" \
    "ds_read_b128 a[112:115], %[wa] offset:16384\n\tds_read_b128 a[116:119], %[wa] offset:17408\n\tds_read_b128 a[120:123], %[wa] offset:18432\n\t" \
    "ds_read_b128 a[124:127], %[wa] offset:19456\n\tds_read_b128 a[128:131], %[wb] offset:16384\n\tds_read_b128 a[132:135], %[wb] offset:17408\n\t" \
    "ds_read_b128 v[120:123], %[wb] offset:18432\n\ts_waitcnt lgkmcnt(7)\n\tv_mfma_f32_16x16x32_bf16 v[64:67], a[84:87], v[32:35], v[64:67]\n\t" \
    "v_mfma_f32_16x16x32_bf16 v[68:71], a[84:87], v[36:39], v[68:71]\n\tv_mfma_f32_16x16x32_bf16 v[72:75], a[88:91], v[32:35], v[72:75]\n\t" \
    "v_mfma_f32_16x16x32_bf16 v[76:79], a[88:91], v[36:39], v[76:79]\n\tv_mfma_f32_16x16x32_bf16 v[80:83], a[92:95], v[32:35], v[80:83]\n\t" \
    "v_mfma_f32_16x16x32_bf16 v[84:87], a[92:95], v[36:39], v[84:87]\n\tv_mfma_f32_16x16x32_bf16 v[88:91], a[96:99], v[32:35], v[88:91]\n\t" \
    "v_mfma_f32_16x16x32_bf16 v[92:95], a[96:99], v[36:39], v[92:95]\n\tv_mfma_f32_16x16x32_bf16 v[96:99], a[100:103], v[32:35], v[96:99]\n\t" \
    "v_mfma_f32_16x16x32_bf16 v[100:103], a[100:103], v[36:39], v[100:103]\n\tv_mfma_f32_16x16x32_bf16 v[104:107], a[104:107], v[32:35], v[104:107]\n\t" \
    "v_mfma_f32_16x16x32_bf16 v[108:111], a[104:107], v[36:39], v[108:111]\n\tv_mfma_f32_16x16x32_bf16 v[112:115], a[108:111], v[32:35], v[112:115]\n\t" \
    "v_mfma_f32_16x16x32_bf16 v[116:119], a[108:111], v[36:39], v[116:119]\n\tbuffer_load_dwordx4 v[32:35], %[x0], %[ws], s44 offen sc1\n\t" \
    "buffer_load_dwordx4 v[36:39], %[x1], %[ws], s44 offen sc1\n\ts_add_u32 s44, s44, 1024\n\ts_waitcnt vmcnt(12)\n\tds_read_b128 a[136:139], %[wa] offset:20480\n\t" \
    "ds_read_b128 a[140:143], %[wa] offset:21504\n\tds_read_b128 a[144:147], %[wa] offset:22528\n\tds_read_b128 a[148:151], %[wa] offset:23552\n\t" \
    "ds_read_b128 a[152:155], %[wb] offset:20480\n\tds_read_b128 a[156:159], %[wb] offset:21504\n\tds_read_b128 a[160:163], %[wb] offset:22528\n\ts_waitcnt lgkmcnt(7)\n\t" \
    "v_mfma_f32_16x16x32_bf16 v[64:67], a[112:115], v[40:43], v[64:67]\n\tv_mfma_f32_16x16x32_bf16 v[68:71], a[112:115], v[44:47], v[68:71]\n\t" \
    "v_mfma_f32_16x16x32_bf16 v[72:75], a[116:119], v[40:43], v[72:75]\n\tv_mfma_f32_16x16x32_bf16 v[76:79], a[116:119], v[44:47], v[76:79]\n\t" \
    "v_mfma_f32_16x16x32_bf16 v[80:83], a[120:123], v[40:43], v[80:83]\n\tv_mfma_f32_16x16x32_bf16 v[84:87], a[120:123], v[44:47], v[84:87]\n\t" \
    "v_mfma_f32_16x16x32_bf16 v[88:91], a[124:127], v[40:43], v[88:91]\n\tv_mfma_f32_16x16x32_bf16 v[92:95], a[124:127], v[44:47], v[92:95]\n\t" \
    "v_mfma_f32_16x16x32_bf16 v[96:99], a[128:131], v[40:43], v[96:99]\n\tv_mfma_f32_16x16x32_bf16 v[100:103], a[128:131], v[44:47], v[100:103]\n\t" \
    "v_mfma_f32_16x16x32_bf16 v[104:107], a[132:135], v[40:43], v[104:107]\n\tv_mfma_f32_16x16x32_bf16 v[108:111], a[132:135], v[44:47], v[108:111]\n\t" \
    "v_mfma_f32_16x16x32_bf16 v[112:115], v[120:123], v[40:43], v[112:115]\n\tv_mfma_f32_16x16x32_bf16 v[116:119], v[120:123], v[44:47], v[116:119]\n\t" \
    "buffer_load_dwordx4 v[40:43], %[x0], %[ws], s44 offen sc1\n\tbuffer_load_dwordx4 v[44:47], %[x1], %[ws], s44 offen sc1\n\ts_add_u32 s44, s44, 1024\n\ts_waitcnt vmcnt(12)\n\t" \
    "ds_read_b128 a[164:167], %[wa] offset:24576\n\tds_read_b128 a[168:171], %[wa] offset:25600\n\tds_read_b128 a[172:175], %[wa] offset:26624\n\t" \
    "ds_read_b128 a[176:179], %[wa] offset:27648\n\tds_read_b128 a[180:183], %[wb] offset:24576\n\tds_read_b128 a[184:187], %[wb] offset:25600\n\t" \
    "ds_read_b128 v[124:127], %[wb] offset:26624\n\ts_waitcnt lgkmcnt(7)\n\tv_mfma_f32_16x16x32_bf16 v[64:67], a[136:139], v[48:51], v[64:67]\n\t" \
    "v_mfma_f32_16x16x32_bf16 v[68:71], a[136:139], v[52:55], v[68:71]\n\tv_mfma_f32_16x16x32_bf16 v[72:75], a[140:143], v[48:51], v[72:75]\n\t" \
    "v_mfma_f32_16x16x32_bf16 v[76:79], a[140:143], v[52:55], v[76:79]\n\tv_mfma_f32_16x16x32_bf16 v[80:83], a[144:147], v[48:51], v[80:83]\n\t" \
    "v_mfma_f32_16x16x32_bf16 v[84:87], a[144:147], v[52:55], v[84:87]\n\tv_mfma_f32_16x16x32_bf16 v[88:91], a[148:151], v[48:51], v[88:91]\n\t" \
    "v_mfma_f32_16x16x32_bf16 v[92:95], a[148:151], v[52:55], v[92:95]\n\tv_mfma_f32_16x16x32_bf16 v[96:99], a[152:155], v[48:51], v[96:99]\n\t" \
    "v_mfma_f32_16x16x32_bf16 v[100:103], a[152:155], v[52:55], v[100:103]\n\tv_mfma_f32_16x16x32_bf16 v[104:107], a[156:159], v[48:51], v[104:107]\n\t" \
    "v_mfma_f32_16x16x32_bf16 v[108:111], a[156:159], v[52:55], v[108:111]\n\tv_mfma_f32_16x16x32_bf16 v[112:115], a[160:163], v[48:51], v[112:115]\n\t" \
    "v_mfma_f32_16x16x32_bf16 v[116:119], a[160:163], v[52:55], v[116:119]\n\tbuffer_load_dwordx4 v[48:51], %[x0], %[ws], s44 offen sc1\n\t" \
    "buffer_load_dwordx4 v[52:55], %[x1], %[ws], s44 offen sc1\n\ts_add_u32 s44, s44, 1024\n\ts_waitcnt vmcnt(12)\n\tds_read_b128 a[188:191], %[wa] offset:28672\n\t" \
    "ds_read_b128 a[192:195], %[wa] offset:29696\n\tds_read_b128 a[196:199], %[wa] offset:30720\n\tds_read_b128 a[200:203], %[wa] offset:31744\n\t" \
    "ds_read_b128 a[204:207], %[wb] offset:28672\n\tds_read_b128 a[208:211], %[wb] offset:29696\n\tds_read_b128 a[212:215], %[wb] offset:30720\n\ts_waitcnt lgkmcnt(7)\n\t" \
    "v_mfma_f32_16x16x32_bf16 v[64:67], a[164:167], v[56:59], v[64:67]\n\tv_mfma_f32_16x16x32_bf16 v[68:71], a[164:167], v[60:63], v[68:71]\n\t" \
    "v_mfma_f32_16x16x32_bf16 v[72:75], a[168:171], v[56:59], v[72:75]\n\tv_mfma_f32_16x16x32_bf16 v[76:79], a[168:171], v[60:63], v[76:79]\n\t" \
    "v_mfma_f32_16x16x32_bf16 v[80:83], a[172:175], v[56:59], v[80:83]\n\tv_mfma_f32_16x16x32_bf16 v[84:87], a[172:175], v[60:63], v[84:87]\n\t" \
    "v_mfma_f32_16x16x32_bf16 v[88:91], a[176:179], v[56:59], v[88:91]\n\tv_mfma_f32_16x16x32_bf16 v[92:95], a[176:179], v[60:63], v[92:95]\n\t" \
    "v_mfma_f32_16x16x32_bf16 v[96:99], a[180:183], v[56:59], v[96:99]\n\tv_mfma_f32_16x16x32_bf16 v[100:103], a[180:183], v[60:63], v[100:103]\n\t" \
    "v_mfma_f32_16x16x32_bf16 v[104:107], a[184:187], v[56:59], v[104:107]\n\tv_mfma_f32_16x16x32_bf16 v[108:111], a[184:187], v[60:63], v[108:111]\n\t" \
    "v_mfma_f32_16x16x32_bf16 v[112:115], v[124:127], v[56:59], v[112:115]\n\tv_mfma_f32_16x16x32_bf16 v[116:119], v[124:127], v[60:63], v[116:119]\n\t" \
    "buffer_load_dwordx4 v[56:59], %[x0], %[ws], s44 offen sc1\n\tbuffer_load_dwordx4 v[60:63], %[x1], %[ws], s44 offen sc1\n\ts_add_u32 s44, s44, 101376\n\ts_waitcnt vmcnt(12)\n\t" \
    "ds_read_b128 a[216:219], %[wa] offset:32768\n\tds_read_b128 a[220:223], %[wa] offset:33792\n\tds_read_b128 a[224:227], %[wa] offset:34816\n\t" \
    "ds_read_b128 a[228:231], %[wa] offset:35840\n\tds_read_b128 a[232:235], %[wb] offset:32768\n\tds_read_b128 a[236:239], %[wb] offset:33792\n\t" \
    "ds_read_b128 v[128:131], %[wb] offset:34816\n\ts_waitcnt lgkmcnt(7)\n\tv_mfma_f32_16x16x32_bf16 v[64:67], a[188:191], v[8:11], v[64:67]\n\t" \
    "v_mfma_f32_16x16x32_bf16 v[68:71], a[188:191], v[12:15], v[68:71]\n\tv_mfma_f32_16x16x32_bf16 v[72:75], a[192:195], v[8:11], v[72:75]\n\t" \
    "v_mfma_f32_16x16x32_bf16 v[76:79], a[192:195], v[12:15], v[76:79]\n\tv_mfma_f32_16x16x32_bf16 v[80:83], a[196:199], v[8:11], v[80:83]\n\t" \
    "v_mfma_f32_16x16x32_bf16 v[84:87], a[196:199], v[12:15], v[84:87]\n\tv_mfma_f32_16x16x32_bf16 v[88:91], a[200:203], v[8:11], v[88:91]\n\t" \
    "v_mfma_f32_16x16x32_bf16 v[92:95], a[200:203], v[12:15], v[92:95]\n\tv_mfma_f32_16x16x32_bf16 v[96:99], a[204:207], v[8:11], v[96:99]\n\t" \
    "v_mfma_f32_16x16x32_bf16 v[100:103], a[204:207], v[12:15], v[100:103]\n\tv_mfma_f32_16x16x32_bf16 v[104:107], a[208:211], v[8:11], v[104:107]\n\t" \
    "v_mfma_f32_16x16x32_bf16 v[108:111], a[208:211], v[12:15], v[108:111]\n\tv_mfma_f32_16x16x32_bf16 v[112:115], a[212:215], v[8:11], v[112:115]\n\t" \
    "v_mfma_f32_16x16x32_bf16 v[116:119], a[212:215], v[12:15], v[116:119]\n\tbuffer_load_dwordx4 v[8:11], %[x0], %[ws], s44 offen sc1\n\t" \
    "buffer_load_dwordx4 v[12:15], %[x1], %[ws], s44 offen sc1\n\ts_add_u32 s44, s44, 1024\n\ts_waitcnt vmcnt(12)\n\tds_read_b128 a[240:243], %[wa] offset:36864\n\t" \
    "ds_read_b128 a[244:247], %[wa] offset:37888\n\tds_read_b128 a[248:251], %[wa] offset:38912\n\tds_read_b128 a[252:255], %[wa] offset:39936\n\t" \
    "ds_read_b128 v[132:135], %[wb] offset:36864\n\tds_read_b128 v[136:139], %[wb] offset:37888\n\tds_read_b128 v[140:143], %[wb] offset:38912\n\ts_waitcnt lgkmcnt(7)\n\t" \
    "v_mfma_f32_16x16x32_bf16 v[64:67], a[216:219], v[16:19], v[64:67]\n\tv_mfma_f32_16x16x32_bf16 v[68:71], a[216:219], v[20:23], v[68:71]\n\t" \
    "v_mfma_f32_16x16x32_bf16 v[72:75], a[220:223], v[16:19], v[72:75]\n\tv_mfma_f32_16x16x32_bf16 v[76:79], a[220:223], v[20:23], v[76:79]\n\t" \
    "v_mfma_f32_16x16x32_bf16 v[80:83], a[224:227], v[16:19], v[80:83]\n\tv_mfma_f32_16x16x32_bf16 v[84:87], a[224:227], v[20:23], v[84:87]\n\t" \
    "v_mfma_f32_16x16x32_bf16 v[88:91], a[228:231], v[16:19], v[88:91]\n\tv_mfma_f32_16x16x32_bf16 v[92:95], a[228:231], v[20:23], v[92:95]\n\t" \
    "v_mfma_f32_16x16x32_bf16 v[96:99], a[232:235], v[16:19], v[96:99]\n\tv_mfma_f32_16x16x32_bf16 v[100:103], a[232:235], v[20:23], v[100:103]\n\t" \
    "v_mfma_f32_16x16x32_bf16 v[104:107], a[236:239], v[16:19], v[104:107]\n\tv_mfma_f32_16x16x32_bf16 v[108:111], a[236:239], v[20:23], v[108:111]\n\t" \
    "v_mfma_f32_16x16x32_bf16 v[112:115], v[128:131], v[16:19], v[112:115]\n\tv_mfma_f32_16x16x32_bf16 v[116:119], v[128:131], v[20:23], v[116:119]\n\t" \
    "buffer_load_dwordx4 v[16:19], %[x0], %[ws], s44 offen sc1\n\tbuffer_load_dwordx4 v[20:23], %[x1], %[ws], s44 offen sc1\n\ts_add_u32 s44, s44, 1024\n\ts_waitcnt vmcnt(12)\n\t" \
    "ds_read_b128 v[144:147], %[wa] offset:40960\n\tds_read_b128 v[148:151], %[wa] offset:41984\n\tds_read_b128 v[152:155], %[wa] offset:43008\n\t" \
    "ds_read_b128 v[156:159], %[wa] offset:44032\n\tds_read_b128 v[160:163], %[wb] offset:40960\n\tds_read_b128 v[164:167], %[wb] offset:41984\n\t" \
    "ds_read_b128 v[168:171], %[wb] offset:43008\n\ts_waitcnt lgkmcnt(7)\n\tv_mfma_f32_16x16x32_bf16 v[64:67], a[240:243], v[24:27], v[64:67]\n\t" \
    "v_mfma_f32_16x16x32_bf16 v[68:71], a[240:243], v[28:31], v[68:71]\n\tv_mfma_f32_16x16x32_bf16 v[72:75], a[244:247], v[24:27], v[72:75]\n\t" \
    "v_mfma_f32_16x16x32_bf16 v[76:79], a[244:247], v[28:31], v[76:79]\n\tv_mfma_f32_16x16x32_bf16 v[80:83], a[248:251], v[24:27], v[80:83]\n\t" \
    "v_mfma_f32_16x16x32_bf16 v[84:87], a[248:251], v[28:31], v[84:87]\n\tv_mfma_f32_16x16x32_bf16 v[88:91], a[252:255], v[24:27], v[88:91]\n\t" \
    "v_mfma_f32_16x16x32_bf16 v[92:95], a[252:255], v[28:31], v[92:95]\n\tv_mfma_f32_16x16x32_bf16 v[96:99], v[132:135], v[24:27], v[96:99]\n\t" \
    "v_mfma_f32_16x16x32_bf16 v[100:103], v[132:135], v[28:31], v[100:103]\n\tv_mfma_f32_16x16x32_bf16 v[104:107], v[136:139], v[24:27], v[104:107]\n\t" \
    "v_mfma_f32_16x16x32_bf16 v[108:111], v[136:139], v[28:31], v[108:111]\n\tv_mfma_f32_16x16x32_bf16 v[112:115], v[140:143], v[24:27], v[112:115]\n\t" \
    "v_mfma_f32_16x16x32_bf16 v[116:119], v[140:143], v[28:31], v[116:119]\n\tbuffer_load_dwordx4 v[24:27], %[x0], %[ws], s44 offen sc1\n\t" \
    "buffer_load_dwordx4 v[28:31], %[x1], %[ws], s44 offen sc1\n\ts_add_u32 s44, s44, 1024\n\ts_waitcnt vmcnt(12)\n\tds_read_b128 v[172:175], %[wa] offset:45056\n\t" \
    "ds_read_b128 v[176:179], %[wa] offset:46080\n\tds_read_b128 v[180:183], %[wa] offset:47104\n\tds_read_b128 v[184:187], %[wa] offset:48128\n\t" \
    "ds_read_b128 v[188:191], %[wb] offset:45056\n\tds_read_b128 v[192:195], %[wb] offset:46080\n\tds_read_b128 v[196:199], %[wb] offset:47104\n\ts_waitcnt lgkmcnt(7)\n\t" \
    "v_mfma_f32_16x16x32_bf16 v[64:67], v[144:147], v[32:35], v[64:67]\n\tv_mfma_f32_16x16x32_bf16 v[68:71], v[144:147], v[36:39], v[68:71]\n\t" \
    "v_mfma_f32_16x16x32_bf16 v[72:75], v[148:151], v[32:35], v[72:75]\n\tv_mfma_f32_16x16x32_bf16 v[76:79], v[148:151], v[36:39], v[76:79]\n\t" \
    "v_mfma_f32_16x16x32_bf16 v[80:83], v[152:155], v[32:35], v[80:83]\n\tv_mfma_f32_16x16x32_bf16 v[84:87], v[152:155], v[36:39], v[84:87]\n\t" \
    "v_mfma_f32_16x16x32_bf16 v[88:91], v[156:159], v[32:35], v[88:91]\n\tv_mfma_f32_16x16x32_bf16 v[92:95], v[156:159], v[36:39], v[92:95]\n\t" \
    "v_mfma_f32_16x16x32_bf16 v[96:99], v[160:163], v[32:35], v[96:99]\n\tv_mfma_f32_16x16x32_bf16 v[100:103], v[160:163], v[36:39], v[100:103]\n\t" \
    "v_mfma_f32_16x16x32_bf16 v[104:107], v[164:167], v[32:35], v[104:107]\n\tv_mfma_f32_16x16x32_bf16 v[108:111], v[164:167], v[36:39], v[108:111]\n\t" \
    "v_mfma_f32_16x16x32_bf16 v[112:115], v[168:171], v[32:35], v[112:115]\n\tv_mfma_f32_16x16x32_bf16 v[116:119], v[168:171], v[36:39], v[116:119]\n\t" \
    "buffer_load_dwordx4 v[32:35], %[x0], %[ws], s44 offen sc1\n\tbuffer_load_dwordx4 v[36:39], %[x1], %[ws], s44 offen sc1\n\ts_add_u32 s44, s44, 1024\n\ts_waitcnt vmcnt(12)\n\t" \
    "ds_read_b128 v[200:203], %[wa] offset:49152\n\tds_read_b128 v[204:207], %[wa] offset:50176\n\tds_read_b128 v[208:211], %[wa] offset:51200\n\t" \
    "ds_read_b128 v[212:215], %[wa] offset:52224\n\tds_read_b128 v[216:219], %[wb] offset:49152\n\tds_read_b128 v[220:223], %[wb] offset:50176\n\t" \
    "ds_read_b128 v[224:227], %[wb] offset:51200\n\ts_waitcnt lgkmcnt(7)\n\tv_mfma_f32_16x16x32_bf16 v[64:67], v[172:175], v[40:43], v[64:67]\n\t" \
    "v_mfma_f32_16x16x32_bf16 v[68:71], v[172:175], v[44:47], v[68:71]\n\tv_mfma_f32_16x16x32_bf16 v[72:75], v[176:179], v[40:43], v[72:75]\n\t" \
    "v_mfma_f32_16x16x32_bf16 v[76:79], v[176:179], v[44:47], v[76:79]\n\tv_mfma_f32_16x16x32_bf16 v[80:83], v[180:183], v[40:43], v[80:83]\n\t" \
    "v_mfma_f32_16x16x32_bf16 v[84:87], v[180:183], v[44:47], v[84:87]\n\tv_mfma_f32_16x16x32_bf16 v[88:91], v[184:187], v[40:43], v[88:91]\n\t" \
    "v_mfma_f32_16x16x32_bf16 v[92:95], v[184:187], v[44:47], v[92:95]\n\tv_mfma_f32_16x16x32_bf16 v[96:99], v[188:191], v[40:43], v[96:99]\n\t" \
    "v_mfma_f32_16x16x32_bf16 v[100:103], v[188:191], v[44:47], v[100:103]\n\tv_mfma_f32_16x16x32_bf16 v[104:107], v[192:195], v[40:43], v[104:107]\n\t" \
    "v_mfma_f32_16x16x32_bf16 v[108:111], v[192:195], v[44:47], v[108:111]\n\tv_mfma_f32_16x16x32_bf16 v[112:115], v[196:199], v[40:43], v[112:115]\n\t" \
    "v_mfma_f32_16x16x32_bf16 v[116:119], v[196:199], v[44:47], v[116:119]\n\tbuffer_load_dwordx4 v[40:43], %[x0], %[ws], s44 offen sc1\n\t" \
    "buffer_load_dwordx4 v[44:47], %[x1], %[ws], s44 offen sc1\n\ts_add_u32 s44, s44, 1024\n\ts_waitcnt vmcnt(12)\n\tds_read_b128 v[228:231], %[wa] offset:53248\n\t" \
    "ds_read_b128 v[232:235], %[wa] offset:54272\n\tds_read_b128 v[236:239], %[wa] offset:55296\n\tds_read_b128 v[240:243], %[wa] offset:56320\n\t" \
    "ds_read_b128 v[244:247], %[wb] offset:53248\n\tds_read_b128 v[248:251], %[wb] offset:54272\n\tds_read_b128 v[252:255], %[wb] offset:55296\n\ts_waitcnt lgkmcnt(7)\n\t" \
    "v_mfma_f32_16x16x32_bf16 v[64:67], v[200:203], v[48:51], v[64:67]\n\tv_mfma_f32_16x16x32_bf16 v[68:71], v[200:203], v[52:55], v[68:71]\n\t" \
    "v_mfma_f32_16x16x32_bf16 v[72:75], v[204:207], v[48:51], v[72:75]\n\tv_mfma_f32_16x16x32_bf16 v[76:79], v[204:207], v[52:55], v[76:79]\n\t" \
    "v_mfma_f32_16x16x32_bf16 v[80:83], v[208:211], v[48:51], v[80:83]\n\tv_mfma_f32_16x16x32_bf16 v[84:87], v[208:211], v[52:55], v[84:87]\n\t" \
    "v_mfma_f32_16x16x32_bf16 v[88:91], v[212:215], v[48:51], v[88:91]\n\tv_mfma_f32_16x16x32_bf16 v[92:95], v[212:215], v[52:55], v[92:95]\n\t" \
    "v_mfma_f32_16x16x32_bf16 v[96:99], v[216:219], v[48:51], v[96:99]\n\tv_mfma_f32_16x16x32_bf16 v[100:103], v[216:219], v[52:55], v[100:103]\n\t" \
    "v_mfma_f32_16x16x32_bf16 v[104:107], v[220:223], v[48:51], v[104:107]\n\tv_mfma_f32_16x16x32_bf16 v[108:111], v[220:223], v[52:55], v[108:111]\n\t" \
    "v_mfma_f32_16x16x32_bf16 v[112:115], v[224:227], v[48:51], v[112:115]\n\tv_mfma_f32_16x16x32_bf16 v[116:119], v[224:227], v[52:55], v[116:119]\n\t" \
    "buffer_load_dwordx4 v[48:51], %[x0], %[ws], s44 offen sc1\n\tbuffer_load_dwordx4 v[52:55], %[x1], %[ws], s44 offen sc1\n\ts_add_u32 s44, s44, 1024\n\ts_waitcnt vmcnt(12)\n\t" \
    "s_waitcnt lgkmcnt(0)\n\tv_mfma_f32_16x16x32_bf16 v[64:67], v[228:231], v[56:59], v[64:67]\n\tv_mfma_f32_16x16x32_bf16 v[68:71], v[228:231], v[60:63], v[68:71]\n\t" \
    "v_mfma_f32_16x16x32_bf16 v[72:75], v[232:235], v[56:59], v[72:75]\n\tv_mfma_f32_16x16x32_bf16 v[76:79], v[232:235], v[60:63], v[76:79]\n\t" \
    "v_mfma_f32_16x16x32_bf16 v[80:83], v[236:239], v[56:59], v[80:83]\n\tv_cvt_pk_bf16_f32 v120, v64, v65\n\tv_cvt_pk_bf16_f32 v122, v68, v69\n\tv_cvt_pk_bf16_f32 v121, v66, v67\n\t" \
    "v_cvt_pk_bf16_f32 v123, v70, v71\n\tv_mfma_f32_16x16x32_bf16 v[84:87], v[236:239], v[60:63], v[84:87]\n\tv_permlane16_swap_b32 v120, v122\n\ts_nop 0\n\t" \
    "v_permlane16_swap_b32 v121, v123\n\tbuffer_store_dwordx4 v[120:123], %[so], %[ws], s46 offen offset:0 sc1\n\tv_mfma_f32_16x16x32_bf16 v[88:91], v[240:243], v[56:59], v[88:91]\n\t" \
    "v_cvt_pk_bf16_f32 v124, v72, v73\n\tv_cvt_pk_bf16_f32 v126, v76, v77\n\tv_cvt_pk_bf16_f32 v125, v74, v75\n\tv_cvt_pk_bf16_f32 v127, v78, v79\n\t" \
    "v_mfma_f32_16x16x32_bf16 v[92:95], v[240:243], v[60:63], v[92:95]\n\tv_permlane16_swap_b32 v124, v126\n\ts_nop 0\n\tv_permlane16_swap_b32 v125, v127\n\t" \
    "buffer_store_dwordx4 v[124:127], %[so], %[ws], s46 offen offset:512 sc1\n\tv_mfma_f32_16x16x32_bf16 v[96:99], v[244:247], v[56:59], v[96:99]\n\tv_cvt_pk_bf16_f32 v128, v80, v81\n\t" \
    "v_cvt_pk_bf16_f32 v130, v84, v85\n\tv_cvt_pk_bf16_f32 v129, v82, v83\n\tv_cvt_pk_bf16_f32 v131, v86, v87\n\t" \
    "v_mfma_f32_16x16x32_bf16 v[100:103], v[244:247], v[60:63], v[100:103]\n\tv_permlane16_swap_b32 v128, v130\n\ts_nop 0\n\tv_permlane16_swap_b32 v129, v131\n\t" \
    "buffer_store_dwordx4 v[128:131], %[so], %[ws], s46 offen offset:1024 sc1\n\tv_mfma_f32_16x16x32_bf16 v[104:107], v[248:251], v[56:59], v[104:107]\n\t" \
    "v_cvt_pk_bf16_f32 v120, v88, v89\n\tv_cvt_pk_bf16_f32 v122, v92, v93\n\tv_cvt_pk_bf16_f32 v121, v90, v91\n\tv_cvt_pk_bf16_f32 v123, v94, v95\n\t" \
    "v_mfma_f32_16x16x32_bf16 v[108:111], v[248:251], v[60:63], v[108:111]\n\tv_permlane16_swap_b32 v120, v122\n\ts_nop 0\n\tv_permlane16_swap_b32 v121, v123\n\t" \
    "buffer_store_dwordx4 v[120:123], %[so], %[ws], s46 offen offset:1536 sc1\n\tv_mfma_f32_16x16x32_bf16 v[112:115], v[252:255], v[56:59], v[112:115]\n\t" \
    "v_cvt_pk_bf16_f32 v124, v96, v97\n\tv_cvt_pk_bf16_f32 v126, v100, v101\n\tv_cvt_pk_bf16_f32 v125, v98, v99\n\tv_cvt_pk_bf16_f32 v127, v102, v103\n\t" \
    "v_mfma_f32_16x16x32_bf16 v[116:119], v[252:255], v[60:63], v[116:119]\n\tv_permlane16_swap_b32 v124, v126\n\ts_nop 0\n\tv_permlane16_swap_b32 v125, v127\n\t" \
    "buffer_store_dwordx4 v[124:127], %[so], %[ws], s46 offen offset:2048 sc1\n\tbuffer_load_dwordx4 v[56:59], %[x0], %[ws], s44 offen sc1\n\t" \
    "buffer_load_dwordx4 v[60:63], %[x1], %[ws], s44 offen sc1\n\ts_add_u32 s44, s44, 1024\n\tgl%=:\n\ts_waitcnt vmcnt(17)\n\tv_mfma_f32_16x16x32_bf16 v[64:67], a[0:3], v[8:11], 0\n\t" \
    "v_cvt_pk_bf16_f32 v128, v104, v105\n\tv_cvt_pk_bf16_f32 v130, v108, v109\n\tv_cvt_pk_bf16_f32 v129, v106, v107\n\tv_cvt_pk_bf16_f32 v131, v110, v111\n\t" \
    "v_mfma_f32_16x16x32_bf16 v[68:71], a[0:3], v[12:15], 0\n\tv_permlane16_swap_b32 v128, v130\n\ts_nop 0\n\tv_permlane16_swap_b32 v129, v131\n\t" \
    "buffer_store_dwordx4 v[128:131], %[so], %[ws], s46 offen offset:2560 sc1\n\tv_mfma_f32_16x16x32_bf16 v[72:75], a[4:7], v[8:11], 0\n\tv_cvt_pk_bf16_f32 v120, v112, v113\n\t" \
    "v_cvt_pk_bf16_f32 v122, v116, v117\n\tv_cvt_pk_bf16_f32 v121, v114, v115\n\tv_cvt_pk_bf16_f32 v123, v118, v119\n\tv_mfma_f32_16x16x32_bf16 v[76:79], a[4:7], v[12:15], 0\n\t" \
    "v_permlane16_swap_b32 v120, v122\n\ts_nop 0\n\tv_permlane16_swap_b32 v121, v123\n\tbuffer_store_dwordx4 v[120:123], %[so], %[ws], s46 offen offset:3072 sc1\n\t" \
    "v_mfma_f32_16x16x32_bf16 v[80:83], a[8:11], v[8:11], 0\n\tv_mfma_f32_16x16x32_bf16 v[84:87], a[8:11], v[12:15], 0\n\tv_mfma_f32_16x16x32_bf16 v[88:91], a[12:15], v[8:11], 0\n\t" \
    "v_mfma_f32_16x16x32_bf16 v[92:95], a[12:15], v[12:15], 0\n\tv_mfma_f32_16x16x32_bf16 v[96:99], a[16:19], v[8:11], 0\n\t" \
    "v_mfma_f32_16x16x32_bf16 v[100:103], a[16:19], v[12:15], 0\n\tv_mfma_f32_16x16x32_bf16 v[104:107], a[20:23], v[8:11], 0\n\t" \
    "v_mfma_f32_16x16x32_bf16 v[108:111], a[20:23], v[12:15], 0\n\tv_mfma_f32_16x16x32_bf16 v[112:115], a[24:27], v[8:11], 0\n\t" \
    "v_mfma_f32_16x16x32_bf16 v[116:119], a[24:27], v[12:15], 0\n\tbuffer_load_dwordx4 v[8:11], %[x0], %[ws], s44 offen sc1\n\t" \
    "buffer_load_dwordx4 v[12:15], %[x1], %[ws], s44 offen sc1\n\ts_add_u32 s44, s44, 1024\n\ts_waitcnt vmcnt(19)\n\tv_mfma_f32_16x16x32_bf16 v[64:67], a[28:31], v[16:19], v[64:67]\n\t" \
    "v_mfma_f32_16x16x32_bf16 v[68:71], a[28:31], v[20:23], v[68:71]\n\tv_mfma_f32_16x16x32_bf16 v[72:75], a[32:35], v[16:19], v[72:75]\n\t" \
    "v_mfma_f32_16x16x32_bf16 v[76:79], a[32:35], v[20:23], v[76:79]\n\tv_mfma_f32_16x16x32_bf16 v[80:83], a[36:39], v[16:19], v[80:83]\n\t" \
    "v_mfma_f32_16x16x32_bf16 v[84:87], a[36:39], v[20:23], v[84:87]\n\tv_mfma_f32_16x16x32_bf16 v[88:91], a[40:43], v[16:19], v[88:91]\n\t" \
    "v_mfma_f32_16x16x32_bf16 v[92:95], a[40:43], v[20:23], v[92:95]\n\tv_mfma_f32_16x16x32_bf16 v[96:99], a[44:47], v[16:19], v[96:99]\n\t" \
    "v_mfma_f32_16x16x32_bf16 v[100:103], a[44:47], v[20:23], v[100:103]\n\tv_mfma_f32_16x16x32_bf16 v[104:107], a[48:51], v[16:19], v[104:107]\n\t" \
    "v_mfma_f32_16x16x32_bf16 v[108:111], a[48:51], v[20:23], v[108:111]\n\tv_mfma_f32_16x16x32_bf16 v[112:115], a[52:55], v[16:19], v[112:115]\n\t" \
    "v_mfma_f32_16x16x32_bf16 v[116:119], a[52:55], v[20:23], v[116:119]\n\tbuffer_load_dwordx4 v[16:19], %[x0], %[ws], s44 offen sc1\n\t" \
    "buffer_load_dwordx4 v[20:23], %[x1], %[ws], s44 offen sc1\n\ts_add_u32 s44, s44, 1024\n\ts_add_u32 s46, s46, 1835008\n\ts_add_u32 s47, s47, 1835008\n\ts_waitcnt vmcnt(19)\n\t" \
    "ds_read_b128 v[120:123], %[wb] offset:18432\n\tds_read_b128 v[124:127], %[wb] offset:26624\n\tds_read_b128 v[128:131], %[wb] offset:34816\n\t" \
    "v_mfma_f32_16x16x32_bf16 v[64:67], a[56:59], v[24:27], v[64:67]\n\tv_mfma_f32_16x16x32_bf16 v[68:71], a[56:59], v[28:31], v[68:71]\n\t" \
    "v_mfma_f32_16x16x32_bf16 v[72:75], a[60:63], v[24:27], v[72:75]\n\tv_mfma_f32_16x16x32_bf16 v[76:79], a[60:63], v[28:31], v[76:79]\n\t" \
    "v_mfma_f32_16x16x32_bf16 v[80:83], a[64:67], v[24:27], v[80:83]\n\tv_mfma_f32_16x16x32_bf16 v[84:87], a[64:67], v[28:31], v[84:87]\n\t" \
    "v_mfma_f32_16x16x32_bf16 v[88:91], a[68:71], v[24:27], v[88:91]\n\tv_mfma_f32_16x16x32_bf16 v[92:95], a[68:71], v[28:31], v[92:95]\n\t" \
    "v_mfma_f32_16x16x32_bf16 v[96:99], a[72:75], v[24:27], v[96:99]\n\tv_mfma_f32_16x16x32_bf16 v[100:103], a[72:75], v[28:31], v[100:103]\n\t" \
    "v_mfma_f32_16x16x32_bf16 v[104:107], a[76:79], v[24:27], v[104:107]\n\tv_mfma_f32_16x16x32_bf16 v[108:111], a[76:79], v[28:31], v[108:111]\n\t" \
    "v_mfma_f32_16x16x32_bf16 v[112:115], a[80:83], v[24:27], v[112:115]\n\tv_mfma_f32_16x16x32_bf16 v[116:119], a[80:83], v[28:31], v[116:119]\n\t" \
    "buffer_load_dwordx4 v[24:27], %[x0], %[ws], s44 offen sc1\n\tbuffer_load_dwordx4 v[28:31], %[x1], %[ws], s44 offen sc1\n\ts_add_u32 s44, s44, 1024\n\ts_waitcnt vmcnt(19)\n\t" \
    "v_mfma_f32_16x16x32_bf16 v[64:67], a[84:87], v[32:35], v[64:67]\n\tv_mfma_f32_16x16x32_bf16 v[68:71], a[84:87], v[36:39], v[68:71]\n\t" \
    "v_mfma_f32_16x16x32_bf16 v[72:75], a[88:91], v[32:35], v[72:75]\n\tv_mfma_f32_16x16x32_bf16 v[76:79], a[88:91], v[36:39], v[76:79]\n\t" \
    "v_mfma_f32_16x16x32_bf16 v[80:83], a[92:95], v[32:35], v[80:83]\n\tv_mfma_f32_16x16x32_bf16 v[84:87], a[92:95], v[36:39], v[84:87]\n\t" \
    "v_mfma_f32_16x16x32_bf16 v[88:91], a[96:99], v[32:35], v[88:91]\n\tv_mfma_f32_16x16x32_bf16 v[92:95], a[96:99], v[36:39], v[92:95]\n\t" \
    "v_mfma_f32_16x16x32_bf16 v[96:99], a[100:103], v[32:35], v[96:99]\n\tv_mfma_f32_16x16x32_bf16 v[100:103], a[100:103], v[36:39], v[100:103]\n\t" \
    "v_mfma_f32_16x16x32_bf16 v[104:107], a[104:107], v[32:35], v[104:107]\n\tv_mfma_f32_16x16x32_bf16 v[108:111], a[104:107], v[36:39], v[108:111]\n\t" \
    "v_mfma_f32_16x16x32_bf16 v[112:115], a[108:111], v[32:35], v[112:115]\n\tv_mfma_f32_16x16x32_bf16 v[116:119], a[108:111], v[36:39], v[116:119]\n\t" \
    "buffer_load_dwordx4 v[32:35], %[x0], %[ws], s44 offen sc1\n\tbuffer_load_dwordx4 v[36:39], %[x1], %[ws], s44 offen sc1\n\ts_add_u32 s44, s44, 1024\n\ts_waitcnt vmcnt(19)\n\t" \
    "s_waitcnt vmcnt(8)\n\tbuffer_store_dword %[x0], %[fo], %[ws], %[fs] offen sc1\n\ts_waitcnt lgkmcnt(0)\n\tv_mfma_f32_16x16x32_bf16 v[64:67], a[112:115], v[40:43], v[64:67]\n\t" \
    "v_mfma_f32_16x16x32_bf16 v[68:71], a[112:115], v[44:47], v[68:71]\n\tv_mfma_f32_16x16x32_bf16 v[72:75], a[116:119], v[40:43], v[72:75]\n\t" \
    "v_mfma_f32_16x16x32_bf16 v[76:79], a[116:119], v[44:47], v[76:79]\n\tv_mfma_f32_16x16x32_bf16 v[80:83], a[120:123], v[40:43], v[80:83]\n\t" \
    "v_mfma_f32_16x16x32_bf16 v[84:87], a[120:123], v[44:47], v[84:87]\n\tv_mfma_f32_16x16x32_bf16 v[88:91], a[124:127], v[40:43], v[88:91]\n\t" \
    "v_mfma_f32_16x16x32_bf16 v[92:95], a[124:127], v[44:47], v[92:95]\n\tv_mfma_f32_16x16x32_bf16 v[96:99], a[128:131], v[40:43], v[96:99]\n\t" \
    "v_mfma_f32_16x16x32_bf16 v[100:103], a[128:131], v[44:47], v[100:103]\n\tv_mfma_f32_16x16x32_bf16 v[104:107], a[132:135], v[40:43], v[104:107]\n\t" \
    "v_mfma_f32_16x16x32_bf16 v[108:111], a[132:135], v[44:47], v[108:111]\n\tv_mfma_f32_16x16x32_bf16 v[112:115], v[120:123], v[40:43], v[112:115]\n\t" \
    "v_mfma_f32_16x16x32_bf16 v[116:119], v[120:123], v[44:47], v[116:119]\n\tbuffer_load_dwordx4 v[40:43], %[x0], %[ws], s44 offen sc1\n\t" \
    "buffer_load_dwordx4 v[44:47], %[x1], %[ws], s44 offen sc1\n\ts_add_u32 s44, s44, 1024\n\ts_waitcnt vmcnt(20)\n\t" \
    "v_mfma_f32_16x16x32_bf16 v[64:67], a[136:139], v[48:51], v[64:67]\n\tv_mfma_f32_16x16x32_bf16 v[68:71], a[136:139], v[52:55], v[68:71]\n\t" \
    "v_mfma_f32_16x16x32_bf16 v[72:75], a[140:143], v[48:51], v[72:75]\n\tv_mfma_f32_16x16x32_bf16 v[76:79], a[140:143], v[52:55], v[76:79]\n\t" \
    "v_mfma_f32_16x16x32_bf16 v[80:83], a[144:147], v[48:51], v[80:83]\n\tv_mfma_f32_16x16x32_bf16 v[84:87], a[144:147], v[52:55], v[84:87]\n\t" \
    "v_mfma_f32_16x16x32_bf16 v[88:91], a[148:151], v[48:51], v[88:91]\n\tv_mfma_f32_16x16x32_bf16 v[92:95], a[148:151], v[52:55], v[92:95]\n\t" \
    "v_mfma_f32_16x16x32_bf16 v[96:99], a[152:155], v[48:51], v[96:99]\n\tv_mfma_f32_16x16x32_bf16 v[100:103], a[152:155], v[52:55], v[100:103]\n\t" \
    "v_mfma_f32_16x16x32_bf16 v[104:107], a[156:159], v[48:51], v[104:107]\n\tv_mfma_f32_16x16x32_bf16 v[108:111], a[156:159], v[52:55], v[108:111]\n\t" \
    "v_mfma_f32_16x16x32_bf16 v[112:115], a[160:163], v[48:51], v[112:115]\n\tv_mfma_f32_16x16x32_bf16 v[116:119], a[160:163], v[52:55], v[116:119]\n\t" \
    "buffer_load_dwordx4 v[48:51], %[x0], %[ws], s44 offen sc1\n\tbuffer_load_dwordx4 v[52:55], %[x1], %[ws], s44 offen sc1\n\ts_add_u32 s44, s44, 1024\n\t" \
    "s_max_u32 %[fs], %[fs], 0x2000\n\ts_add_u32 %[fs], %[fs], 0x800\n\ts_waitcnt vmcnt(15)\n\tv_mfma_f32_16x16x32_bf16 v[64:67], a[164:167], v[56:59], v[64:67]\n\t" \
    "v_mfma_f32_16x16x32_bf16 v[68:71], a[164:167], v[60:63], v[68:71]\n\tv_mfma_f32_16x16x32_bf16 v[72:75], a[168:171], v[56:59], v[72:75]\n\t" \
    "v_mfma_f32_16x16x32_bf16 v[76:79], a[168:171], v[60:63], v[76:79]\n\tv_mfma_f32_16x16x32_bf16 v[80:83], a[172:175], v[56:59], v[80:83]\n\t" \
    "v_mfma_f32_16x16x32_bf16 v[84:87], a[172:175], v[60:63], v[84:87]\n\tv_mfma_f32_16x16x32_bf16 v[88:91], a[176:179], v[56:59], v[88:91]\n\t" \
    "v_mfma_f32_16x16x32_bf16 v[92:95], a[176:179], v[60:63], v[92:95]\n\tv_mfma_f32_16x16x32_bf16 v[96:99], a[180:183], v[56:59], v[96:99]\n\t" \
    "v_mfma_f32_16x16x32_bf16 v[100:103], a[180:183], v[60:63], v[100:103]\n\tv_mfma_f32_16x16x32_bf16 v[104:107], a[184:187], v[56:59], v[104:107]\n\t" \
    "v_mfma_f32_16x16x32_bf16 v[108:111], a[184:187], v[60:63], v[108:111]\n\tv_mfma_f32_16x16x32_bf16 v[112:115], v[124:127], v[56:59], v[112:115]\n\t" \
    "v_mfma_f32_16x16x32_bf16 v[116:119], v[124:127], v[60:63], v[116:119]\n\tbuffer_load_dwordx4 v[56:59], %[x0], %[ws], s44 offen sc1\n\t" \
    "buffer_load_dwordx4 v[60:63], %[x1], %[ws], s44 offen sc1\n\ts_add_u32 s44, s44, 101376\n\ts_waitcnt vmcnt(13)\n\t" \
    "v_mfma_f32_16x16x32_bf16 v[64:67], a[188:191], v[8:11], v[64:67]\n\tv_mfma_f32_16x16x32_bf16 v[68:71], a[188:191], v[12:15], v[68:71]\n\t" \
    "v_mfma_f32_16x16x32_bf16 v[72:75], a[192:195], v[8:11], v[72:75]\n\tv_mfma_f32_16x16x32_bf16 v[76:79], a[192:195], v[12:15], v[76:79]\n\t" \
    "v_mfma_f32_16x16x32_bf16 v[80:83], a[196:199], v[8:11], v[80:83]\n\tv_mfma_f32_16x16x32_bf16 v[84:87], a[196:199], v[12:15], v[84:87]\n\t" \
    "v_mfma_f32_16x16x32_bf16 v[88:91], a[200:203], v[8:11], v[88:91]\n\tv_mfma_f32_16x16x32_bf16 v[92:95], a[200:203], v[12:15], v[92:95]\n\t" \
    "v_mfma_f32_16x16x32_bf16 v[96:99], a[204:207], v[8:11], v[96:99]\n\tv_mfma_f32_16x16x32_bf16 v[100:103], a[204:207], v[12:15], v[100:103]\n\t" \
    "v_mfma_f32_16x16x32_bf16 v[104:107], a[208:211], v[8:11], v[104:107]\n\tv_mfma_f32_16x16x32_bf16 v[108:111], a[208:211], v[12:15], v[108:111]\n\t" \
    "v_mfma_f32_16x16x32_bf16 v[112:115], a[212:215], v[8:11], v[112:115]\n\tv_mfma_f32_16x16x32_bf16 v[116:119], a[212:215], v[12:15], v[116:119]\n\t" \
    "buffer_load_dwordx4 v[8:11], %[x0], %[ws], s44 offen sc1\n\tbuffer_load_dwordx4 v[12:15], %[x1], %[ws], s44 offen sc1\n\ts_add_u32 s44, s44, 1024\n\ts_waitcnt vmcnt(13)\n\t" \
    "v_mfma_f32_16x16x32_bf16 v[64:67], a[216:219], v[16:19], v[64:67]\n\tv_mfma_f32_16x16x32_bf16 v[68:71], a[216:219], v[20:23], v[68:71]\n\t" \
    "v_mfma_f32_16x16x32_bf16 v[72:75], a[220:223], v[16:19], v[72:75]\n\tv_mfma_f32_16x16x32_bf16 v[76:79], a[220:223], v[20:23], v[76:79]\n\t" \
    "v_mfma_f32_16x16x32_bf16 v[80:83], a[224:227], v[16:19], v[80:83]\n\tv_mfma_f32_16x16x32_bf16 v[84:87], a[224:227], v[20:23], v[84:87]\n\t" \
    "v_mfma_f32_16x16x32_bf16 v[88:91], a[228:231], v[16:19], v[88:91]\n\tv_mfma_f32_16x16x32_bf16 v[92:95], a[228:231], v[20:23], v[92:95]\n\t" \
    "v_mfma_f32_16x16x32_bf16 v[96:99], a[232:235], v[16:19], v[96:99]\n\tv_mfma_f32_16x16x32_bf16 v[100:103], a[232:235], v[20:23], v[100:103]\n\t" \
    "v_mfma_f32_16x16x32_bf16 v[104:107], a[236:239], v[16:19], v[104:107]\n\tv_mfma_f32_16x16x32_bf16 v[108:111], a[236:239], v[20:23], v[108:111]\n\t" \
    "v_mfma_f32_16x16x32_bf16 v[112:115], v[128:131], v[16:19], v[112:115]\n\tv_mfma_f32_16x16x32_bf16 v[116:119], v[128:131], v[20:23], v[116:119]\n\t" \
    "buffer_load_dwordx4 v[16:19], %[x0], %[ws], s44 offen sc1\n\tbuffer_load_dwordx4 v[20:23], %[x1], %[ws], s44 offen sc1\n\ts_add_u32 s44, s44, 1024\n\ts_waitcnt vmcnt(13)\n\t" \
    "v_mfma_f32_16x16x32_bf16 v[64:67], a[240:243], v[24:27], v[64:67]\n\tv_mfma_f32_16x16x32_bf16 v[68:71], a[240:243], v[28:31], v[68:71]\n\t" \
    "v_mfma_f32_16x16x32_bf16 v[72:75], a[244:247], v[24:27], v[72:75]\n\tv_mfma_f32_16x16x32_bf16 v[76:79], a[244:247], v[28:31], v[76:79]\n\t" \
    "v_mfma_f32_16x16x32_bf16 v[80:83], a[248:251], v[24:27], v[80:83]\n\tv_mfma_f32_16x16x32_bf16 v[84:87], a[248:251], v[28:31], v[84:87]\n\t" \
    "v_mfma_f32_16x16x32_bf16 v[88:91], a[252:255], v[24:27], v[88:91]\n\tv_mfma_f32_16x16x32_bf16 v[92:95], a[252:255], v[28:31], v[92:95]\n\t" \
    "v_mfma_f32_16x16x32_bf16 v[96:99], v[132:135], v[24:27], v[96:99]\n\tv_mfma_f32_16x16x32_bf16 v[100:103], v[132:135], v[28:31], v[100:103]\n\t" \
    "v_mfma_f32_16x16x32_bf16 v[104:107], v[136:139], v[24:27], v[104:107]\n\tv_mfma_f32_16x16x32_bf16 v[108:111], v[136:139], v[28:31], v[108:111]\n\t" \
    "v_mfma_f32_16x16x32_bf16 v[112:115], v[140:143], v[24:27], v[112:115]\n\tv_mfma_f32_16x16x32_bf16 v[116:119], v[140:143], v[28:31], v[116:119]\n\t" \
    "buffer_load_dwordx4 v[24:27], %[x0], %[ws], s44 offen sc1\n\tbuffer_load_dwordx4 v[28:31], %[x1], %[ws], s44 offen sc1\n\ts_add_u32 s44, s44, 1024\n\ts_waitcnt vmcnt(13)\n\t" \
    "v_mfma_f32_16x16x32_bf16 v[64:67], v[144:147], v[32:35], v[64:67]\n\tv_mfma_f32_16x16x32_bf16 v[68:71], v[144:147], v[36:39], v[68:71]\n\t" \
    "v_mfma_f32_16x16x32_bf16 v[72:75], v[148:151], v[32:35], v[72:75]\n\tv_mfma_f32_16x16x32_bf16 v[76:79], v[148:151], v[36:39], v[76:79]\n\t" \
    "v_mfma_f32_16x16x32_bf16 v[80:83], v[152:155], v[32:35], v[80:83]\n\tv_mfma_f32_16x16x32_bf16 v[84:87], v[152:155], v[36:39], v[84:87]\n\t" \
    "v_mfma_f32_16x16x32_bf16 v[88:91], v[156:159], v[32:35], v[88:91]\n\tv_mfma_f32_16x16x32_bf16 v[92:95], v[156:159], v[36:39], v[92:95]\n\t" \
    "v_mfma_f32_16x16x32_bf16 v[96:99], v[160:163], v[32:35], v[96:99]\n\tv_mfma_f32_16x16x32_bf16 v[100:103], v[160:163], v[36:39], v[100:103]\n\t" \
    "v_mfma_f32_16x16x32_bf16 v[104:107], v[164:167], v[32:35], v[104:107]\n\tv_mfma_f32_16x16x32_bf16 v[108:111], v[164:167], v[36:39], v[108:111]\n\t" \
    "v_mfma_f32_16x16x32_bf16 v[112:115], v[168:171], v[32:35], v[112:115]\n\tv_mfma_f32_16x16x32_bf16 v[116:119], v[168:171], v[36:39], v[116:119]\n\t" \
    "buffer_load_dwordx4 v[32:35], %[x0], %[ws], s44 offen sc1\n\tbuffer_load_dwordx4 v[36:39], %[x1], %[ws], s44 offen sc1\n\ts_add_u32 s44, s44, 1024\n\ts_waitcnt vmcnt(12)\n\t" \
    "v_mfma_f32_16x16x32_bf16 v[64:67], v[172:175], v[40:43], v[64:67]\n\tv_mfma_f32_16x16x32_bf16 v[68:71], v[172:175], v[44:47], v[68:71]\n\t" \
    "v_mfma_f32_16x16x32_bf16 v[72:75], v[176:179], v[40:43], v[72:75]\n\tv_mfma_f32_16x16x32_bf16 v[76:79], v[176:179], v[44:47], v[76:79]\n\t" \
    "v_mfma_f32_16x16x32_bf16 v[80:83], v[180:183], v[40:43], v[80:83]\n\tv_mfma_f32_16x16x32_bf16 v[84:87], v[180:183], v[44:47], v[84:87]\n\t" \
    "v_mfma_f32_16x16x32_bf16 v[88:91], v[184:187], v[40:43], v[88:91]\n\tv_mfma_f32_16x16x32_bf16 v[92:95], v[184:187], v[44:47], v[92:95]\n\t" \
    "v_mfma_f32_16x16x32_bf16 v[96:99], v[188:191], v[40:43], v[96:99]\n\tv_mfma_f32_16x16x32_bf16 v[100:103], v[188:191], v[44:47], v[100:103]\n\t" \
    "v_mfma_f32_16x16x32_bf16 v[104:107], v[192:195], v[40:43], v[104:107]\n\tv_mfma_f32_16x16x32_bf16 v[108:111], v[192:195], v[44:47], v[108:111]\n\t" \
    "v_mfma_f32_16x16x32_bf16 v[112:115], v[196:199], v[40:43], v[112:115]\n\tv_mfma_f32_16x16x32_bf16 v[116:119], v[196:199], v[44:47], v[116:119]\n\t" \
    "buffer_load_dwordx4 v[40:43], %[x0], %[ws], s44 offen sc1\n\tbuffer_load_dwordx4 v[44:47], %[x1], %[ws], s44 offen sc1\n\ts_add_u32 s44, s44, 1024\n\ts_waitcnt vmcnt(12)\n\t" \
    "v_mfma_f32_16x16x32_bf16 v[64:67], v[200:203], v[48:51], v[64:67]\n\tv_mfma_f32_16x16x32_bf16 v[68:71], v[200:203], v[52:55], v[68:71]\n\t" \
    "v_mfma_f32_16x16x32_bf16 v[72:75], v[204:207], v[48:51], v[72:75]\n\tv_mfma_f32_16x16x32_bf16 v[76:79], v[204:207], v[52:55], v[76:79]\n\t" \
    "v_mfma_f32_16x16x32_bf16 v[80:83], v[208:211], v[48:51], v[80:83]\n\tv_mfma_f32_16x16x32_bf16 v[84:87], v[208:211], v[52:55], v[84:87]\n\t" \
    "v_mfma_f32_16x16x32_bf16 v[88:91], v[212:215], v[48:51], v[88:91]\n\tv_mfma_f32_16x16x32_bf16 v[92:95], v[212:215], v[52:55], v[92:95]\n\t" \
    "v_mfma_f32_16x16x32_bf16 v[96:99], v[216:219], v[48:51], v[96:99]\n\tv_mfma_f32_16x16x32_bf16 v[100:103], v[216:219], v[52:55], v[100:103]\n\t" \
    "v_mfma_f32_16x16x32_bf16 v[104:107], v[220:223], v[48:51], v[104:107]\n\tv_mfma_f32_16x16x32_bf16 v[108:111], v[220:223], v[52:55], v[108:111]\n\t" \
    "v_mfma_f32_16x16x32_bf16 v[112:115], v[224:227], v[48:51], v[112:115]\n\tv_mfma_f32_16x16x32_bf16 v[116:119], v[224:227], v[52:55], v[116:119]\n\t" \
    "buffer_load_dwordx4 v[48:51], %[x0], %[ws], s44 offen sc1\n\tbuffer_load_dwordx4 v[52:55], %[x1], %[ws], s44 offen sc1\n\ts_add_u32 s44, s44, 1024\n\ts_waitcnt vmcnt(12)\n\t" \
    "v_mfma_f32_16x16x32_bf16 v[64:67], v[228:231], v[56:59], v[64:67]\n\tv_mfma_f32_16x16x32_bf16 v[68:71], v[228:231], v[60:63], v[68:71]\n\t" \
    "v_mfma_f32_16x16x32_bf16 v[72:75], v[232:235], v[56:59], v[72:75]\n\tv_mfma_f32_16x16x32_bf16 v[76:79], v[232:235], v[60:63], v[76:79]\n\t" \
    "v_mfma_f32_16x16x32_bf16 v[80:83], v[236:239], v[56:59], v[80:83]\n\tv_cvt_pk_bf16_f32 v120, v64, v65\n\tv_cvt_pk_bf16_f32 v122, v68, v69\n\tv_cvt_pk_bf16_f32 v121, v66, v67\n\t" \
    "v_cvt_pk_bf16_f32 v123, v70, v71\n\tv_mfma_f32_16x16x32_bf16 v[84:87], v[236:239], v[60:63], v[84:87]\n\tv_permlane16_swap_b32 v120, v122\n\ts_nop 0\n\t" \
    "v_permlane16_swap_b32 v121, v123\n\tbuffer_store_dwordx4 v[120:123], %[so], %[ws], s46 offen offset:0 sc1\n\tv_mfma_f32_16x16x32_bf16 v[88:91], v[240:243], v[56:59], v[88:91]\n\t" \
    "v_cvt_pk_bf16_f32 v124, v72, v73\n\tv_cvt_pk_bf16_f32 v126, v76, v77\n\tv_cvt_pk_bf16_f32 v125, v74, v75\n\tv_cvt_pk_bf16_f32 v127, v78, v79\n\t" \
    "v_mfma_f32_16x16x32_bf16 v[92:95], v[240:243], v[60:63], v[92:95]\n\tv_permlane16_swap_b32 v124, v126\n\ts_nop 0\n\tv_permlane16_swap_b32 v125, v127\n\t" \
    "buffer_store_dwordx4 v[124:127], %[so], %[ws], s46 offen offset:512 sc1\n\tv_mfma_f32_16x16x32_bf16 v[96:99], v[244:247], v[56:59], v[96:99]\n\tv_cvt_pk_bf16_f32 v128, v80, v81\n\t" \
    "v_cvt_pk_bf16_f32 v130, v84, v85\n\tv_cvt_pk_bf16_f32 v129, v82, v83\n\tv_cvt_pk_bf16_f32 v131, v86, v87\n\t" \
    "v_mfma_f32_16x16x32_bf16 v[100:103], v[244:247], v[60:63], v[100:103]\n\tv_permlane16_swap_b32 v128, v130\n\ts_nop 0\n\tv_permlane16_swap_b32 v129, v131\n\t" \
    "buffer_store_dwordx4 v[128:131], %[so], %[ws], s46 offen offset:1024 sc1\n\tv_mfma_f32_16x16x32_bf16 v[104:107], v[248:251], v[56:59], v[104:107]\n\t" \
    "v_cvt_pk_bf16_f32 v120, v88, v89\n\tv_cvt_pk_bf16_f32 v122, v92, v93\n\tv_cvt_pk_bf16_f32 v121, v90, v91\n\tv_cvt_pk_bf16_f32 v123, v94, v95\n\t" \
    "v_mfma_f32_16x16x32_bf16 v[108:111], v[248:251], v[60:63], v[108:111]\n\tv_permlane16_swap_b32 v120, v122\n\ts_nop 0\n\tv_permlane16_swap_b32 v121, v123\n\t" \
    "buffer_store_dwordx4 v[120:123], %[so], %[ws], s46 offen offset:1536 sc1\n\tv_mfma_f32_16x16x32_bf16 v[112:115], v[252:255], v[56:59], v[112:115]\n\t" \
    "v_cvt_pk_bf16_f32 v124, v96, v97\n\tv_cvt_pk_bf16_f32 v126, v100, v101\n\tv_cvt_pk_bf16_f32 v125, v98, v99\n\tv_cvt_pk_bf16_f32 v127, v102, v103\n\t" \
    "v_mfma_f32_16x16x32_bf16 v[116:119], v[252:255], v[60:63], v[116:119]\n\tv_permlane16_swap_b32 v124, v126\n\ts_nop 0\n\tv_permlane16_swap_b32 v125, v127\n\t" \
    "buffer_store_dwordx4 v[124:127], %[so], %[ws], s46 offen offset:2048 sc1\n\tbuffer_load_dwordx4 v[56:59], %[x0], %[ws], s44 offen sc1\n\t" \
    "buffer_load_dwordx4 v[60:63], %[x1], %[ws], s44 offen sc1\n\ts_add_u32 s44, s44, 1024\n\ts_sub_u32 s45, s45, 1\n\ts_cmp_lg_u32 s45, 0\n\ts_cbranch_scc1 gl%=\n\ts_nop 7\n\t" \
    "v_cvt_pk_bf16_f32 v128, v104, v105\n\tv_cvt_pk_bf16_f32 v130, v108, v109\n\tv_cvt_pk_bf16_f32 v129, v106, v107\n\tv_cvt_pk_bf16_f32 v131, v110, v111\n\ts_nop 0\n\t" \
    "v_permlane16_swap_b32 v128, v130\n\ts_nop 0\n\tv_permlane16_swap_b32 v129, v131\n\tbuffer_store_dwordx4 v[128:131], %[so], %[ws], s46 offen offset:2560 sc1\n\t" \
    "v_cvt_pk_bf16_f32 v120, v112, v113\n\tv_cvt_pk_bf16_f32 v122, v116, v117\n\tv_cvt_pk_bf16_f32 v121, v114, v115\n\tv_cvt_pk_bf16_f32 v123, v118, v119\n\ts_nop 0\n\t" \
    "v_permlane16_swap_b32 v120, v122\n\ts_nop 0\n\tv_permlane16_swap_b32 v121, v123\n\tbuffer_store_dwordx4 v[120:123], %[so], %[ws], s46 offen offset:3072 sc1\n\t" \
    "s_waitcnt vmcnt(0)\n\tbuffer_store_dword %[x0], %[fo], %[ws], %[fs] offen sc1\n\ts_waitcnt vmcnt(0)\n\t"
#define GEMM_ASM1 \
    "s_nop 4\n\ts_mov_b32 s44, %[xb]\n\ts_mov_b32 s46, %[ob]\n\ts_add_u32 s47, s46, 4096\n\ts_mov_b32 s45, 15\n\tbuffer_load_dwordx4 v[8:11], %[x0], %[ws], s44 offen sc1\n\t" \
    "buffer_load_dwordx4 v[12:15], %[x1], %[ws], s44 offen sc1\n\ts_add_u32 s44, s44, 1024\n\tbuffer_load_dwordx4 v[16:19], %[x0], %[ws], s44 offen sc1\n\t" \
    "buffer_load_dwordx4 v[20:23], %[x1], %[ws], s44 offen sc1\n\ts_add_u32 s44, s44, 1024\n\tbuffer_load_dwordx4 v[24:27], %[x0], %[ws], s44 offen sc1\n\t" \
    "buffer_load_dwordx4 v[28:31], %[x1], %[ws], s44 offen sc1\n\ts_add_u32 s44, s44, 1024\n\tbuffer_load_dwordx4 v[32:35], %[x0], %[ws], s44 offen sc1\n\t" \
    "buffer_load_dwordx4 v[36:39], %[x1], %[ws], s44 offen sc1\n\ts_add_u32 s44, s44, 1024\n\tbuffer_load_dwordx4 v[40:43], %[x0], %[ws], s44 offen sc1\n\t" \
    "buffer_load_dwordx4 v[44:47], %[x1], %[ws], s44 offen sc1\n\ts_add_u32 s44, s44, 1024\n\tbuffer_load_dwordx4 v[48:51], %[x0], %[ws], s44 offen sc1\n\t" \
    "buffer_load_dwordx4 v[52:55], %[x1], %[ws], s44 offen sc1\n\ts_add_u32 s44, s44, 1024\n\tbuffer_load_dwordx4 v[56:59], %[x0], %[ws], s44 offen sc1\n\t" \
    "buffer_load_dwordx4 v[60:63], %[x1], %[ws], s44 offen sc1\n\ts_add_u32 s44, s44, 1024\n\tds_read_b128 a[0:3], %[wa] offset:0\n\tds_read_b128 a[4:7], %[wa] offset:1024\n\t" \
    "ds_read_b128 a[8:11], %[wa] offset:2048\n\tds_read_b128 a[12:15], %[wa] offset:3072\n\tds_read_b128 a[16:19], %[wb] offset:0\n\tds_read_b128 a[20:23], %[wb] offset:1024\n\t" \
    "ds_read_b128 a[24:27], %[wb] offset:2048\n\ts_waitcnt vmcnt(12)\n\tds_read_b128 a[28:31], %[wa] offset:4096\n\tds_read_b128 a[32:35], %[wa] offset:5120\n\t" \
    "ds_read_b128 a[36:39], %[wa] offset:6144\n\tds_read_b128 a[40:43], %[wa] offset:7168\n\tds_read_b128 a[44:47], %[wb] offset:4096\n\tds_read_b128 a[48:51], %[wb] offset:5120\n\t" \
    "ds_read_b128 a[52:55], %[wb] offset:6144\n\ts_waitcnt lgkmcnt(7)\n\tv_mfma_f32_16x16x32_bf16 v[64:67], v[8:11], a[0:3], 0\n\t" \
    "v_mfma_f32_16x16x32_bf16 v[68:71], v[12:15], a[0:3], 0\n\tv_mfma_f32_16x16x32_bf16 v[72:75], v[8:11], a[4:7], 0\n\tv_mfma_f32_16x16x32_bf16 v[76:79], v[12:15], a[4:7], 0\n\t" \
    "v_mfma_f32_16x16x32_bf16 v[80:83], v[8:11], a[8:11], 0\n\tv_mfma_f32_16x16x32_bf16 v[84:87], v[12:15], a[8:11], 0\n\tv_mfma_f32_16x16x32_bf16 v[88:91], v[8:11], a[12:15], 0\n\t" \
    "v_mfma_f32_16x16x32_bf16 v[92:95], v[12:15], a[12:15], 0\n\tv_mfma_f32_16x16x32_bf16 v[96:99], v[8:11], a[16:19], 0\n\t" \
    "v_mfma_f32_16x16x32_bf16 v[100:103], v[12:15], a[16:19], 0\n\tv_mfma_f32_16x16x32_bf16 v[104:107], v[8:11], a[20:23], 0\n\t" \
    "v_mfma_f32_16x16x32_bf16 v[108:111], v[12:15], a[20:23], 0\n\tv_mfma_f32_16x16x32_bf16 v[112:115], v[8:11], a[24:27], 0\n\t" \
    "v_mfma_f32_16x16x32_bf16 v[116:119], v[12:15], a[24:27], 0\n\tbuffer_load_dwordx4 v[8:11], %[x0], %[ws], s44 offen sc1\n\t" \
    "buffer_load_dwordx4 v[12:15], %[x1], %[ws], s44 offen sc1\n\ts_add_u32 s44, s44, 1024\n\ts_waitcnt vmcnt(12)\n\tds_read_b128 a[56:59], %[wa] offset:8192\n\t" \
    "ds_read_b128 a[60:63], %[wa] offset:9216\n\tds_read_b128 a[64:67], %[wa] offset:10240\n\tds_read_b128 a[68:71], %[wa] offset:11264\n\tds_read_b128 a[72:75], %[wb] offset:8192\n\t" \
    "ds_read_b128 a[76:79], %[wb] offset:9216\n\tds_read_b128 a[80:83], %[wb] offset:10240\n\ts_waitcnt lgkmcnt(7)\n\tv_mfma_f32_16x16x32_bf16 v[64:67], v[16:19], a[28:31], v[64:67]\n\t" \
    "v_mfma_f32_16x16x32_bf16 v[68:71], v[20:23], a[28:31], v[68:71]\n\tv_mfma_f32_16x16x32_bf16 v[72:75], v[16:19], a[32:35], v[72:75]\n\t" \
    "v_mfma_f32_16x16x32_bf16 v[76:79], v[20:23], a[32:35], v[76:79]\n\tv_mfma_f32_16x16x32_bf16 v[80:83], v[16:19], a[36:39], v[80:83]\n\t" \
    "v_mfma_f32_16x16x32_bf16 v[84:87], v[20:23], a[36:39], v[84:87]\n\tv_mfma_f32_16x16x32_bf16 v[88:91], v[16:19], a[40:43], v[88:91]\n\t" \
    "v_mfma_f32_16x16x32_bf16 v[92:95], v[20:23], a[40:43], v[92:95]\n\tv_mfma_f32_16x16x32_bf16 v[96:99], v[16:19], a[44:47], v[96:99]\n\t" \
    "v_mfma_f32_16x16x32_bf16 v[100:103], v[20:23], a[44:47], v[100:103]\n\tv_mfma_f32_16x16x32_bf16 v[104:107], v[16:19], a[48:51], v[104:107]\n\t" \
    "v_mfma_f32_16x16x32_bf16 v[108:111], v[20:23], a[48:51], v[108:111]\n\tv_mfma_f32_16x16x32_bf16 v[112:115], v[16:19], a[52:55], v[112:115]\n\t" \
    "v_mfma_f32_16x16x32_bf16 v[116:119], v[20:23], a[52:55], v[116:119]\n\tbuffer_load_dwordx4 v[16:19], %[x0], %[ws], s44 offen sc1\n\t" \
    "buffer_load_dwordx4 v[20:23], %[x1], %[ws], s44 offen sc1\n\ts_add_u32 s44, s44, 1024\n\ts_waitcnt vmcnt(12)\n\tds_read_b128 a[84:87], %[wa] offset:12288\n\t" \
    "ds_read_b128 a[88:91], %[wa] offset:13312\n\tds_read_b128 a[92:95], %[wa] offset:14336\n\tds_read_b128 a[96:99], %[wa] offset:15360\n\t" \
    "ds_read_b128 a[100:103], %[wb] offset:12288\n\tds_read_b128 a[104:107], %[wb] offset:13312\n\tds_read_b128 a[108:111], %[wb] offset:14336\n\ts_waitcnt lgkmcnt(7)\n\t" \
    "v_mfma_f32_16x16x32_bf16 v[64:67], v[24:27], a[56:59], v[64:67]\n\tv_mfma_f32_16x16x32_bf16 v[68:71], v[28:31], a[56:59], v[68:71]\n\t" \
    "v_mfma_f32_16x16x32_bf16 v[72:75], v[24:27], a[60:63], v[72:75]\n\tv_mfma_f32_16x16x32_bf16 v[76:79], v[28:31], a[60:63], v[76:79]\n\t" \
    "v_mfma_f32_16x16x32_bf16 v[80:83], v[24:27], a[64:67], v[80:83]\n\tv_mfma_f32_16x16x32_bf16 v[84:87], v[28:31], a[64:67], v[84:87]\n\t" \
    "v_mfma_f32_16x16x32_bf16 v[88:91], v[24:27], a[68:71], v[88:91]\n\tv_mfma_f32_16x16x32_bf16 v[92:95], v[28:31], a[68:71], v[92:95]\n\t" \
    "v_mfma_f32_16x16x32_bf16 v[96:99], v[24:27], a[72:75], v[96:99]\n\tv_mfma_f32_16x16x32_bf16 v[100:103], v[28:31], a[72:75], v[100:103]\n\t" \
    "v_mfma_f32_16x16x32_bf16 v[104:107], v[24:27], a[76:79], v[104:107]\n\tv_mfma_f32_16x16x32_bf16 v[108:111], v[28:31], a[76:79], v[108:111]\n\t" \
    "v_mfma_f32_16x16x32_bf16 v[112:115], v[24:27], a[80:83], v[112:115]\n\tv_mfma_f32_16x16x32_bf16 v[116:119], v[28:31], a[80:83], v[116:119]\n\t" \
    "buffer_load_dwordx4 v[24:27], %[x0], %[ws], s44 offen sc1\n\tbuffer_load_dwordx4 v[28:31], %[x1], %[ws], s44 offen sc1\n\ts_add_u32 s44, s44, 1024\n\ts_waitcnt vmcnt(12)\n\t" \
    "ds_read_b128 a[112:115], %[wa] offset:16384\n\tds_read_b128 a[116:119], %[wa] offset:17408\n\tds_read_b128 a[120:123], %[wa] offset:18432\n\t" \
    "ds_read_b128 a[124:127], %[wa] offset:19456\n\tds_read_b128 a[128:131], %[wb] offset:16384\n\tds_read_b128 a[132:135], %[wb] offset:17408\n\t" \
    "ds_read_b128 v[120:123], %[wb] offset:18432\n\ts_waitcnt lgkmcnt(7)\n\tv_mfma_f32_16x16x32_bf16 v[64:67], v[32:35], a[84:87], v[64:67]\n\t" \
    "v_mfma_f32_16x16x32_bf16 v[68:71], v[36:39], a[84:87], v[68:71]\n\tv_mfma_f32_16x16x32_bf16 v[72:75], v[32:35], a[88:91], v[72:75]\n\t" \
    "v_mfma_f32_16x16x32_bf16 v[76:79], v[36:39], a[88:91], v[76:79]\n\tv_mfma_f32_16x16x32_bf16 v[80:83], v[32:35], a[92:95], v[80:83]\n\t" \
    "v_mfma_f32_16x16x32_bf16 v[84:87], v[36:39], a[92:95], v[84:87]\n\tv_mfma_f32_16x16x32_bf16 v[88:91], v[32:35], a[96:99], v[88:91]\n\t" \
    "v_mfma_f32_16x16x32_bf16 v[92:95], v[36:39], a[96:99], v[92:95]\n\tv_mfma_f32_16x16x32_bf16 v[96:99], v[32:35], a[100:103], v[96:99]\n\t" \
    "v_mfma_f32_16x16x32_bf16 v[100:103], v[36:39], a[100:103], v[100:103]\n\tv_mfma_f32_16x16x32_bf16 v[104:107], v[32:35], a[104:107], v[104:107]\n\t" \
    "v_mfma_f32_16x16x32_bf16 v[108:111], v[36:39], a[104:107], v[108:111]\n\tv_mfma_f32_16x16x32_bf16 v[112:115], v[32:35], a[108:111], v[112:115]\n\t" \
    "v_mfma_f32_16x16x32_bf16 v[116:119], v[36:39], a[108:111], v[116:119]\n\tbuffer_load_dwordx4 v[32:35], %[x0], %[ws], s44 offen sc1\n\t" \
    "buffer_load_dwordx4 v[36:39], %[x1], %[ws], s44 offen sc1\n\ts_add_u32 s44, s44, 1024\n\ts_waitcnt vmcnt(12)\n\tds_read_b128 a[136:139], %[wa] offset:20480\n\t" \
    "ds_read_b128 a[140:143], %[wa] offset:21504\n\tds_read_b128 a[144:147], %[wa] offset:22528\n\tds_read_b128 a[148:151], %[wa] offset:23552\n\t" \
    "ds_read_b128 a[152:155], %[wb] offset:20480\n\tds_read_b128 a[156:159], %[wb] offset:21504\n\tds_read_b128 a[160:163], %[wb] offset:22528\n\ts_waitcnt lgkmcnt(7)\n\t" \
    "v_mfma_f32_16x16x32_bf16 v[64:67], v[40:43], a[112:115], v[64:67]\n\tv_mfma_f32_16x16x32_bf16 v[68:71], v[44:47], a[112:115], v[68:71]\n\t" \
    "v_mfma_f32_16x16x32_bf16 v[72:75], v[40:43], a[116:119], v[72:75]\n\tv_mfma_f32_16x16x32_bf16 v[76:79], v[44:47], a[116:119], v[76:79]\n\t" \
    "v_mfma_f32_16x16x32_bf16 v[80:83], v[40:43], a[120:123], v[80:83]\n\tv_mfma_f32_16x16x32_bf16 v[84:87], v[44:47], a[120:123], v[84:87]\n\t" \
    "v_mfma_f32_16x16x32_bf16 v[88:91], v[40:43], a[124:127], v[88:91]\n\tv_mfma_f32_16x16x32_bf16 v[92:95], v[44:47], a[124:127], v[92:95]\n\t" \
    "v_mfma_f32_16x16x32_bf16 v[96:99], v[40:43], a[128:131], v[96:99]\n\tv_mfma_f32_16x16x32_bf16 v[100:103], v[44:47], a[128:131], v[100:103]\n\t" \
    "v_mfma_f32_16x16x32_bf16 v[104:107], v[40:43], a[132:135], v[104:107]\n\tv_mfma_f32_16x16x32_bf16 v[108:111], v[44:47], a[132:135], v[108:111]\n\t" \
    "v_mfma_f32_16x16x32_bf16 v[112:115], v[40:43], v[120:123], v[112:115]\n\tv_mfma_f32_16x16x32_bf16 v[116:119], v[44:47], v[120:123], v[116:119]\n\t" \
    "buffer_load_dwordx4 v[40:43], %[x0], %[ws], s44 offen sc1\n\tbuffer_load_dwordx4 v[44:47], %[x1], %[ws], s44 offen sc1\n\ts_add_u32 s44, s44, 1024\n\ts_waitcnt vmcnt(12)\n\t" \
    "ds_read_b128 a[164:167], %[wa] offset:24576\n\tds_read_b128 a[168:171], %[wa] offset:25600\n\tds_read_b128 a[172:175], %[wa] offset:26624\n\t" \
    "ds_read_b128 a[176:179], %[wa] offset:27648\n\tds_read_b128 a[180:183], %[wb] offset:24576\n\tds_read_b128 a[184:187], %[wb] offset:25600\n\t" \
    "ds_read_b128 v[124:127], %[wb] offset:26624\n\ts_waitcnt lgkmcnt(7)\n\tv_mfma_f32_16x16x32_bf16 v[64:67], v[48:51], a[136:139], v[64:67]\n\t" \
    "v_mfma_f32_16x16x32_bf16 v[68:71], v[52:55], a[136:139], v[68:71]\n\tv_mfma_f32_16x16x32_bf16 v[72:75], v[48:51], a[140:143], v[72:75]\n\t" \
    "v_mfma_f32_16x16x32_bf16 v[76:79], v[52:55], a[140:143], v[76:79]\n\tv_mfma_f32_16x16x32_bf16 v[80:83], v[48:51], a[144:147], v[80:83]\n\t" \
    "v_mfma_f32_16x16x32_bf16 v[84:87], v[52:55], a[144:147], v[84:87]\n\tv_mfma_f32_16x16x32_bf16 v[88:91], v[48:51], a[148:151], v[88:91]\n\t" \
    "v_mfma_f32_16x16x32_bf16 v[92:95], v[52:55], a[148:151], v[92:95]\n\tv_mfma_f32_16x16x32_bf16 v[96:99], v[48:51], a[152:155], v[96:99]\n\t" \
    "v_mfma_f32_16x16x32_bf16 v[100:103], v[52:55], a[152:155], v[100:103]\n\tv_mfma_f32_16x16x32_bf16 v[104:107], v[48:51], a[156:159], v[104:107]\n\t" \
    "v_mfma_f32_16x16x32_bf16 v[108:111], v[52:55], a[156:159], v[108:111]\n\tv_mfma_f32_16x16x32_bf16 v[112:115], v[48:51], a[160:163], v[112:115]\n\t" \
    "v_mfma_f32_16x16x32_bf16 v[116:119], v[52:55], a[160:163], v[116:119]\n\tbuffer_load_dwordx4 v[48:51], %[x0], %[ws], s44 offen sc1\n\t" \
    "buffer_load_dwordx4 v[52:55], %[x1], %[ws], s44 offen sc1\n\ts_add_u32 s44, s44, 1024\n\ts_waitcnt vmcnt(12)\n\tds_read_b128 a[188:191], %[wa] offset:28672\n\t" \
    "ds_read_b128 a[192:195], %[wa] offset:29696\n\tds_read_b128 a[196:199], %[wa] offset:30720\n\tds_read_b128 a[200:203], %[wa] offset:31744\n\t" \
    "ds_read_b128 a[204:207], %[wb] offset:28672\n\tds_read_b128 a[208:211], %[wb] offset:29696\n\tds_read_b128 a[212:215], %[wb] offset:30720\n\ts_waitcnt lgkmcnt(7)\n\t" \
    "v_mfma_f32_16x16x32_bf16 v[64:67], v[56:59], a[164:167], v[64:67]\n\tv_mfma_f32_16x16x32_bf16 v[68:71], v[60:63], a[164:167], v[68:71]\n\t" \
    "v_mfma_f32_16x16x32_bf16 v[72:75], v[56:59], a[168:171], v[72:75]\n\tv_mfma_f32_16x16x32_bf16 v[76:79], v[60:63], a[168:171], v[76:79]\n\t" \
    "v_mfma_f32_16x16x32_bf16 v[80:83], v[56:59], a[172:175], v[80:83]\n\tv_mfma_f32_16x16x32_bf16 v[84:87], v[60:63], a[172:175], v[84:87]\n\t" \
    "v_mfma_f32_16x16x32_bf16 v[88:91], v[56:59], a[176:179], v[88:91]\n\tv_mfma_f32_16x16x32_bf16 v[92:95], v[60:63], a[176:179], v[92:95]\n\t" \
    "v_mfma_f32_16x16x32_bf16 v[96:99], v[56:59], a[180:183], v[96:99]\n\tv_mfma_f32_16x16x32_bf16 v[100:103], v[60:63], a[180:183], v[100:103]\n\t" \
    "v_mfma_f32_16x16x32_bf16 v[104:107], v[56:59], a[184:187], v[104:107]\n\tv_mfma_f32_16x16x32_bf16 v[108:111], v[60:63], a[184:187], v[108:111]\n\t" \
    "v_mfma_f32_16x16x32_bf16 v[112:115], v[56:59], v[124:127], v[112:115]\n\tv_mfma_f32_16x16x32_bf16 v[116:119], v[60:63], v[124:127], v[116:119]\n\t" \
    "buffer_load_dwordx4 v[56:59], %[x0], %[ws], s44 offen sc1\n\tbuffer_load_dwordx4 v[60:63], %[x1], %[ws], s44 offen sc1\n\ts_add_u32 s44, s44, 101376\n\ts_waitcnt vmcnt(12)\n\t" \
    "ds_read_b128 a[216:219], %[wa] offset:32768\n\tds_read_b128 a[220:223], %[wa] offset:33792\n\tds_read_b128 a[224:227], %[wa] offset:34816\n\t" \
    "ds_read_b128 a[228:231], %[wa] offset:35840\n\tds_read_b128 a[232:235], %[wb] offset:32768\n\tds_read_b128 a[236:239], %[wb] offset:33792\n\t" \
    "ds_read_b128 v[128:131], %[wb] offset:34816\n\ts_waitcnt lgkmcnt(7)\n\tv_mfma_f32_16x16x32_bf16 v[64:67], v[8:11], a[188:191], v[64:67]\n\t" \
    "v_mfma_f32_16x16x32_bf16 v[68:71], v[12:15], a[188:191], v[68:71]\n\tv_mfma_f32_16x16x32_bf16 v[72:75], v[8:11], a[192:195], v[72:75]\n\t" \
    "v_mfma_f32_16x16x32_bf16 v[76:79], v[12:15], a[192:195], v[76:79]\n\tv_mfma_f32_16x16x32_bf16 v[80:83], v[8:11], a[196:199], v[80:83]\n\t" \
    "v_mfma_f32_16x16x32_bf16 v[84:87], v[12:15], a[196:199], v[84:87]\n\tv_mfma_f32_16x16x32_bf16 v[88:91], v[8:11], a[200:203], v[88:91]\n\t" \
    "v_mfma_f32_16x16x32_bf16 v[92:95], v[12:15], a[200:203], v[92:95]\n\tv_mfma_f32_16x16x32_bf16 v[96:99], v[8:11], a[204:207], v[96:99]\n\t" \
    "v_mfma_f32_16x16x32_bf16 v[100:103], v[12:15], a[204:207], v[100:103]\n\tv_mfma_f32_16x16x32_bf16 v[104:107], v[8:11], a[208:211], v[104:107]\n\t" \
    "v_mfma_f32_16x16x32_bf16 v[108:111], v[12:15], a[208:211], v[108:111]\n\tv_mfma_f32_16x16x32_bf16 v[112:115], v[8:11], a[212:215], v[112:115]\n\t" \
    "v_mfma_f32_16x16x32_bf16 v[116:119], v[12:15], a[212:215], v[116:119]\n\tbuffer_load_dwordx4 v[8:11], %[x0], %[ws], s44 offen sc1\n\t" \
    "buffer_load_dwordx4 v[12:15], %[x1], %[ws], s44 offen sc1\n\ts_add_u32 s44, s44, 1024\n\ts_waitcnt vmcnt(12)\n\tds_read_b128 a[240:243], %[wa] offset:36864\n\t" \
    "ds_read_b128 a[244:247], %[wa] offset:37888\n\tds_read_b128 a[248:251], %[wa] offset:38912\n\tds_read_b128 a[252:255], %[wa] offset:39936\n\t" \
    "ds_read_b128 v[132:135], %[wb] offset:36864\n\tds_read_b128 v[136:139], %[wb] offset:37888\n\tds_read_b128 v[140:143], %[wb] offset:38912\n\ts_waitcnt lgkmcnt(7)\n\t" \
    "v_mfma_f32_16x16x32_bf16 v[64:67], v[16:19], a[216:219], v[64:67]\n\tv_mfma_f32_16x16x32_bf16 v[68:71], v[20:23], a[216:219], v[68:71]\n\t" \
    "v_mfma_f32_16x16x32_bf16 v[72:75], v[16:19], a[220:223], v[72:75]\n\tv_mfma_f32_16x16x32_bf16 v[76:79], v[20:23], a[220:223], v[76:79]\n\t" \
    "v_mfma_f32_16x16x32_bf16 v[80:83], v[16:19], a[224:227], v[80:83]\n\tv_mfma_f32_16x16x32_bf16 v[84:87], v[20:23], a[224:227], v[84:87]\n\t" \
    "v_mfma_f32_16x16x32_bf16 v[88:91], v[16:19], a[228:231], v[88:91]\n\tv_mfma_f32_16x16x32_bf16 v[92:95], v[20:23], a[228:231], v[92:95]\n\t" \
    "v_mfma_f32_16x16x32_bf16 v[96:99], v[16:19], a[232:235], v[96:99]\n\tv_mfma_f32_16x16x32_bf16 v[100:103], v[20:23], a[232:235], v[100:103]\n\t" \
    "v_mfma_f32_16x16x32_bf16 v[104:107], v[16:19], a[236:239], v[104:107]\n\tv_mfma_f32_16x16x32_bf16 v[108:111], v[20:23], a[236:239], v[108:111]\n\t" \
    "v_mfma_f32_16x16x32_bf16 v[112:115], v[16:19], v[128:131], v[112:115]\n\tv_mfma_f32_16x16x32_bf16 v[116:119], v[20:23], v[128:131], v[116:119]\n\t" \
    "buffer_load_dwordx4 v[16:19], %[x0], %[ws], s44 offen sc1\n\tbuffer_load_dwordx4 v[20:23], %[x1], %[ws], s44 offen sc1\n\ts_add_u32 s44, s44, 1024\n\ts_waitcnt vmcnt(12)\n\t" \
    "ds_read_b128 v[144:147], %[wa] offset:40960\n\tds_read_b128 v[148:151], %[wa] offset:41984\n\tds_read_b128 v[152:155], %[wa] offset:43008\n\t" \
    "ds_read_b128 v[156:159], %[wa] offset:44032\n\tds_read_b128 v[160:163], %[wb] offset:40960\n\tds_read_b128 v[164:167], %[wb] offset:41984\n\t" \
    "ds_read_b128 v[168:171], %[wb] offset:43008\n\ts_waitcnt lgkmcnt(7)\n\tv_mfma_f32_16x16x32_bf16 v[64:67], v[24:27], a[240:243], v[64:67]\n\t" \
    "v_mfma_f32_16x16x32_bf16 v[68:71], v[28:31], a[240:243], v[68:71]\n\tv_mfma_f32_16x16x32_bf16 v[72:75], v[24:27], a[244:247], v[72:75]\n\t" \
    "v_mfma_f32_16x16x32_bf16 v[76:79], v[28:31], a[244:247], v[76:79]\n\tv_mfma_f32_16x16x32_bf16 v[80:83], v[24:27], a[248:251], v[80:83]\n\t" \
    "v_mfma_f32_16x16x32_bf16 v[84:87], v[28:31], a[248:251], v[84:87]\n\tv_mfma_f32_16x16x32_bf16 v[88:91], v[24:27], a[252:255], v[88:91]\n\t" \
    "v_mfma_f32_16x16x32_bf16 v[92:95], v[28:31], a[252:255], v[92:95]\n\tv_mfma_f32_16x16x32_bf16 v[96:99], v[24:27], v[132:135], v[96:99]\n\t" \
    "v_mfma_f32_16x16x32_bf16 v[100:103], v[28:31], v[132:135], v[100:103]\n\tv_mfma_f32_16x16x32_bf16 v[104:107], v[24:27], v[136:139], v[104:107]\n\t" \
    "v_mfma_f32_16x16x32_bf16 v[108:111], v[28:31], v[136:139], v[108:111]\n\tv_mfma_f32_16x16x32_bf16 v[112:115], v[24:27], v[140:143], v[112:115]\n\t" \
    "v_mfma_f32_16x16x32_bf16 v[116:119], v[28:31], v[140:143], v[116:119]\n\tbuffer_load_dwordx4 v[24:27], %[x0], %[ws], s44 offen sc1\n\t" \
    "buffer_load_dwordx4 v[28:31], %[x1], %[ws], s44 offen sc1\n\ts_add_u32 s44, s44, 1024\n\ts_waitcnt vmcnt(12)\n\tds_read_b128 v[172:175], %[wa] offset:45056\n\t" \
    "ds_read_b128 v[176:179], %[wa] offset:46080\n\tds_read_b128 v[180:183], %[wa] offset:47104\n\tds_read_b128 v[184:187], %[wa] offset:48128\n\t" \
    "ds_read_b128 v[188:191], %[wb] offset:45056\n\tds_read_b128 v[192:195], %[wb] offset:46080\n\tds_read_b128 v[196:199], %[wb] offset:47104\n\ts_waitcnt lgkmcnt(7)\n\t" \
    "v_mfma_f32_16x16x32_bf16 v[64:67], v[32:35], v[144:147], v[64:67]\n\tv_mfma_f32_16x16x32_bf16 v[68:71], v[36:39], v[144:147], v[68:71]\n\t" \
    "v_mfma_f32_16x16x32_bf16 v[72:75], v[32:35], v[148:151], v[72:75]\n\tv_mfma_f32_16x16x32_bf16 v[76:79], v[36:39], v[148:151], v[76:79]\n\t" \
    "v_mfma_f32_16x16x32_bf16 v[80:83], v[32:35], v[152:155], v[80:83]\n\tv_mfma_f32_16x16x32_bf16 v[84:87], v[36:39], v[152:155], v[84:87]\n\t" \
    "v_mfma_f32_16x16x32_bf16 v[88:91], v[32:35], v[156:159], v[88:91]\n\tv_mfma_f32_16x16x32_bf16 v[92:95], v[36:39], v[156:159], v[92:95]\n\t" \
    "v_mfma_f32_16x16x32_bf16 v[96:99], v[32:35], v[160:163], v[96:99]\n\tv_mfma_f32_16x16x32_bf16 v[100:103], v[36:39], v[160:163], v[100:103]\n\t" \
    "v_mfma_f32_16x16x32_bf16 v[104:107], v[32:35], v[164:167], v[104:107]\n\tv_mfma_f32_16x16x32_bf16 v[108:111], v[36:39], v[164:167], v[108:111]\n\t" \
    "v_mfma_f32_16x16x32_bf16 v[112:115], v[32:35], v[168:171], v[112:115]\n\tv_mfma_f32_16x16x32_bf16 v[116:119], v[36:39], v[168:171], v[116:119]\n\t" \
    "buffer_load_dwordx4 v[32:35], %[x0], %[ws], s44 offen sc1\n\tbuffer_load_dwordx4 v[36:39], %[x1], %[ws], s44 offen sc1\n\ts_add_u32 s44, s44, 1024\n\ts_waitcnt vmcnt(12)\n\t" \
    "ds_read_b128 v[200:203], %[wa] offset:49152\n\tds_read_b128 v[204:207], %[wa] offset:50176\n\tds_read_b128 v[208:211], %[wa] offset:51200\n\t" \
    "ds_read_b128 v[212:215], %[wa] offset:52224\n\tds_read_b128 v[216:219], %[wb] offset:49152\n\tds_read_b128 v[220:223], %[wb] offset:50176\n\t" \
    "ds_read_b128 v[224:227], %[wb] offset:51200\n\ts_waitcnt lgkmcnt(7)\n\tv_mfma_f32_16x16x32_bf16 v[64:67], v[40:43], v[172:175], v[64:67]\n\t" \
    "v_mfma_f32_16x16x32_bf16 v[68:71], v[44:47], v[172:175], v[68:71]\n\tv_mfma_f32_16x16x32_bf16 v[72:75], v[40:43], v[176:179], v[72:75]\n\t" \
    "v_mfma_f32_16x16x32_bf16 v[76:79], v[44:47], v[176:179], v[76:79]\n\tv_mfma_f32_16x16x32_bf16 v[80:83], v[40:43], v[180:183], v[80:83]\n\t" \
    "v_mfma_f32_16x16x32_bf16 v[84:87], v[44:47], v[180:183], v[84:87]\n\tv_mfma_f32_16x16x32_bf16 v[88:91], v[40:43], v[184:187], v[88:91]\n\t" \
    "v_mfma_f32_16x16x32_bf16 v[92:95], v[44:47], v[184:187], v[92:95]\n\tv_mfma_f32_16x16x32_bf16 v[96:99], v[40:43], v[188:191], v[96:99]\n\t" \
    "v_mfma_f32_16x16x32_bf16 v[100:103], v[44:47], v[188:191], v[100:103]\n\tv_mfma_f32_16x16x32_bf16 v[104:107], v[40:43], v[192:195], v[104:107]\n\t" \
    "v_mfma_f32_16x16x32_bf16 v[108:111], v[44:47], v[192:195], v[108:111]\n\tv_mfma_f32_16x16x32_bf16 v[112:115], v[40:43], v[196:199], v[112:115]\n\t" \
    "v_mfma_f32_16x16x32_bf16 v[116:119], v[44:47], v[196:199], v[116:119]\n\tbuffer_load_dwordx4 v[40:43], %[x0], %[ws], s44 offen sc1\n\t" \
    "buffer_load_dwordx4 v[44:47], %[x1], %[ws], s44 offen sc1\n\ts_add_u32 s44, s44, 1024\n\ts_waitcnt vmcnt(12)\n\tds_read_b128 v[228:231], %[wa] offset:53248\n\t" \
    "ds_read_b128 v[232:235], %[wa] offset:54272\n\tds_read_b128 v[236:239], %[wa] offset:55296\n\tds_read_b128 v[240:243], %[wa] offset:56320\n\t" \
    "ds_read_b128 v[244:247], %[wb] offset:53248\n\tds_read_b128 v[248:251], %[wb] offset:54272\n\tds_read_b128 v[252:255], %[wb] offset:55296\n\ts_waitcnt lgkmcnt(7)\n\t" \
    "v_mfma_f32_16x16x32_bf16 v[64:67], v[48:51], v[200:203], v[64:67]\n\tv_mfma_f32_16x16x32_bf16 v[68:71], v[52:55], v[200:203], v[68:71]\n\t" \
    "v_mfma_f32_16x16x32_bf16 v[72:75], v[48:51], v[204:207], v[72:75]\n\tv_mfma_f32_16x16x32_bf16 v[76:79], v[52:55], v[204:207], v[76:79]\n\t" \
    "v_mfma_f32_16x16x32_bf16 v[80:83], v[48:51], v[208:211], v[80:83]\n\tv_mfma_f32_16x16x32_bf16 v[84:87], v[52:55], v[208:211], v[84:87]\n\t" \
    "v_mfma_f32_16x16x32_bf16 v[88:91], v[48:51], v[212:215], v[88:91]\n\tv_mfma_f32_16x16x32_bf16 v[92:95], v[52:55], v[212:215], v[92:95]\n\t" \
    "v_mfma_f32_16x16x32_bf16 v[96:99], v[48:51], v[216:219], v[96:99]\n\tv_mfma_f32_16x16x32_bf16 v[100:103], v[52:55], v[216:219], v[100:103]\n\t" \
    "v_mfma_f32_16x16x32_bf16 v[104:107], v[48:51], v[220:223], v[104:107]\n\tv_mfma_f32_16x16x32_bf16 v[108:111], v[52:55], v[220:223], v[108:111]\n\t" \
    "v_mfma_f32_16x16x32_bf16 v[112:115], v[48:51], v[224:227], v[112:115]\n\tv_mfma_f32_16x16x32_bf16 v[116:119], v[52:55], v[224:227], v[116:119]\n\t" \
    "buffer_load_dwordx4 v[48:51], %[x0], %[ws], s44 offen sc1\n\tbuffer_load_dwordx4 v[52:55], %[x1], %[ws], s44 offen sc1\n\ts_add_u32 s44, s44, 1024\n\ts_waitcnt vmcnt(12)\n\t" \
    "s_waitcnt lgkmcnt(0)\n\tv_mfma_f32_16x16x32_bf16 v[64:67], v[56:59], v[228:231], v[64:67]\n\tv_mfma_f32_16x16x32_bf16 v[68:71], v[60:63], v[228:231], v[68:71]\n\t" \
    "v_mfma_f32_16x16x32_bf16 v[72:75], v[56:59], v[232:235], v[72:75]\n\tv_mfma_f32_16x16x32_bf16 v[76:79], v[60:63], v[232:235], v[76:79]\n\t" \
    "v_mfma_f32_16x16x32_bf16 v[80:83], v[56:59], v[236:239], v[80:83]\n\tv_cvt_pk_bf16_f32 v120, v64, v65\n\tv_cvt_pk_bf16_f32 v122, v68, v69\n\tv_cvt_pk_bf16_f32 v121, v66, v67\n\t" \
    "v_cvt_pk_bf16_f32 v123, v70, v71\n\tv_mfma_f32_16x16x32_bf16 v[84:87], v[60:63], v[236:239], v[84:87]\n\tbuffer_store_dwordx4 v[120:123], %[so], %[ws], s46 offen offset:0 sc1\n\t" \
    "v_mfma_f32_16x16x32_bf16 v[88:91], v[56:59], v[240:243], v[88:91]\n\tv_cvt_pk_bf16_f32 v124, v72, v73\n\tv_cvt_pk_bf16_f32 v126, v76, v77\n\tv_cvt_pk_bf16_f32 v125, v74, v75\n\t" \
    "v_cvt_pk_bf16_f32 v127, v78, v79\n\tv_mfma_f32_16x16x32_bf16 v[92:95], v[60:63], v[240:243], v[92:95]\n\t" \
    "buffer_store_dwordx4 v[124:127], %[so], %[ws], s46 offen offset:1024 sc1\n\tv_mfma_f32_16x16x32_bf16 v[96:99], v[56:59], v[244:247], v[96:99]\n\t" \
    "v_cvt_pk_bf16_f32 v128, v80, v81\n\tv_cvt_pk_bf16_f32 v130, v84, v85\n\tv_cvt_pk_bf16_f32 v129, v82, v83\n\tv_cvt_pk_bf16_f32 v131, v86, v87\n\t" \
    "v_mfma_f32_16x16x32_bf16 v[100:103], v[60:63], v[244:247], v[100:103]\n\tbuffer_store_dwordx4 v[128:131], %[so], %[ws], s46 offen offset:2048 sc1\n\t" \
    "v_mfma_f32_16x16x32_bf16 v[104:107], v[56:59], v[248:251], v[104:107]\n\tv_cvt_pk_bf16_f32 v120, v88, v89\n\tv_cvt_pk_bf16_f32 v122, v92, v93\n\t" \
    "v_cvt_pk_bf16_f32 v121, v90, v91\n\tv_cvt_pk_bf16_f32 v123, v94, v95\n\tv_mfma_f32_16x16x32_bf16 v[108:111], v[60:63], v[248:251], v[108:111]\n\t" \
    "buffer_store_dwordx4 v[120:123], %[so], %[ws], s46 offen offset:3072 sc1\n\tv_mfma_f32_16x16x32_bf16 v[112:115], v[56:59], v[252:255], v[112:115]\n\t" \
    "v_cvt_pk_bf16_f32 v124, v96, v97\n\tv_cvt_pk_bf16_f32 v126, v100, v101\n\tv_cvt_pk_bf16_f32 v125, v98, v99\n\tv_cvt_pk_bf16_f32 v127, v102, v103\n\t" \
    "v_mfma_f32_16x16x32_bf16 v[116:119], v[60:63], v[252:255], v[116:119]\n\tbuffer_store_dwordx4 v[124:127], %[so], %[ws], s47 offen offset:0 sc1\n\t" \
    "buffer_load_dwordx4 v[56:59], %[x0], %[ws], s44 offen sc1\n\tbuffer_load_dwordx4 v[60:63], %[x1], %[ws], s44 offen sc1\n\ts_add_u32 s44, s44, 1024\n\tgl%=:\n\t" \
    "s_waitcnt vmcnt(17)\n\tv_mfma_f32_16x16x32_bf16 v[64:67], v[8:11], a[0:3], 0\n\tv_cvt_pk_bf16_f32 v128, v104, v105\n\tv_cvt_pk_bf16_f32 v130, v108, v109\n\t" \
    "v_cvt_pk_bf16_f32 v129, v106, v107\n\tv_cvt_pk_bf16_f32 v131, v110, v111\n\tv_mfma_f32_16x16x32_bf16 v[68:71], v[12:15], a[0:3], 0\n\t" \
    "buffer_store_dwordx4 v[128:131], %[so], %[ws], s47 offen offset:1024 sc1\n\tv_mfma_f32_16x16x32_bf16 v[72:75], v[8:11], a[4:7], 0\n\tv_cvt_pk_bf16_f32 v120, v112, v113\n\t" \
    "v_cvt_pk_bf16_f32 v122, v116, v117\n\tv_cvt_pk_bf16_f32 v121, v114, v115\n\tv_cvt_pk_bf16_f32 v123, v118, v119\n\tv_mfma_f32_16x16x32_bf16 v[76:79], v[12:15], a[4:7], 0\n\t" \
    "buffer_store_dwordx4 v[120:123], %[so], %[ws], s47 offen offset:2048 sc1\n\tv_mfma_f32_16x16x32_bf16 v[80:83], v[8:11], a[8:11], 0\n\t" \
    "v_mfma_f32_16x16x32_bf16 v[84:87], v[12:15], a[8:11], 0\n\tv_mfma_f32_16x16x32_bf16 v[88:91], v[8:11], a[12:15], 0\n\tv_mfma_f32_16x16x32_bf16 v[92:95], v[12:15], a[12:15], 0\n\t" \
    "v_mfma_f32_16x16x32_bf16 v[96:99], v[8:11], a[16:19], 0\n\tv_mfma_f32_16x16x32_bf16 v[100:103], v[12:15], a[16:19], 0\n\t" \
    "v_mfma_f32_16x16x32_bf16 v[104:107], v[8:11], a[20:23], 0\n\tv_mfma_f32_16x16x32_bf16 v[108:111], v[12:15], a[20:23], 0\n\t" \
    "v_mfma_f32_16x16x32_bf16 v[112:115], v[8:11], a[24:27], 0\n\tv_mfma_f32_16x16x32_bf16 v[116:119], v[12:15], a[24:27], 0\n\t" \
    "buffer_load_dwordx4 v[8:11], %[x0], %[ws], s44 offen sc1\n\tbuffer_load_dwordx4 v[12:15], %[x1], %[ws], s44 offen sc1\n\ts_add_u32 s44, s44, 1024\n\ts_waitcnt vmcnt(19)\n\t" \
    "v_mfma_f32_16x16x32_bf16 v[64:67], v[16:19], a[28:31], v[64:67]\n\tv_mfma_f32_16x16x32_bf16 v[68:71], v[20:23], a[28:31], v[68:71]\n\t" \
    "v_mfma_f32_16x16x32_bf16 v[72:75], v[16:19], a[32:35], v[72:75]\n\tv_mfma_f32_16x16x32_bf16 v[76:79], v[20:23], a[32:35], v[76:79]\n\t" \
    "v_mfma_f32_16x16x32_bf16 v[80:83], v[16:19], a[36:39], v[80:83]\n\tv_mfma_f32_16x16x32_bf16 v[84:87], v[20:23], a[36:39], v[84:87]\n\t" \
    "v_mfma_f32_16x16x32_bf16 v[88:91], v[16:19], a[40:43], v[88:91]\n\tv_mfma_f32_16x16x32_bf16 v[92:95], v[20:23], a[40:43], v[92:95]\n\t" \
    "v_mfma_f32_16x16x32_bf16 v[96:99], v[16:19], a[44:47], v[96:99]\n\tv_mfma_f32_16x16x32_bf16 v[100:103], v[20:23], a[44:47], v[100:103]\n\t" \
    "v_mfma_f32_16x16x32_bf16 v[104:107], v[16:19], a[48:51], v[104:107]\n\tv_mfma_f32_16x16x32_bf16 v[108:111], v[20:23], a[48:51], v[108:111]\n\t" \
    "v_mfma_f32_16x16x32_bf16 v[112:115], v[16:19], a[52:55], v[112:115]\n\tv_mfma_f32_16x16x32_bf16 v[116:119], v[20:23], a[52:55], v[116:119]\n\t" \
    "buffer_load_dwordx4 v[16:19], %[x0], %[ws], s44 offen sc1\n\tbuffer_load_dwordx4 v[20:23], %[x1], %[ws], s44 offen sc1\n\ts_add_u32 s44, s44, 1024\n\t" \
    "s_add_u32 s46, s46, 1835008\n\ts_add_u32 s47, s47, 1835008\n\ts_waitcnt vmcnt(19)\n\tds_read_b128 v[120:123], %[wb] offset:18432\n\tds_read_b128 v[124:127], %[wb] offset:26624\n\t" \
    "ds_read_b128 v[128:131], %[wb] offset:34816\n\tv_mfma_f32_16x16x32_bf16 v[64:67], v[24:27], a[56:59], v[64:67]\n\t" \
    "v_mfma_f32_16x16x32_bf16 v[68:71], v[28:31], a[56:59], v[68:71]\n\tv_mfma_f32_16x16x32_bf16 v[72:75], v[24:27], a[60:63], v[72:75]\n\t" \
    "v_mfma_f32_16x16x32_bf16 v[76:79], v[28:31], a[60:63], v[76:79]\n\tv_mfma_f32_16x16x32_bf16 v[80:83], v[24:27], a[64:67], v[80:83]\n\t" \
    "v_mfma_f32_16x16x32_bf16 v[84:87], v[28:31], a[64:67], v[84:87]\n\tv_mfma_f32_16x16x32_bf16 v[88:91], v[24:27], a[68:71], v[88:91]\n\t" \
    "v_mfma_f32_16x16x32_bf16 v[92:95], v[28:31], a[68:71], v[92:95]\n\tv_mfma_f32_16x16x32_bf16 v[96:99], v[24:27], a[72:75], v[96:99]\n\t" \
    "v_mfma_f32_16x16x32_bf16 v[100:103], v[28:31], a[72:75], v[100:103]\n\tv_mfma_f32_16x16x32_bf16 v[104:107], v[24:27], a[76:79], v[104:107]\n\t" \
    "v_mfma_f32_16x16x32_bf16 v[108:111], v[28:31], a[76:79], v[108:111]\n\tv_mfma_f32_16x16x32_bf16 v[112:115], v[24:27], a[80:83], v[112:115]\n\t" \
    "v_mfma_f32_16x16x32_bf16 v[116:119], v[28:31], a[80:83], v[116:119]\n\tbuffer_load_dwordx4 v[24:27], %[x0], %[ws], s44 offen sc1\n\t" \
    "buffer_load_dwordx4 v[28:31], %[x1], %[ws], s44 offen sc1\n\ts_add_u32 s44, s44, 1024\n\ts_waitcnt vmcnt(19)\n\tv_mfma_f32_16x16x32_bf16 v[64:67], v[32:35], a[84:87], v[64:67]\n\t" \
    "v_mfma_f32_16x16x32_bf16 v[68:71], v[36:39], a[84:87], v[68:71]\n\tv_mfma_f32_16x16x32_bf16 v[72:75], v[32:35], a[88:91], v[72:75]\n\t" \
    "v_mfma_f32_16x16x32_bf16 v[76:79], v[36:39], a[88:91], v[76:79]\n\tv_mfma_f32_16x16x32_bf16 v[80:83], v[32:35], a[92:95], v[80:83]\n\t" \
    "v_mfma_f32_16x16x32_bf16 v[84:87], v[36:39], a[92:95], v[84:87]\n\tv_mfma_f32_16x16x32_bf16 v[88:91], v[32:35], a[96:99], v[88:91]\n\t" \
    "v_mfma_f32_16x16x32_bf16 v[92:95], v[36:39], a[96:99], v[92:95]\n\tv_mfma_f32_16x16x32_bf16 v[96:99], v[32:35], a[100:103], v[96:99]\n\t" \
    "v_mfma_f32_16x16x32_bf16 v[100:103], v[36:39], a[100:103], v[100:103]\n\tv_mfma_f32_16x16x32_bf16 v[104:107], v[32:35], a[104:107], v[104:107]\n\t" \
    "v_mfma_f32_16x16x32_bf16 v[108:111], v[36:39], a[104:107], v[108:111]\n\tv_mfma_f32_16x16x32_bf16 v[112:115], v[32:35], a[108:111], v[112:115]\n\t" \
    "v_mfma_f32_16x16x32_bf16 v[116:119], v[36:39], a[108:111], v[116:119]\n\tbuffer_load_dwordx4 v[32:35], %[x0], %[ws], s44 offen sc1\n\t" \
    "buffer_load_dwordx4 v[36:39], %[x1], %[ws], s44 offen sc1\n\ts_add_u32 s44, s44, 1024\n\ts_waitcnt vmcnt(19)\n\ts_waitcnt vmcnt(8)\n\t" \
    "buffer_store_dword %[x0], %[fo], %[ws], %[fs] offen sc1\n\ts_waitcnt lgkmcnt(0)\n\tv_mfma_f32_16x16x32_bf16 v[64:67], v[40:43], a[112:115], v[64:67]\n\t" \
    "v_mfma_f32_16x16x32_bf16 v[68:71], v[44:47], a[112:115], v[68:71]\n\tv_mfma_f32_16x16x32_bf16 v[72:75], v[40:43], a[116:119], v[72:75]\n\t" \
    "v_mfma_f32_16x16x32_bf16 v[76:79], v[44:47], a[116:119], v[76:79]\n\tv_mfma_f32_16x16x32_bf16 v[80:83], v[40:43], a[120:123], v[80:83]\n\t" \
    "v_mfma_f32_16x16x32_bf16 v[84:87], v[44:47], a[120:123], v[84:87]\n\tv_mfma_f32_16x16x32_bf16 v[88:91], v[40:43], a[124:127], v[88:91]\n\t" \
    "v_mfma_f32_16x16x32_bf16 v[92:95], v[44:47], a[124:127], v[92:95]\n\tv_mfma_f32_16x16x32_bf16 v[96:99], v[40:43], a[128:131], v[96:99]\n\t" \
    "v_mfma_f32_16x16x32_bf16 v[100:103], v[44:47], a[128:131], v[100:103]\n\tv_mfma_f32_16x16x32_bf16 v[104:107], v[40:43], a[132:135], v[104:107]\n\t" \
    "v_mfma_f32_16x16x32_bf16 v[108:111], v[44:47], a[132:135], v[108:111]\n\tv_mfma_f32_16x16x32_bf16 v[112:115], v[40:43], v[120:123], v[112:115]\n\t" \
    "v_mfma_f32_16x16x32_bf16 v[116:119], v[44:47], v[120:123], v[116:119]\n\tbuffer_load_dwordx4 v[40:43], %[x0], %[ws], s44 offen sc1\n\t" \
    "buffer_load_dwordx4 v[44:47], %[x1], %[ws], s44 offen sc1\n\ts_add_u32 s44, s44, 1024\n\ts_waitcnt vmcnt(20)\n\t" \
    "v_mfma_f32_16x16x32_bf16 v[64:67], v[48:51], a[136:139], v[64:67]\n\tv_mfma_f32_16x16x32_bf16 v[68:71], v[52:55], a[136:139], v[68:71]\n\t" \
    "v_mfma_f32_16x16x32_bf16 v[72:75], v[48:51], a[140:143], v[72:75]\n\tv_mfma_f32_16x16x32_bf16 v[76:79], v[52:55], a[140:143], v[76:79]\n\t" \
    "v_mfma_f32_16x16x32_bf16 v[80:83], v[48:51], a[144:147], v[80:83]\n\tv_mfma_f32_16x16x32_bf16 v[84:87], v[52:55], a[144:147], v[84:87]\n\t" \
    "v_mfma_f32_16x16x32_bf16 v[88:91], v[48:51], a[148:151], v[88:91]\n\tv_mfma_f32_16x16x32_bf16 v[92:95], v[52:55], a[148:151], v[92:95]\n\t" \
    "v_mfma_f32_16x16x32_bf16 v[96:99], v[48:51], a[152:155], v[96:99]\n\tv_mfma_f32_16x16x32_bf16 v[100:103], v[52:55], a[152:155], v[100:103]\n\t" \
    "v_mfma_f32_16x16x32_bf16 v[104:107], v[48:51], a[156:159], v[104:107]\n\tv_mfma_f32_16x16x32_bf16 v[108:111], v[52:55], a[156:159], v[108:111]\n\t" \
    "v_mfma_f32_16x16x32_bf16 v[112:115], v[48:51], a[160:163], v[112:115]\n\tv_mfma_f32_16x16x32_bf16 v[116:119], v[52:55], a[160:163], v[116:119]\n\t" \
    "buffer_load_dwordx4 v[48:51], %[x0], %[ws], s44 offen sc1\n\tbuffer_load_dwordx4 v[52:55], %[x1], %[ws], s44 offen sc1\n\ts_add_u32 s44, s44, 1024\n\t" \
    "s_max_u32 %[fs], %[fs], 0x2000\n\ts_add_u32 %[fs], %[fs], 0x800\n\ts_waitcnt vmcnt(15)\n\tv_mfma_f32_16x16x32_bf16 v[64:67], v[56:59], a[164:167], v[64:67]\n\t" \
    "v_mfma_f32_16x16x32_bf16 v[68:71], v[60:63], a[164:167], v[68:71]\n\tv_mfma_f32_16x16x32_bf16 v[72:75], v[56:59], a[168:171], v[72:75]\n\t" \
    "v_mfma_f32_16x16x32_bf16 v[76:79], v[60:63], a[168:171], v[76:79]\n\tv_mfma_f32_16x16x32_bf16 v[80:83], v[56:59], a[172:175], v[80:83]\n\t" \
    "v_mfma_f32_16x16x32_bf16 v[84:87], v[60:63], a[172:175], v[84:87]\n\tv_mfma_f32_16x16x32_bf16 v[88:91], v[56:59], a[176:179], v[88:91]\n\t" \
    "v_mfma_f32_16x16x32_bf16 v[92:95], v[60:63], a[176:179], v[92:95]\n\tv_mfma_f32_16x16x32_bf16 v[96:99], v[56:59], a[180:183], v[96:99]\n\t" \
    "v_mfma_f32_16x16x32_bf16 v[100:103], v[60:63], a[180:183], v[100:103]\n\tv_mfma_f32_16x16x32_bf16 v[104:107], v[56:59], a[184:187], v[104:107]\n\t" \
    "v_mfma_f32_16x16x32_bf16 v[108:111], v[60:63], a[184:187], v[108:111]\n\tv_mfma_f32_16x16x32_bf16 v[112:115], v[56:59], v[124:127], v[112:115]\n\t" \
    "v_mfma_f32_16x16x32_bf16 v[116:119], v[60:63], v[124:127], v[116:119]\n\tbuffer_load_dwordx4 v[56:59], %[x0], %[ws], s44 offen sc1\n\t" \
    "buffer_load_dwordx4 v[60:63], %[x1], %[ws], s44 offen sc1\n\ts_add_u32 s44, s44, 101376\n\ts_waitcnt vmcnt(13)\n\t" \
    "v_mfma_f32_16x16x32_bf16 v[64:67], v[8:11], a[188:191], v[64:67]\n\tv_mfma_f32_16x16x32_bf16 v[68:71], v[12:15], a[188:191], v[68:71]\n\t" \
    "v_mfma_f32_16x16x32_bf16 v[72:75], v[8:11], a[192:195], v[72:75]\n\tv_mfma_f32_16x16x32_bf16 v[76:79], v[12:15], a[192:195], v[76:79]\n\t" \
    "v_mfma_f32_16x16x32_bf16 v[80:83], v[8:11], a[196:199], v[80:83]\n\tv_mfma_f32_16x16x32_bf16 v[84:87], v[12:15], a[196:199], v[84:87]\n\t" \
    "v_mfma_f32_16x16x32_bf16 v[88:91], v[8:11], a[200:203], v[88:91]\n\tv_mfma_f32_16x16x32_bf16 v[92:95], v[12:15], a[200:203], v[92:95]\n\t" \
    "v_mfma_f32_16x16x32_bf16 v[96:99], v[8:11], a[204:207], v[96:99]\n\tv_mfma_f32_16x16x32_bf16 v[100:103], v[12:15], a[204:207], v[100:103]\n\t" \
    "v_mfma_f32_16x16x32_bf16 v[104:107], v[8:11], a[208:211], v[104:107]\n\tv_mfma_f32_16x16x32_bf16 v[108:111], v[12:15], a[208:211], v[108:111]\n\t" \
    "v_mfma_f32_16x16x32_bf16 v[112:115], v[8:11], a[212:215], v[112:115]\n\tv_mfma_f32_16x16x32_bf16 v[116:119], v[12:15], a[212:215], v[116:119]\n\t" \
    "buffer_load_dwordx4 v[8:11], %[x0], %[ws], s44 offen sc1\n\tbuffer_load_dwordx4 v[12:15], %[x1], %[ws], s44 offen sc1\n\ts_add_u32 s44, s44, 1024\n\ts_waitcnt vmcnt(13)\n\t" \
    "v_mfma_f32_16x16x32_bf16 v[64:67], v[16:19], a[216:219], v[64:67]\n\tv_mfma_f32_16x16x32_bf16 v[68:71], v[20:23], a[216:219], v[68:71]\n\t" \
    "v_mfma_f32_16x16x32_bf16 v[72:75], v[16:19], a[220:223], v[72:75]\n\tv_mfma_f32_16x16x32_bf16 v[76:79], v[20:23], a[220:223], v[76:79]\n\t" \
    "v_mfma_f32_16x16x32_bf16 v[80:83], v[16:19], a[224:227], v[80:83]\n\tv_mfma_f32_16x16x32_bf16 v[84:87], v[20:23], a[224:227], v[84:87]\n\t" \
    "v_mfma_f32_16x16x32_bf16 v[88:91], v[16:19], a[228:231], v[88:91]\n\tv_mfma_f32_16x16x32_bf16 v[92:95], v[20:23], a[228:231], v[92:95]\n\t" \
    "v_mfma_f32_16x16x32_bf16 v[96:99], v[16:19], a[232:235], v[96:99]\n\tv_mfma_f32_16x16x32_bf16 v[100:103], v[20:23], a[232:235], v[100:103]\n\t" \
    "v_mfma_f32_16x16x32_bf16 v[104:107], v[16:19], a[236:239], v[104:107]\n\tv_mfma_f32_16x16x32_bf16 v[108:111], v[20:23], a[236:239], v[108:111]\n\t" \
    "v_mfma_f32_16x16x32_bf16 v[112:115], v[16:19], v[128:131], v[112:115]\n\tv_mfma_f32_16x16x32_bf16 v[116:119], v[20:23], v[128:131], v[116:119]\n\t" \
    "buffer_load_dwordx4 v[16:19], %[x0], %[ws], s44 offen sc1\n\tbuffer_load_dwordx4 v[20:23], %[x1], %[ws], s44 offen sc1\n\ts_add_u32 s44, s44, 1024\n\ts_waitcnt vmcnt(13)\n\t" \
    "v_mfma_f32_16x16x32_bf16 v[64:67], v[24:27], a[240:243], v[64:67]\n\tv_mfma_f32_16x16x32_bf16 v[68:71], v[28:31], a[240:243], v[68:71]\n\t" \
    "v_mfma_f32_16x16x32_bf16 v[72:75], v[24:27], a[244:247], v[72:75]\n\tv_mfma_f32_16x16x32_bf16 v[76:79], v[28:31], a[244:247], v[76:79]\n\t" \
    "v_mfma_f32_16x16x32_bf16 v[80:83], v[24:27], a[248:251], v[80:83]\n\tv_mfma_f32_16x16x32_bf16 v[84:87], v[28:31], a[248:251], v[84:87]\n\t" \
    "v_mfma_f32_16x16x32_bf16 v[88:91], v[24:27], a[252:255], v[88:91]\n\tv_mfma_f32_16x16x32_bf16 v[92:95], v[28:31], a[252:255], v[92:95]\n\t" \
    "v_mfma_f32_16x16x32_bf16 v[96:99], v[24:27], v[132:135], v[96:99]\n\tv_mfma_f32_16x16x32_bf16 v[100:103], v[28:31], v[132:135], v[100:103]\n\t" \
    "v_mfma_f32_16x16x32_bf16 v[104:107], v[24:27], v[136:139], v[104:107]\n\tv_mfma_f32_16x16x32_bf16 v[108:111], v[28:31], v[136:139], v[108:111]\n\t" \
    "v_mfma_f32_16x16x32_bf16 v[112:115], v[24:27], v[140:143], v[112:115]\n\tv_mfma_f32_16x16x32_bf16 v[116:119], v[28:31], v[140:143], v[116:119]\n\t" \
    "buffer_load_dwordx4 v[24:27], %[x0], %[ws], s44 offen sc1\n\tbuffer_load_dwordx4 v[28:31], %[x1], %[ws], s44 offen sc1\n\ts_add_u32 s44, s44, 1024\n\ts_waitcnt vmcnt(13)\n\t" \
    "v_mfma_f32_16x16x32_bf16 v[64:67], v[32:35], v[144:147], v[64:67]\n\tv_mfma_f32_16x16x32_bf16 v[68:71], v[36:39], v[144:147], v[68:71]\n\t" \
    "v_mfma_f32_16x16x32_bf16 v[72:75], v[32:35], v[148:151], v[72:75]\n\tv_mfma_f32_16x16x32_bf16 v[76:79], v[36:39], v[148:151], v[76:79]\n\t" \
    "v_mfma_f32_16x16x32_bf16 v[80:83], v[32:35], v[152:155], v[80:83]\n\tv_mfma_f32_16x16x32_bf16 v[84:87], v[36:39], v[152:155], v[84:87]\n\t" \
    "v_mfma_f32_16x16x32_bf16 v[88:91], v[32:35], v[156:159], v[88:91]\n\tv_mfma_f32_16x16x32_bf16 v[92:95], v[36:39], v[156:159], v[92:95]\n\t" \
    "v_mfma_f32_16x16x32_bf16 v[96:99], v[32:35], v[160:163], v[96:99]\n\tv_mfma_f32_16x16x32_bf16 v[100:103], v[36:39], v[160:163], v[100:103]\n\t" \
    "v_mfma_f32_16x16x32_bf16 v[104:107], v[32:35], v[164:167], v[104:107]\n\tv_mfma_f32_16x16x32_bf16 v[108:111], v[36:39], v[164:167], v[108:111]\n\t" \
    "v_mfma_f32_16x16x32_bf16 v[112:115], v[32:35], v[168:171], v[112:115]\n\tv_mfma_f32_16x16x32_bf16 v[116:119], v[36:39], v[168:171], v[116:119]\n\t" \
    "buffer_load_dwordx4 v[32:35], %[x0], %[ws], s44 offen sc1\n\tbuffer_load_dwordx4 v[36:39], %[x1], %[ws], s44 offen sc1\n\ts_add_u32 s44, s44, 1024\n\ts_waitcnt vmcnt(12)\n\t" \
    "v_mfma_f32_16x16x32_bf16 v[64:67], v[40:43], v[172:175], v[64:67]\n\tv_mfma_f32_16x16x32_bf16 v[68:71], v[44:47], v[172:175], v[68:71]\n\t" \
    "v_mfma_f32_16x16x32_bf16 v[72:75], v[40:43], v[176:179], v[72:75]\n\tv_mfma_f32_16x16x32_bf16 v[76:79], v[44:47], v[176:179], v[76:79]\n\t" \
    "v_mfma_f32_16x16x32_bf16 v[80:83], v[40:43], v[180:183], v[80:83]\n\tv_mfma_f32_16x16x32_bf16 v[84:87], v[44:47], v[180:183], v[84:87]\n\t" \
    "v_mfma_f32_16x16x32_bf16 v[88:91], v[40:43], v[184:187], v[88:91]\n\tv_mfma_f32_16x16x32_bf16 v[92:95], v[44:47], v[184:187], v[92:95]\n\t" \
    "v_mfma_f32_16x16x32_bf16 v[96:99], v[40:43], v[188:191], v[96:99]\n\tv_mfma_f32_16x16x32_bf16 v[100:103], v[44:47], v[188:191], v[100:103]\n\t" \
    "v_mfma_f32_16x16x32_bf16 v[104:107], v[40:43], v[192:195], v[104:107]\n\tv_mfma_f32_16x16x32_bf16 v[108:111], v[44:47], v[192:195], v[108:111]\n\t" \
    "v_mfma_f32_16x16x32_bf16 v[112:115], v[40:43], v[196:199], v[112:115]\n\tv_mfma_f32_16x16x32_bf16 v[116:119], v[44:47], v[196:199], v[116:119]\n\t" \
    "buffer_load_dwordx4 v[40:43], %[x0], %[ws], s44 offen sc1\n\tbuffer_load_dwordx4 v[44:47], %[x1], %[ws], s44 offen sc1\n\ts_add_u32 s44, s44, 1024\n\ts_waitcnt vmcnt(12)\n\t" \
    "v_mfma_f32_16x16x32_bf16 v[64:67], v[48:51], v[200:203], v[64:67]\n\tv_mfma_f32_16x16x32_bf16 v[68:71], v[52:55], v[200:203], v[68:71]\n\t" \
    "v_mfma_f32_16x16x32_bf16 v[72:75], v[48:51], v[204:207], v[72:75]\n\tv_mfma_f32_16x16x32_bf16 v[76:79], v[52:55], v[204:207], v[76:79]\n\t" \
    "v_mfma_f32_16x16x32_bf16 v[80:83], v[48:51], v[208:211], v[80:83]\n\tv_mfma_f32_16x16x32_bf16 v[84:87], v[52:55], v[208:211], v[84:87]\n\t" \
    "v_mfma_f32_16x16x32_bf16 v[88:91], v[48:51], v[212:215], v[88:91]\n\tv_mfma_f32_16x16x32_bf16 v[92:95], v[52:55], v[212:215], v[92:95]\n\t" \
    "v_mfma_f32_16x16x32_bf16 v[96:99], v[48:51], v[216:219], v[96:99]\n\tv_mfma_f32_16x16x32_bf16 v[100:103], v[52:55], v[216:219], v[100:103]\n\t" \
    "v_mfma_f32_16x16x32_bf16 v[104:107], v[48:51], v[220:223], v[104:107]\n\tv_mfma_f32_16x16x32_bf16 v[108:111], v[52:55], v[220:223], v[108:111]\n\t" \
    "v_mfma_f32_16x16x32_bf16 v[112:115], v[48:51], v[224:227], v[112:115]\n\tv_mfma_f32_16x16x32_bf16 v[116:119], v[52:55], v[224:227], v[116:119]\n\t" \
    "buffer_load_dwordx4 v[48:51], %[x0], %[ws], s44 offen sc1\n\tbuffer_load_dwordx4 v[52:55], %[x1], %[ws], s44 offen sc1\n\ts_add_u32 s44, s44, 1024\n\ts_waitcnt vmcnt(12)\n\t" \
    "v_mfma_f32_16x16x32_bf16 v[64:67], v[56:59], v[228:231], v[64:67]\n\tv_mfma_f32_16x16x32_bf16 v[68:71], v[60:63], v[228:231], v[68:71]\n\t" \
    "v_mfma_f32_16x16x32_bf16 v[72:75], v[56:59], v[232:235], v[72:75]\n\tv_mfma_f32_16x16x32_bf16 v[76:79], v[60:63], v[232:235], v[76:79]\n\t" \
    "v_mfma_f32_16x16x32_bf16 v[80:83], v[56:59], v[236:239], v[80:83]\n\tv_cvt_pk_bf16_f32 v120, v64, v65\n\tv_cvt_pk_bf16_f32 v122, v68, v69\n\tv_cvt_pk_bf16_f32 v121, v66, v67\n\t" \
    "v_cvt_pk_bf16_f32 v123, v70, v71\n\tv_mfma_f32_16x16x32_bf16 v[84:87], v[60:63], v[236:239], v[84:87]\n\tbuffer_store_dwordx4 v[120:123], %[so], %[ws], s46 offen offset:0 sc1\n\t" \
    "v_mfma_f32_16x16x32_bf16 v[88:91], v[56:59], v[240:243], v[88:91]\n\tv_cvt_pk_bf16_f32 v124, v72, v73\n\tv_cvt_pk_bf16_f32 v126, v76, v77\n\tv_cvt_pk_bf16_f32 v125, v74, v75\n\t" \
    "v_cvt_pk_bf16_f32 v127, v78, v79\n\tv_mfma_f32_16x16x32_bf16 v[92:95], v[60:63], v[240:243], v[92:95]\n\t" \
    "buffer_store_dwordx4 v[124:127], %[so], %[ws], s46 offen offset:1024 sc1\n\tv_mfma_f32_16x16x32_bf16 v[96:99], v[56:59], v[244:247], v[96:99]\n\t" \
    "v_cvt_pk_bf16_f32 v128, v80, v81\n\tv_cvt_pk_bf16_f32 v130, v84, v85\n\tv_cvt_pk_bf16_f32 v129, v82, v83\n\tv_cvt_pk_bf16_f32 v131, v86, v87\n\t" \
    "v_mfma_f32_16x16x32_bf16 v[100:103], v[60:63], v[244:247], v[100:103]\n\tbuffer_store_dwordx4 v[128:131], %[so], %[ws], s46 offen offset:2048 sc1\n\t" \
    "v_mfma_f32_16x16x32_bf16 v[104:107], v[56:59], v[248:251], v[104:107]\n\tv_cvt_pk_bf16_f32 v120, v88, v89\n\tv_cvt_pk_bf16_f32 v122, v92, v93\n\t" \
    "v_cvt_pk_bf16_f32 v121, v90, v91\n\tv_cvt_pk_bf16_f32 v123, v94, v95\n\tv_mfma_f32_16x16x32_bf16 v[108:111], v[60:63], v[248:251], v[108:111]\n\t" \
    "buffer_store_dwordx4 v[120:123], %[so], %[ws], s46 offen offset:3072 sc1\n\tv_mfma_f32_16x16x32_bf16 v[112:115], v[56:59], v[252:255], v[112:115]\n\t" \
    "v_cvt_pk_bf16_f32 v124, v96, v97\n\tv_cvt_pk_bf16_f32 v126, v100, v101\n\tv_cvt_pk_bf16_f32 v125, v98, v99\n\tv_cvt_pk_bf16_f32 v127, v102, v103\n\t" \
    "v_mfma_f32_16x16x32_bf16 v[116:119], v[60:63], v[252:255], v[116:119]\n\tbuffer_store_dwordx4 v[124:127], %[so], %[ws], s47 offen offset:0 sc1\n\t" \
    "buffer_load_dwordx4 v[56:59], %[x0], %[ws], s44 offen sc1\n\tbuffer_load_dwordx4 v[60:63], %[x1], %[ws], s44 offen sc1\n\ts_add_u32 s44, s44, 1024\n\ts_sub_u32 s45, s45, 1\n\t" \
    "s_cmp_lg_u32 s45, 0\n\ts_cbranch_scc1 gl%=\n\ts_nop 7\n\tv_cvt_pk_bf16_f32 v128, v104, v105\n\tv_cvt_pk_bf16_f32 v130, v108, v109\n\tv_cvt_pk_bf16_f32 v129, v106, v107\n\t" \
    "v_cvt_pk_bf16_f32 v131, v110, v111\n\ts_nop 0\n\tbuffer_store_dwordx4 v[128:131], %[so], %[ws], s47 offen offset:1024 sc1\n\tv_cvt_pk_bf16_f32 v120, v112, v113\n\t" \
    "v_cvt_pk_bf16_f32 v122, v116, v117\n\tv_cvt_pk_bf16_f32 v121, v114, v115\n\tv_cvt_pk_bf16_f32 v123, v118, v119\n\ts_nop 0\n\t" \
    "buffer_store_dwordx4 v[120:123], %[so], %[ws], s47 offen offset:2048 sc1\n\ts_waitcnt vmcnt(0)\n\tbuffer_store_dword %[x0], %[fo], %[ws], %[fs] offen sc1\n\ts_waitcnt vmcnt(0)\n\t"
#define CL_GEMM "v8","v9","v10","v11","v12","v13","v14","v15","v16","v17","v18","v19","v20","v21","v22","v23","v24","v25","v26","v27","v28","v29","v30","v31","v32","v33","v34","v35","v36","v37","v38","v39","v40","v41","v42","v43","v44","v45","v46","v47","v48","v49","v50","v51","v52","v53","v54","v55","v56","v57","v58","v59","v60","v61","v62","v63","v64","v65","v66","v67","v68","v69","v70","v71","v72","v73","v74","v75","v76","v77","v78","v79","v80","v81","v82","v83","v84","v85","v86","v87","v88","v89","v90","v91","v92","v93","v94","v95","v96","v97","v98","v99","v100","v101","v102","v103","v104","v105","v106","v107","v108","v109","v110","v111","v112","v113","v114","v115","v116","v117","v118","v119","v120","v121","v122","v123","v124","v125","v126","v127","v128","v129","v130","v131","v132","v133","v134","v135","v136","v137","v138","v139","v140","v141","v142","v143","v144","v145","v146","v147","v148","v149","v150","v151","v152","v153","v154","v155","v156","v157","v158","v159","v160","v161","v162","v163","v164","v165","v166","v167","v168","v169","v170","v171","v172","v173","v174","v175","v176","v177","v178","v179","v180","v181","v182","v183","v184","v185","v186","v187","v188","v189","v190","v191","v192","v193","v194","v195","v196","v197","v198","v199","v200","v201","v202","v203","v204","v205","v206","v207","v208","v209","v210","v211","v212","v213","v214","v215","v216","v217","v218","v219","v220","v221","v222","v223","v224","v225","v226","v227","v228","v229","v230","v231","v232","v233","v234","v235","v236","v237","v238","v239","v240","v241","v242","v243","v244","v245","v246","v247","v248","v249","v250","v251","v252","v253","v254","v255","a0","a1","a2","a3","a4","a5","a6","a7","a8","a9","a10","a11","a12","a13","a14","a15","a16","a17","a18","a19","a20","a21","a22","a23","a24","a25","a26","a27","a28","a29","a30","a31","a32","a33","a34","a35","a36","a37","a38","a39","a40","a41","a42","a43","a44","a45","a46","a47","a48","a49","a50","a51","a52","a53","a54","a55","a56","a57","a58","a59","a60","a61","a62","a63","a64","a65","a66","a67","a68","a69","a70","a71","a72","a73","a74","a75","a76","a77","a78","a79","a80","a81","a82","a83","a84","a85","a86","a87","a88","a89","a90","a91","a92","a93","a94","a95","a96","a97","a98","a99","a100","a101","a102","a103","a104","a105","a106","a107","a108","a109","a110","a111","a112","a113","a114","a115","a116","a117","a118","a119","a120","a121","a122","a123","a124","a125","a126","a127","a128","a129","a130","a131","a132","a133","a134","a135","a136","a137","a138","a139","a140","a141","a142","a143","a144","a145","a146","a147","a148","a149","a150","a151","a152","a153","a154","a155","a156","a157","a158","a159","a160","a161","a162","a163","a164","a165","a166","a167","a168","a169","a170","a171","a172","a173","a174","a175","a176","a177","a178","a179","a180","a181","a182","a183","a184","a185","a186","a187","a188","a189","a190","a191","a192","a193","a194","a195","a196","a197","a198","a199","a200","a201","a202","a203","a204","a205","a206","a207","a208","a209","a210","a211","a212","a213","a214","a215","a216","a217","a218","a219","a220","a221","a222","a223","a224","a225","a226","a227","a228","a229","a230","a231","a232","a233","a234","a235","a236","a237","a238","a239","a240","a241","a242","a243","a244","a245","a246","a247","a248","a249","a250","a251","a252","a253","a254","a255","s44","s45","s46","s47","scc","memory"
template <int TYPE>
DEV void gemm_uv(const Params& p, LDS char* lds, int h, int q, int wave) {
    const int lane = fresh_lane();
    const int x = lane & 15, g = lane >> 4;
    u32 h0;
    const u32 ok = w0<TYPE>(p, lds, h, q, 0, wave, F_X + (lane & 31) * 128, lane < 56 ? O_XP + wave * 28672 + lane * 512 : OOB, h0);
    rsrc_t ws = mkrs(p.ws, WS_END);
    STAMP(4, 0, 0);
    int budget = 400000;
    if (!ok) wait_beat(ws, lane, h0, budget);
    if (TYPE == 1 && wave == 0 && lane == 0) st1c(0u, ws, F_B + (h * 4 + q) * 128, 0);
    if (!ok) wait_flags(ws, F_X + (lane & 31) * 128, true, 0, budget, 1);
    STAMP(4, 0, 6);
    const u32 wa = (u32)(uintptr_t)lds + lane * 16;
    const u32 fo = lane == 0 ? F_W - 8192 + h * 128 + ((TYPE * 4 + wave) * 4 + q) * 4 : OOB;
    u32 fs = wave * 2048;
    const u32 so = TYPE == 0 ? (u32)((g & 1) * 14336 + ((g >> 1) * 16 + x) * 16 + q * 3584) : (u32)(lane * 16 + q * 7168);
#define GEMM_OPS : [fs] "+s"(fs) : [ws] "s"(ws), [xb] "s"(O_XP + (u32)wave * 28672u), [ob] "s"((TYPE == 0 ? O_U : O_VO) + (u32)(wave * 16 + h) * 28672u), \
        [x0] "v"(lane * 16), [x1] "v"(lane * 16 + 14336), [wa] "v"(wa), [wb] "v"(wa + L_BUF), [so] "v"(so), [fo] "v"(fo) : CL_GEMM
    if (TYPE == 0) asm volatile(GEMM_ASM0 GEMM_OPS);
    else asm volatile(GEMM_ASM1 GEMM_OPS);
}

DEV float phi(float t) { return (t > 0.f ? t + 1.f : ex2(t * 1.4426950408889634f)) + 1e-6f; }

DEV void part_a(const Params& p, LDS char* lds, int wi, int wave, int& budget, u32& fb) {
    const int lane = fresh_lane();
    const int x = lane & 15, g = lane >> 4, tid = wave * 64 + lane;
    rsrc_t ws = mkrs(p.ws, WS_END);
    {
        const int h = wi >> 3, rc = wi & 7;
        fb = ((u32)__builtin_amdgcn_readlane(wait_flags(ws, lane < 32 ? F_K + (lane & 3) * 1024 + (lane >> 2) * 4 : F_K + 128 + (wi & 3) * 128, true, 0, budget, 0), 32) & 4u) << 11;
        bar();
        u4 st[21], X[4][14];
#pragma unroll
        for (int j = 0; j < 21; ++j) st[j] = ld4c(ws, lane * 16 + (j & 3) * 1024, O_AW + (u32)(h * 84 + wave * 21 + (j & ~3)) * 1024u);
#pragma unroll
        for (int m = 0; m < 14; ++m)
#pragma unroll
            for (int t = 0; t < 4; ++t) X[t][m] = ld4c(ws, lane * 16 + (m & 3) * 1024, O_AX + (u32)((rc * 16 + wave * 4 + t) * 14 + (m & ~3)) * 1024u);
#pragma unroll
        for (int j = 0; j < 21; ++j) *(LDS u4*)(lds + (wave * 21 + j) * 1024 + lane * 16) = st[j];
        bar();
        f4 acc[4][6];
#pragma unroll
        for (int t = 0; t < 4; ++t)
#pragma unroll
            for (int c = 0; c < 6; ++c) acc[t][c] = (f4){0, 0, 0, 0};
#pragma unroll
        for (int m = 0; m < 14; ++m)
#pragma unroll
            for (int c = 0; c < 6; ++c) {
                u4 w = lds4(lds, (m * 6 + c) * 1024 + lane * 16);
#pragma unroll
                for (int t = 0; t < 4; ++t) acc[t][c] = c < 2 ? mfma(w, X[t][m], acc[t][c]) : mfma(X[t][m], w, acc[t][c]);
            }
#pragma unroll
        for (int t = 0; t < 4; ++t) {
            u4 o = {pk(phi(acc[t][0][0]), phi(acc[t][0][1])), pk(phi(acc[t][0][2]), phi(acc[t][0][3])),
                    pk(phi(acc[t][1][0]), phi(acc[t][1][1])), pk(phi(acc[t][1][2]), phi(acc[t][1][3]))};
            if (g == 3) { o.z = 0u; o.w = 0u; }
            st4c(o, ws, lane * 16, O_PQ + (u32)((rc * 16 + wave * 4 + t) * 16 + h) * 1024u);
        }
        f4 kv[2][2] = {{{0, 0, 0, 0}, {0, 0, 0, 0}}, {{0, 0, 0, 0}, {0, 0, 0, 0}}};
#pragma unroll
        for (int tp = 0; tp < 2; ++tp) {
            u4 kf[2], vf[2];
#pragma unroll
            for (int d = 0; d < 2; ++d) {
                f4 a = acc[2 * tp][2 + d], b = acc[2 * tp + 1][2 + d];
                kf[d] = (u4){pk(phi(a[0]), phi(a[1])), pk(phi(a[2]), phi(a[3])), pk(phi(b[0]), phi(b[1])), pk(phi(b[2]), phi(b[3]))};
                a = acc[2 * tp][4 + d]; b = acc[2 * tp + 1][4 + d];
                vf[d] = (u4){pk(a[0], a[1]), pk(a[2], a[3]), pk(b[0], b[1]), pk(b[2], b[3])};
            }
            if (x >= 12) { u32 f = x == 12 ? 0x3F803F80u : 0u; vf[1] = (u4){f, f, f, f}; kf[1] = (u4){0, 0, 0, 0}; }
#pragma unroll
            for (int d = 0; d < 2; ++d)
#pragma unroll
                for (int e = 0; e < 2; ++e) kv[d][e] = mfma(vf[e], kf[d], kv[d][e]);
        }
        constexpr int L_R = 90112;
#pragma unroll
        for (int d = 0; d < 2; ++d)
#pragma unroll
            for (int e = 0; e < 2; ++e) *(LDS f4*)(lds + L_R + wave * 4096 + (d * 2 + e) * 1024 + lane * 16) = kv[d][e];
        bar();
        f4 sum = (*(LDS f4*)(lds + L_R + tid * 16) + *(LDS f4*)(lds + L_R + 4096 + tid * 16)) +
                 (*(LDS f4*)(lds + L_R + 8192 + tid * 16) + *(LDS f4*)(lds + L_R + 12288 + tid * 16));
        st4c(BC(u4, sum), ws, tid * 16, O_KV + (u32)wi * 4096u);
        VMCNT(0);
        bar();
        if (tid == 0) st1c(0u, ws, F_A1 + fb + wi * 4, 0);
        STAMP(4, 0, 3);
    }
    if (wi < 112) {
        const int h = wi / 7, ct = (wi % 7) * 4 + wave;
        u32 wo[8];
#pragma unroll
        for (int j = 0; j < 8; ++j) {
            int e = 16 * (j >> 2) + 4 * g + (j & 3);
            wo[j] = e < 28 ? p.Awo[(h * 28 + e) * 448 + 16 * ct + x] : (u16)0;
        }
        u4 wf = {wo[0] | (wo[1] << 16), wo[2] | (wo[3] << 16), wo[4] | (wo[5] << 16), wo[6] | (wo[7] << 16)};
        wait_flags(ws, F_A1 + fb + (h * 8 + (lane & 7)) * 4, true, 0, budget, 0);
        f4 kv[4];
        {
            f4 a = {0, 0, 0, 0};
#pragma unroll
            for (int rc = 0; rc < 8; ++rc) a += BC(f4, ld4c(ws, tid * 16, O_KV + (u32)(h * 8 + rc) * 4096u));
            *(LDS f4*)(lds + tid * 16) = a;
            bar();
#pragma unroll
            for (int t = 0; t < 4; ++t) kv[t] = *(LDS f4*)(lds + t * 1024 + lane * 16);
        }
        f4 g0 = mfma((u4){pk(kv[0][0], kv[0][1]), pk(kv[0][2], kv[0][3]), pk(kv[1][0], kv[1][1]), pk(kv[1][2], kv[1][3])}, wf, (f4){0, 0, 0, 0});
        f4 g1 = mfma((u4){pk(kv[2][0], kv[2][1]), pk(kv[2][2], kv[2][3]), pk(kv[3][0], kv[3][1]), pk(kv[3][2], kv[3][3])}, wf, (f4){0, 0, 0, 0});
        st4c((u4){pk(g0[0], g0[1]), pk(g0[2], g0[3]), pk(g1[0], g1[1]), pk(g1[2], g1[3])}, ws, lane * 16, O_G + (u32)(ct * 16 + h) * 1024u);
        if (ct == 0) {
            float z[8];
#pragma unroll
            for (int j = 0; j < 8; ++j) z[j] = __shfl(j < 4 ? kv[1][0] : kv[3][0], 48 + 4 * g + (j & 3));
            st4c((u4){pk(z[0], z[1]), pk(z[2], z[3]), pk(z[4], z[5]), pk(z[6], z[7])}, ws, lane * 16, O_Z + (u32)h * 1024u);
        }
        VMCNT(0);
        bar();
        if (tid == 0) st1c(0u, ws, F_G + fb + wi * 4, 0);
    }
    {
        STAMP(4, 0, 4);
        wait_flags(ws, F_G + fb + lane * 4, true, 0, budget, 0);
        wait_flags(ws, F_G + fb + 256 + lane * 4, lane < 48, 0, budget, 0);
        STAMP(4, 0, 5);
        const int rt = (wi & ~3) + wave, cq = wi & 3;
        u4 G[32], Q[16];
#pragma unroll
        for (int j = 0; j < 32; ++j)
            G[j] = ld4c(ws, lane * 16 + (j & 3) * 1024, (wave == 3 && j >= 16 ? O_Z - 16384u : O_G + (u32)(112 * cq + 32 * wave) * 1024u) + (j & ~3) * 1024u);
#pragma unroll
        for (int h = 0; h < 16; ++h) Q[h] = ld4c(ws, lane * 16 + (h & 3) * 1024, O_PQ + (u32)(rt * 16 + (h & ~3)) * 1024u);
#pragma unroll
        for (int j = 0; j < 32; ++j) *(LDS u4*)(lds + (wave * 32 + j) * 1024 + lane * 16) = G[j];
        bar();
        f4 acc[7];
#pragma unroll
        for (int c = 0; c < 7; ++c) acc[c] = (f4){0, 0, 0, 0};
#pragma unroll
        for (int h = 0; h < 16; ++h) {
            f4 den = mfma(lds4(lds, (112 + h) * 1024 + lane * 16), Q[h], (f4){0, 0, 0, 0});
            float r = 1.0f / (den[0] + 1e-6f);
#pragma unroll
            for (int k = 0; k < 4; ++k) Q[h][k] = pk(asf(Q[h][k] << 16) * r, asf(Q[h][k] & 0xFFFF0000u) * r);
        }
#pragma unroll
        for (int h = 0; h < 16; ++h)
#pragma unroll
            for (int c = 0; c < 7; ++c) acc[c] = mfma(lds4(lds, (c * 16 + h) * 1024 + lane * 16), Q[h], acc[c]);
#pragma unroll
        for (int c = 0; c < 7; ++c)
            *(u2*)(p.out + (size_t)(rt * 16 + x) * 896 + (7 * cq + c) * 16 + 4 * g) = (u2){pk(acc[c][0], acc[c][1]), pk(acc[c][2], acc[c][3])};
        bar();
    }
}

DEV void p_item(const Params& p, rsrc_t ws, int s, int hg, int wave, int& budget, u32 fb) {
    const int lane = fresh_lane();
    const int x = lane & 15, g = lane >> 4, h = 4 * hg + wave;
    const u32 l16 = lane * 16;
    u4 X[2][14], U[2][14];
#pragma unroll
    for (int m = 0; m < 14; ++m)
#pragma unroll
        for (int t = 0; t < 2; ++t) {
            U[t][m] = ld4c(ws, l16 + (m & 3) * 1024, O_U + (u32)(s * 16 + h) * 28672u + t * 14336 + (m >> 2) * 4096);
            X[t][m] = ld4c(ws, l16 + (m & 3) * 1024, O_XP + (u32)s * 28672u + t * 14336 + (m >> 2) * 4096);
        }
    f4 e[3] = {{0, 0, 0, 0}, {0, 0, 0, 0}, {0, 0, 0, 0}};
#pragma unroll
    for (int m = 0; m < 14; ++m) { e[0] = mfma(U[0][m], X[0][m], e[0]); e[1] = mfma(U[0][m], X[1][m], e[1]); e[2] = mfma(U[1][m], X[1][m], e[2]); }
    const float NEG = -1.0e9f * 1.4426950408889634f;
#pragma unroll
    for (int r = 0; r < 4; ++r) { bool ok = 4 * g + r <= x; e[0][r] = ok ? e[0][r] : NEG; e[2][r] = ok ? e[2][r] : NEG; }
    {
        const bool tail = s >= NSEG - 3;
        const int need = tail ? NSEG - 4 - (s & 1) : s;
        while (true) {
            MEMBAR();
            const int v = __builtin_amdgcn_readfirstlane((int)ld1c(ws, F_Q + fb + (h * 2 + (s & 1)) * 128, 0));
            if (v >= need || --budget < 0) break;
            if (v < need - 2) __builtin_amdgcn_s_sleep(48); else __builtin_amdgcn_s_sleep(6);
        }
        while (tail) {
            MEMBAR();
            if (__builtin_amdgcn_ballot_w64((int)ld1c(ws, F_U + fb + h * 128 + (lane & 3) * 4, 0) <= s) == 0ull || --budget < 0) break;
        }
        MEMBAR();
    }
    if (s == 63) STAMP(5, 0, 40);
    u4 M[14];
#pragma unroll
    for (int m = 0; m < 14; ++m) M[m] = ld4c(ws, g * 128 + (lane & 7) * 16 + (m & 7) * 512, O_UM + (u32)(s * 16 + h) * 7168u + (m >> 3) * 4096);
    __builtin_amdgcn_sched_barrier(0);
    f4 dm[2] = {{0, 0, 0, 0}, {0, 0, 0, 0}};
#pragma unroll
    for (int m = 0; m < 14; ++m) { dm[0] = mfma(M[m], X[0][m], dm[0]); dm[1] = mfma(M[m], X[1][m], dm[1]); }
#pragma unroll
    for (int it = 0; it < 2; ++it) {
        f4 d0 = e[it], d1 = it ? e[2] : (f4){NEG, NEG, NEG, NEG};
        float mx = fmaxf(fmaxf(fmaxf(d0[0], d0[1]), fmaxf(d0[2], d0[3])), fmaxf(fmaxf(d1[0], d1[1]), fmaxf(d1[2], d1[3])));
        mx = fmaxf(mx, fmaxf(fmaxf(dm[it][0], dm[it][1]), fmaxf(dm[it][2], dm[it][3])));
        mx = xmax16(mx); mx = xmax32(mx);
        float ea = ex2((g < 2 ? dm[it][0] : dm[it][2]) - mx), eb = ex2((g < 2 ? dm[it][1] : dm[it][3]) - mx);
        float l = ea + eb;
#pragma unroll
        for (int r = 0; r < 4; ++r) { d0[r] = ex2(d0[r] - mx); d1[r] = ex2(d1[r] - mx); l += d0[r] + d1[r]; }
        l = xadd16(l); l = xadd32(l);
        float r = __builtin_amdgcn_rcpf(l);
        st4c((u4){pk(d0[0] * r, d0[1] * r), pk(d0[2] * r, d0[3] * r), pk(d1[0] * r, d1[1] * r), pk(d1[2] * r, d1[3] * r)}, ws, l16,
             O_PX + (u32)((s * 16 + h) * 2 + it) * 1024u);
        st1c(pk(ea * r, eb * r), ws, (wave * 16 + x) * 16 + g * 4, O_PM + (u32)((s * 4 + hg) * 2 + it) * 1024u);
    }
    if (s == 63) STAMP(5, 0, 41);
    VMCNT(0);
    if (s == 63) STAMP(5, 0, 42);
    if (lane == 0) st1c(0u, ws, fb + (s == NSEG - 1 ? F_L + h * 128 : F_P + (s * 4 + hg) * 32 + wave * 4), 0);
}

DEV void o_items(const Params& p, LDS char* lds, int wi, int wave, int& budget, u32 fb) {
    const int lane = fresh_lane();
    const int tid = wave * 64 + lane;
    rsrc_t ws = mkrs(p.ws, WS_END);
    wait_flags(ws, F_W + 15 * 2048 + tid * 4, true, 0, budget, 0);
    wait_flags(ws, F_W + 15 * 2048 + 1024 + tid * 4, true, 0, budget, 0);
    bar();
#define PACE 300
    const u32 t0 = gnow();
    const u32 dt = (u32)(NSEG - 2 - __builtin_amdgcn_readfirstlane((int)ld1c(ws, F_Q + fb + (wi & 31) * 128, 0))) * (T_A + T_B) * PACE >> 18;
    for (int n = wi; n < 760; n += 128) {
        if (n >= NSEG * 11 && n < 741) break;
        while ((int)(t0 + dt * n - gnow()) > 0 && --budget >= 0) __builtin_amdgcn_s_sleep(4);
        constexpr int SKEW = 12;
        int s, cg;
        if (n < 4 * SKEW) { s = n >> 2; cg = (n & 3) - 4; }
        else if (n < 4 * SKEW + 11 * (NSEG - SKEW)) { s = (n - 4 * SKEW) / 11; cg = n - 4 * SKEW - 11 * s - 4; if (cg < 0) s += SKEW; }
        else { const int k = n - 4 * SKEW - 11 * (NSEG - SKEW); s = k / 7; cg = k - 7 * s; s += NSEG - SKEW; }
        int part = s == NSEG - 1 ? 0 : -1;
        if (n >= 741) { s = NSEG - 1; cg = n < 753 ? ((n - 741) & 3) - 4 : n - 753; part = n < 753 ? 1 + ((n - 741) >> 2) : 1; }
        if (cg < 0) { if (part < 0 || part == wave) p_item(p, ws, s, cg + 4, wave, budget, fb); STAMP(5, s, 8 + n / 128); continue; }
        u4 V[4][4];
#pragma unroll
        for (int j = 0; j < 4; ++j)
#pragma unroll
            for (int c = 0; c < 4; ++c) V[j][c] = ld4c(ws, lane * 16 + c * 1024, O_VO + (u32)(s * 16 + 4 * wave + j) * 28672u + cg * 4096);
        const bool tail = s >= NSEG - 3;
        const u32 fp = fb + (s == NSEG - 1 ? F_L + (wave * 4 + (lane & 3)) * 128 : F_P + (s * 4 + wave) * 32 + (lane & 3) * 4);
        {
            const u32 vo = lane >= 4 ? F_Q + fb + ((wi & 15) * 2 + (s & 1)) * 128 : tail ? F_M + fb + (4 * cg + lane) * 128 + wave * 4 : fp;
            const int far = (tail ? NSEG - 4 - (s & 1) : s - 2);
            while (true) {
                MEMBAR();
                int v = (int)ld1c(ws, vo, 0);
                if (__builtin_amdgcn_ballot_w64(lane < 4 && v < 0) == 0ull || --budget < 0) break;
                if (__builtin_amdgcn_readlane(v, 4) < far) __builtin_amdgcn_s_sleep(32); else __builtin_amdgcn_s_sleep(4);
            }
            MEMBAR();
        }
        u4 PX[4][2], PM[2], MB[4];
#pragma unroll
        for (int c = 0; c < 4; ++c) MB[c] = ld4c(ws, lane * 16, O_MB + (u32)((s * 28 + 4 * cg + c) * 4 + wave) * 1024u);
        while (tail) {
            MEMBAR();
            if (__builtin_amdgcn_ballot_w64((int)ld1c(ws, fp, 0) < 0) == 0ull || --budget < 0) break;
        }
        MEMBAR();
        if (s == 63) STAMP(5, 0, 43);
#pragma unroll
        for (int t = 0; t < 2; ++t) {
            const u32 lo = part == 1 - t ? OOB : lane * 16 + t * 1024;
#pragma unroll
            for (int j = 0; j < 4; ++j) PX[j][t] = ld4c(ws, lo + (j & 1) * 2048, O_PX + (u32)(s * 16 + 4 * wave + (j & 2)) * 2048u);
            PM[t] = ld4c(ws, lo, O_PM + (u32)(s * 4 + wave) * 2048u);
        }
        __builtin_amdgcn_sched_barrier(0);
        f4 acc[2][4];
#pragma unroll
        for (int t = 0; t < 2; ++t)
            if (part != 1 - t)
#pragma unroll
            for (int c = 0; c < 4; ++c) {
                acc[t][c] = (f4){0, 0, 0, 0};
#pragma unroll
                for (int j = 0; j < 4; ++j) acc[t][c] = mfma(V[j][c], PX[j][t], acc[t][c]);
                acc[t][c] = mfma(MB[c], PM[t], acc[t][c]);
                *(LDS f4*)(lds + wave * 8192 + (t * 4 + c) * 1024 + lane * 16) = acc[t][c];
            }
        if (s == 63) STAMP(5, 0, 44);
        bar();
        if (part != 1 - (wave >> 1)) {
            int row = tid >> 3, c8 = tid & 7;
            u32 ro = ((row >> 4) * 4 + (c8 >> 1)) * 1024 + ((2 * (c8 & 1)) * 16 + (row & 15)) * 16;
            f4 a = {0, 0, 0, 0}, b = {0, 0, 0, 0};
#pragma unroll
            for (int w = 0; w < 4; ++w) { a += *(LDS f4*)(lds + w * 8192 + ro); b += *(LDS f4*)(lds + w * 8192 + ro + 256); }
            st4c((u4){pk(a[0], a[1]), pk(a[2], a[3]), pk(b[0], b[1]), pk(b[2], b[3])}, mkrs(p.out, 2048u * 1792u), (u32)(row * 1792 + c8 * 16), (u32)(s * 32 * 1792 + 896 + cg * 128));
        }
        bar();
        STAMP(5, s, 8 + n / 128);
    }
}

DEV void role_probe(const Params& p, LDS char* lds, int xc, int wave) {
    const int lane = fresh_lane();
    rsrc_t ws = mkrs(p.ws, WS_END);
    const u32 tb = gnow();
    const u4 ones = {~0u, ~0u, ~0u, ~0u};
    const u32 bo = (wave == 0 && lane < 8) ? F_H + (lane * 4 + xc) * 128 : OOB;
    u32 beat = ld1c(ws, bo, 0);
#pragma unroll
    for (int k = 0; k < 7; ++k) st4c(ones, ws, lane * 16, (u32)((wave * 7 + k) * 4 + xc) * 1024u);
    VMCNT(0);
    bar();
    while (gnow() - tb < 3000u) __builtin_amdgcn_s_sleep(1);
    beat += 1u;
    st1c(beat, ws, bo, 0);
    STAMP(3, 0, 0);
#pragma unroll
    for (int k = 0; k < 7; ++k) {
        const u32 m0 = nos(*(const u32*)(p.Bm0 + ((lane >> 2) & 7) * 448 + 16 * (7 * wave + k) + 4 * (lane & 3) + 2 * (lane >> 5)));
#pragma unroll
        for (int c = 0; c < 4; ++c) st1c(m0, ws, lane * 4, ((u32)(xc + 1) << 21) + (c << 13) + ((7 * wave + k) >> 3) * GRP + ((7 * wave + k) & 7) * 256);
    }
    VMCNT(0);
    if (wave == 0) {
        for (u32 k = 4500u; k < T_PROBE; k += 1500u) {
            while (gnow() - tb < k) __builtin_amdgcn_s_sleep(2);
            beat += 1u;
            st1c(beat, ws, bo, 0);
        }
    } else probe(p, xc, 7, wave, lane, tb);
    while (gnow() - tb < T_PROBE + 1000u) __builtin_amdgcn_s_sleep(2);
    bar();
    if (wave == 0) {
        u4 v;
        do { MEMBAR(); v = ld4c(ws, F_R + xc * 512 + (lane & 31) * 16, 0); }
        while (__builtin_amdgcn_ballot_w64(lane < 28 && v[0] == ~0u) != 0ull && gnow() - tb < T_PROBE + 5000u);
        u32 best = ~0u;
#pragma unroll
        for (int k = 0; k < 4; ++k) v[k] = v[k] < 4000u ? v[k] : 4000u;
        int df = (int)(v[1] + v[3]) - (int)(v[0] + v[2]);
        df += __shfl_xor(df, 1); df += __shfl_xor(df, 2);
        df = __builtin_amdgcn_readlane(df, 28);
#pragma unroll
        for (int k = 0; k < 4; ++k) {
            u32 a = (lane & 31) < 28 ? v[k] : 4000u;
#pragma unroll
            for (int d = 4; d < 32; d *= 2) { const u32 o = __shfl_xor(a, d); a = o < a ? o : a; }
            a += __shfl_xor(a, 1); a += __shfl_xor(a, 2);
            a = a * 4 + k;
            best = a < best ? a : best;
        }
        if (lane < 4) st1c((best & 3u) | (df > 0 ? 4u : 0u), ws, F_K + 128 + xc * 1024 + lane * 128, 0);
    }
    STAMP(3, 0, 1);
    for (int k = 0; k < 20000 && wave == 0; ++k) {
        MEMBAR();
        if (__builtin_amdgcn_ballot_w64((int)ld1c(ws, F_Q + xc * 1024 + (lane & 1) * 8192, 0) >= 0) != 0ull) break;
        beat += 1u;
        st1c(beat, ws, bo, 0);
        __builtin_amdgcn_s_sleep(32);
    }
}

__global__ void __launch_bounds__(256, 1) mega(Params p) {
    extern __shared__ __attribute__((aligned(16))) char smem[];
    LDS char* lds = (LDS char*)smem;
    const int wave = __builtin_amdgcn_readfirstlane(threadIdx.x >> 6);
    const int xc = blockIdx.x & 7, r = blockIdx.x >> 3;
    STAMP(9, 0, 127);
    {
        u64 pc; u32 n;
        asm volatile("s_getpc_b64 %0\n\ts_mov_b32 %1, mega_end-.\n\t.subsection 1\nmega_end:\n\t.subsection 0" : "=s"(pc), "=s"(n));
        const int lane = fresh_lane();
        asm volatile("s_mov_b32 m0, %2\n\ts_nop 0\n\tbuffer_load_dword %0, %1, 0 offen lds"
                     :: "v"(lane < 8 ? (u32)(lane * 128 + r * 4 + wave) * 128u : OOB), "s"(mkrs((const void*)pc, n & ~3u)), "s"((u32)(uintptr_t)lds + LDS_BYTES - 256) : "memory");
    }
    if (xc < 4) {
        if (r < 16) role_s(p, lds, xc, 4 * xc + (r >> 2), r & 3, wave);
        else if (r < 24) role_xp(p, lds, xc, 4 * xc + ((r - 16) >> 1), r & 1, wave);
        else if (r < 31) role_v(p, lds, xc, r - 24, wave);
        else role_probe(p, lds, xc, wave);
    } else {
        const int wi = (xc - 4) * 32 + r;
        const int grp = wi >> 2;
        if (grp & 1) gemm_uv<1>(p, lds, grp >> 1, wi & 3, wave);
        else gemm_uv<0>(p, lds, grp >> 1, wi & 3, wave);
        STAMP(4, 0, 1);
        int budget = 400000;
        u32 fb;
        part_a(p, lds, wi, wave, budget, fb);
        STAMP(4, 0, 2);
        o_items(p, lds, wi, wave, budget, fb);
    }
    STAMP(9, 0, 126);
}

#define WS_HI 0
static char* ws_base(void* w, size_t n) {
    const uintptr_t m = (2u << 20) - 1;
    return WS_HI == 1 ? (char*)(((uintptr_t)w + n - WS_END) & ~m) : WS_HI == 2 ? (char*)(((uintptr_t)w + (n - WS_END) / 2) & ~m) : (char*)(((uintptr_t)w + m) & ~m);
}
extern "C" void kernel_launch(void* const* d_in, const int* in_sizes, int n_in, void* d_out, int out_size, void* d_ws, size_t ws_size,
                              hipStream_t stream) {
    Params p;
    p.Ax = (const u16*)d_in[0]; p.Awqkv = (const u16*)d_in[1]; p.Awo = (const u16*)d_in[2];
    p.Bx = (const u16*)d_in[3]; p.Bm0 = (const u16*)d_in[4];
    p.Wq = (const u16*)d_in[5]; p.Wk = (const u16*)d_in[6]; p.Wv = (const u16*)d_in[7]; p.Wo = (const u16*)d_in[8];
    p.out = (u16*)d_out;
    p.ws = ws_base(d_ws, ws_size);
    (void)hipFuncSetAttribute((const void*)mega, hipFuncAttributeMaxDynamicSharedMemorySize, LDS_BYTES);
    mega<<<256, 256, LDS_BYTES, stream>>>(p);
}
```
